```python
import torch

def _softmax(x, axis=-1):
    return torch.softmax(x, dim=axis)
def _gelu(x):
    return torch.nn.functional.gelu(x, approximate="tanh")
def _silu(x):
    return torch.nn.functional.silu(x)
def _sigmoid(x):
    return torch.sigmoid(x)
def _layernorm(x, axis=-1, eps=1e-5):
    m = x.mean(dim=axis, keepdim=True)
    v = x.var(dim=axis, keepdim=True, unbiased=False)
    return (x - m) / torch.sqrt(v + eps)
def _scatter_add(tgt, idx, src, axis=0):
    return tgt.clone().index_add_(axis, idx, src)
def _segment_sum(src, seg, n):
    out = torch.zeros((n,) + src.shape[1:], dtype=src.dtype)
    return out.index_add_(0, seg, src)
def _pad(x, pw):
    flat = tuple(v for pair in reversed(pw) for v in pair)
    return torch.nn.functional.pad(x, flat)

kernel_name = 'standalone__mha_block_cached__near_ridge'

def setup_inputs():

    _w = (lambda a: (lambda t: t.to(torch.bfloat16) if t.is_floating_point() else t)(torch.as_tensor(a)))
    B, Sq, D = 8, 208, 512
    H, Sk, Dh = 32, 512, 128
    return {
        "x": _w((lambda *s: torch.randn(*s, dtype=torch.bfloat16) * 0.8)(B, Sq, D)),
        "k_cache": _w((lambda *s: torch.randn(*s, dtype=torch.bfloat16) * 0.8)(B, H, Sk, Dh)),
        "v_cache": _w((lambda *s: torch.randn(*s, dtype=torch.bfloat16) * 0.05)(B, H, Sk, Dh)),
        "wq": _w((lambda *s: torch.randn(*s, dtype=torch.bfloat16) * 0.8)(D, H * Dh) * (D ** -0.5)),
        "wo": _w((lambda *s: torch.randn(*s, dtype=torch.bfloat16) * 0.05)(H * Dh, D) * ((H * Dh) ** -0.5)),
    }

def reference(x, k_cache, v_cache, wq, wo):
    B, Sq, D = x.shape
    H, Dh = 32, 128
    q = (lambda x, *d: x.permute(*d))((x @ wq).reshape(B, Sq, H, Dh), 0, 2, 1, 3)
    scores = torch.einsum("bhqd,bhkd->bhqk", q, k_cache) * (Dh ** -0.5)
    p = _softmax(scores, axis=-1)
    o = torch.einsum("bhqk,bhkd->bhqd", p, v_cache)
    return (lambda x, *d: x.permute(*d))(o, 0, 2, 1, 3).reshape(B, Sq, H * Dh) @ wo
```

```cpp
#include <hip/hip_runtime.h>
#include <hip/hip_bf16.h>
typedef unsigned short u16;
__device__ __forceinline__ float b2f(u16 b){ return __uint_as_float(((unsigned)b)<<16); }
__device__ __forceinline__ u16 f2b(float f){ unsigned u=__float_as_uint(f); u += 0x7FFF + ((u>>16)&1); return (u16)(u>>16); }
constexpr int B=8,SQ=208,DM=512,H=32,SK=512,DH=128;
__global__ void gemm_naive(const u16* A,const u16* W,u16* C,int M,int N,int K){
  int n=blockIdx.x*256+threadIdx.x, m=blockIdx.y;
  float acc=0; for(int k=0;k<K;++k) acc+=b2f(A[(long)m*K+k])*b2f(W[(long)k*N+n]);
  C[(long)m*N+n]=f2b(acc);
}
__global__ void attn_naive(const u16* Q,const u16* Kc,const u16* Vc,u16* O){
  __shared__ float p[SK]; __shared__ float red[512]; __shared__ float qs[DH];
  int q=blockIdx.x,h=blockIdx.y,b=blockIdx.z,t=threadIdx.x;
  if(t<DH) qs[t]=b2f(Q[((long)(b*SQ+q))*(H*DH)+h*DH+t]);
  __syncthreads();
  const u16* kr=Kc+(((long)(b*H+h))*SK+t)*DH;
  float s=0; for(int d=0;d<DH;++d) s+=qs[d]*b2f(kr[d]);
  s*=0.08838834764831845f;
  red[t]=s; __syncthreads();
  for(int o=256;o>0;o>>=1){ if(t<o) red[t]=fmaxf(red[t],red[t+o]); __syncthreads(); }
  float m=red[0]; __syncthreads();
  float e=__expf(s-m); p[t]=e; red[t]=e; __syncthreads();
  for(int o=256;o>0;o>>=1){ if(t<o) red[t]+=red[t+o]; __syncthreads(); }
  float l=red[0];
  if(t<DH){ float a=0; const u16* vb=Vc+((long)(b*H+h))*SK*DH+t; for(int k=0;k<SK;++k) a+=p[k]*b2f(vb[(long)k*DH]);
    O[((long)(b*SQ+q))*(H*DH)+h*DH+t]=f2b(a/l); }
}
extern "C" void kernel_launch(void* const* d_in, const int* in_sizes, int n_in, void* d_out, int out_size, void* d_ws, size_t ws_size, hipStream_t stream){
  const u16* x=(const u16*)d_in[0]; const u16* kc=(const u16*)d_in[1]; const u16* vc=(const u16*)d_in[2]; const u16* wq=(const u16*)d_in[3]; const u16* wo=(const u16*)d_in[4];
  u16* Q=(u16*)d_ws; u16* O=Q+(long)B*SQ*H*DH;
  gemm_naive<<<dim3(H*DH/256,B*SQ),256,0,stream>>>(x,wq,Q,B*SQ,H*DH,DM);
  attn_naive<<<dim3(SQ,H,B),512,0,stream>>>(Q,kc,vc,O);
  gemm_naive<<<dim3(DM/256,B*SQ),256,0,stream>>>(O,wo,(u16*)d_out,B*SQ,DM,H*DH);
}
```

```cpp
#include <hip/hip_runtime.h>
#define CLOBBERS "v0","v1","v2","v3","v4","v5","v6","v7","v8","v9","v10","v11","v12","v13","v14","v15","v16","v17","v18","v19","v20","v21","v22","v23","v24","v25","v26","v27","v28","v29","v30","v31","v32","v33","v34","v35","v36","v37","v38","v39","v40","v41","v42","v43","v44","v45","v46","v47","v48","v49","v50","v51","v52","v53","v54","v55","v56","v57","v58","v59","v60","v61","v62","v63","v64","v65","v66","v67","v68","v69","v70","v71","v72","v73","v74","v75","v76","v77","v78","v79","v80","v81","v82","v83","v84","v85","v86","v87","v88","v89","v90","v91","v92","v93","v94","v95","v96","v97","v98","v99","v100","v101","v102","v103","v104","v105","v106","v107","v108","v109","v110","v111","v112","v113","v114","v115","v116","v117","v118","v119","v120","v121","v122","v123","v124","v125","v126","v127","v128","v129","v130","v131","v132","v133","v134","v135","v136","v137","v138","v139","v140","v141","v142","v143","v144","v145","v146","v147","v148","v149","v150","v151","v152","v153","v154","v155","v156","v157","v158","v159","v160","v161","v162","v163","v164","v165","v166","v167","v168","v169","v170","v171","v172","v173","v174","v175","v176","v177","v178","v179","v180","v181","v182","v183","v184","v185","v186","v187","v188","v189","v190","v191","v192","v193","v194","v195","v196","v197","v198","v199","v200","v201","v202","v203","v204","v205","v206","v207","v208","v209","v210","v211","v212","v213","v214","v215","v216","v217","v218","v219","v220","v221","v222","v223","v224","v225","v226","v227","v228","v229","v230","v231","v232","v233","v234","v235","v236","v237","v238","v239","v240","v241","v242","v243","v244","v245","v246","v247","v248","v249","v250","v251","v252","v253","v254","v255","a0","a1","a2","a3","a4","a5","a6","a7","a8","a9","a10","a11","a12","a13","a14","a15","a16","a17","a18","a19","a20","a21","a22","a23","a24","a25","a26","a27","a28","a29","a30","a31","a32","a33","a34","a35","a36","a37","a38","a39","a40","a41","a42","a43","a44","a45","a46","a47","a48","a49","a50","a51","a52","a53","a54","a55","a56","a57","a58","a59","a60","a61","a62","a63","a64","a65","a66","a67","a68","a69","a70","a71","a72","a73","a74","a75","a76","a77","a78","a79","a80","a81","a82","a83","a84","a85","a86","a87","a88","a89","a90","a91","a92","a93","a94","a95","a96","a97","a98","a99","a100","a101","a102","a103","a104","a105","a106","a107","a108","a109","a110","a111","a112","a113","a114","a115","a116","a117","a118","a119","a120","a121","a122","a123","a124","a125","a126","a127","a128","a129","a130","a131","a132","a133","a134","a135","a136","a137","a138","a139","a140","a141","a142","a143","a144","a145","a146","a147","a148","a149","a150","a151","a152","a153","a154","a155","a156","a157","a158","a159","a160","a161","a162","a163","a164","a165","a166","a167","a168","a169","a170","a171","a172","a173","a174","a175","a176","a177","a178","a179","a180","a181","a182","a183","a184","a185","a186","a187","a188","a189","a190","a191","a192","a193","a194","a195","a196","a197","a198","a199","a200","a201","a202","a203","a204","a205","a206","a207","a208","a209","a210","a211","a212","a213","a214","a215","a216","a217","a218","a219","a220","a221","a222","a223","a224","a225","a226","a227","a228","a229","a230","a231","a232","a233","a234","a235","a236","a237","a238","a239","a240","a241","a242","a243","a244","a245","a246","a247","a248","a249","a250","a251","a252","a253","a254","a255","s20","s21","s22","s23","s24","s25","s26","s27","s28","s29","s30","s31","s33","s34","s35","s36","s37","s38","s39","s40","s41","s42","s43","s44","s45","s46","s47","s48","s49","s50","s51","s52","s53","s54","s55","s56","s57","s58","s59","s60","s61","s62","s63","s64","s65","s66","s67","s68","s69","s70","s71","s72","s73","s74","s75","s76","s77","s78","s79","s80","s81","s82","s83","s84","s85","s86","s87","s88","s89","s90","s91","s92","s93","s94","s95","s96","s97","s98","s99","vcc","scc","memory"

extern "C" __global__ __launch_bounds__(256, 1) __attribute__((aligned(4096))) void mha_fused(const void* x, const void* kc, const void* vc, const void* wq, const void* wo, void* ws, void* out, const void* pad) {
  __shared__ __attribute__((aligned(16))) char lds[163840];
  const unsigned wave = __builtin_amdgcn_readfirstlane(threadIdx.x >> 6);
  asm volatile(
  "L_start:\n"
  "s_load_dwordx16 s[20:35], s[16:17], 0\n"
  "v_mbcnt_lo_u32_b32 v254, -1, 0\n"
  "v_mbcnt_hi_u32_b32 v254, -1, v254\n"
  "s_getpc_b64 s[80:81]\n"
  "L_pc:\n"
  "s_sub_u32 s80, s80, L_pc-L_start\n"
  "s_subb_u32 s81, s81, 0\n"
  "s_and_b32 s81, s81, 0xffff\n"
  "s_mov_b32 s82, L_end-L_start\n"
  "s_mov_b32 s83, 0x20000\n"
  "s_lshr_b32 s58, s18, 3\n"
  "s_lshl_b32 s58, s58, 2\n"
  "s_add_u32 s58, s58, s19\n"
  "s_mul_i32 s58, s58, 0x200\n"
  "v_lshlrev_b32 v0, 7, v254\n"
  "v_cmp_le_u32 vcc, 4, v254\n"
  "v_mov_b32 v1, 0x40000000\n"
  "s_nop 0\n"
  "v_cndmask_b32 v0, v0, v1, vcc\n"
  "buffer_load_dword v253, v0, s[80:83], s58 offen\n"
  "s_and_b32 s58, s18, 1\n"
  "s_lshl_b32 s58, s58, 2\n"
  "s_lshr_b32 s59, s18, 3\n"
  "s_and_b32 s59, s59, 3\n"
  "s_or_b32 s56, s58, s59\n"
  "s_and_b32 s58, s18, 6\n"
  "s_lshl_b32 s58, s58, 2\n"
  "s_lshr_b32 s59, s18, 5\n"
  "s_or_b32 s57, s58, s59\n"
  "s_mul_i32 s64, s19, 0x1800\n"
  "s_lshl_b32 s65, s19, 12\n"
  "s_lshl_b32 s70, s56, 1\n"
  "s_add_u32 s70, s70, s57\n"
  "s_add_u32 s70, s70, 1\n"
  "s_and_b32 s70, s70, 7\n"
  "s_lshl_b32 s70, s70, 2\n"
  "s_mov_b32 s92, 0x76543210\n"
  "s_mov_b32 s93, 0x76543210\n"
  "s_lshr_b64 s[92:93], s[92:93], s70\n"
  "s_mov_b32 s70, s92\n"
  "s_mul_i32 s96, s19, 0xa000\n"
  "s_sub_u32 s96, 0x30000, s96\n"
  "s_lshl_b32 s97, s19, 10\n"
  "s_add_u32 s97, s97, 0x6000\n"
  "v_and_b32 v244, 3, v254\n"
  "v_lshrrev_b32 v245, 5, v254\n"
  "v_lshlrev_b32 v245, 6, v245\n"
  "v_lshl_or_b32 v244, v244, 4, v245\n"
  "v_bfe_u32 v246, v254, 4, 1\n"
  "v_mul_u32_u24 v246, 48, v246\n"
  "v_xor_b32 v246, v246, v244\n"
  "v_bfe_u32 v245, v254, 2, 3\n"
  "s_and_b32 s58, s19, 1\n"
  "s_lshl_b32 s58, s58, 18\n"
  "s_lshr_b32 s63, s19, 1\n"
  "s_lshl_b32 s63, s63, 7\n"
  "s_or_b32 s58, s58, s63\n"
  "s_mul_i32 s59, s19, 0xc000\n"
  "v_lshl_or_b32 v136, v245, 13, v244\n"
  "v_add_u32 v136, s58, v136\n"
  "v_xor_b32 v139, 32, v136\n"
  "v_add_u32 v137, 0xfc00, v139\n"
  "v_add_u32 v138, 0x1f800, v136\n"
  "v_add_u32 v139, 0x2f400, v139\n"
  "v_lshl_or_b32 v132, v245, 10, v246\n"
  "v_add_u32 v132, s59, v132\n"
  "v_add_u32 v133, 0x1c00, v132\n"
  "v_add_u32 v134, 0x3800, v132\n"
  "v_add_u32 v135, 0x5400, v132\n"
  "s_mul_i32 s60, s56, 0x34000\n"
  "s_lshl_b32 s61, s57, 8\n"
  "s_sub_u32 s62, 0x400000, s61\n"
  "s_waitcnt lgkmcnt(0)\n"
  "s_add_u32 s40, s26, s61\n"
  "s_addc_u32 s41, s27, 0\n"
  "s_and_b32 s41, s41, 0xffff\n"
  "s_mov_b32 s42, s62\n"
  "s_mov_b32 s43, 0x20000\n"
  "s_add_u32 s36, s20, s60\n"
  "s_addc_u32 s37, s21, 0\n"
  "s_and_b32 s37, s37, 0xffff\n"
  "s_mov_b32 s38, 0x34000\n"
  "s_mov_b32 s39, 0x20000\n"
  "s_cmp_eq_u32 s19, 3\n"
  "s_cbranch_scc0 L_o1\n"
  "s_add_u32 s84, s30, 0x2000000\n"
  "s_addc_u32 s85, s31, 0\n"
  "s_and_b32 s85, s85, 0xffff\n"
  "s_mov_b32 s86, 0x9000\n"
  "s_mov_b32 s87, 0x20000\n"
  "s_lshl_b32 s98, s56, 7\n"
  "s_lshl_b32 s99, s56, 12\n"
  "s_add_u32 s99, s99, 0x1000\n"
  "v_lshlrev_b32 v185, 2, v254\n"
  "v_cmp_lt_u32 vcc, 31, v254\n"
  "v_mov_b32 v189, 0x40000000\n"
  "s_nop 0\n"
  "v_cndmask_b32 v185, v185, v189, vcc\n"
  "s_lshl_b32 s90, s57, 7\n"
  "s_add_u32 s90, s90, s99\n"
  "buffer_load_dword v184, v185, s[84:87], s90 offen sc1\n"
  "buffer_load_dword v252, v185, s[84:87], s98 offen sc1\n"
  "L_o1:\n"
  "s_nop 4\n"
  "s_bfe_u32 s71, s70, 0x30000\n"
  "s_add_u32 m0, s65, 0x6800\n"
  "s_lshl_b32 s66, s71, 19\n"
  "buffer_load_dwordx4 v136, s[40:43], s66 offen lds\n"
  "buffer_load_dwordx4 v137, s[40:43], s66 offen offset:1024 lds\n"
  "buffer_load_dwordx4 v138, s[40:43], s66 offen offset:2048 lds\n"
  "buffer_load_dwordx4 v139, s[40:43], s66 offen offset:3072 lds\n"
  "s_cmp_lt_u32 s19, 2\n"
  "s_cbranch_scc0 L_o2\n"
  "s_add_u32 m0, s97, 0\n"
  "s_lshl_b32 s67, s71, 7\n"
  "s_add_u32 s67, s67, s96\n"
  "buffer_load_dwordx4 v132, s[36:39], s67 offen lds\n"
  "L_o2:\n"
  "s_add_u32 m0, s64, 0\n"
  "s_lshl_b32 s66, s71, 7\n"
  "buffer_load_dwordx4 v132, s[36:39], s66 offen lds\n"
  "buffer_load_dwordx4 v133, s[36:39], s66 offen offset:1024 lds\n"
  "buffer_load_dwordx4 v134, s[36:39], s66 offen offset:2048 lds\n"
  "buffer_load_dwordx4 v135, s[36:39], s66 offen offset:3072 lds\n"
  "s_add_u32 m0, s64, 0x1000\n"
  "s_lshl_b32 s67, s71, 7\n"
  "s_or_b32 s67, s67, 0x8000\n"
  "buffer_load_dwordx4 v132, s[36:39], s67 offen lds\n"
  "buffer_load_dwordx4 v133, s[36:39], s67 offen offset:1024 lds\n"
  "s_and_b32 s59, s19, 1\n"
  "s_lshl_b32 s58, s59, 13\n"
  "s_or_b32 s58, s58, s63\n"
  "s_lshl_b32 s59, s59, 15\n"
  "s_or_b32 s59, s59, s63\n"
  "s_lshr_b32 s60, s57, 2\n"
  "s_lshl_b32 s60, s60, 5\n"
  "s_xor_b32 s59, s59, s60\n"
  "v_lshl_or_b32 v234, v245, 8, v246\n"
  "v_add_u32 v234, s58, v234\n"
  "v_add_u32 v235, 0x400, v234\n"
  "v_add_u32 v236, 0x800, v234\n"
  "v_add_u32 v237, 0xc00, v234\n"
  "v_bfe_u32 v247, v254, 2, 2\n"
  "v_and_b32 v248, 16, v254\n"
  "v_or_b32 v247, v247, v248\n"
  "v_lshl_or_b32 v238, v247, 8, v244\n"
  "v_add_u32 v238, s58, v238\n"
  "v_xor_b32 v239, 32, v238\n"
  "v_xor_b32 v248, s59, v244\n"
  "v_lshl_or_b32 v240, v247, 10, v248\n"
  "v_xor_b32 v243, 32, v240\n"
  "v_add_u32 v241, 0xc00, v243\n"
  "v_add_u32 v242, 0x1800, v240\n"
  "v_add_u32 v243, 0x2400, v243\n"
  "s_lshl_b32 s58, s56, 5\n"
  "s_or_b32 s58, s58, s57\n"
  "s_lshl_b32 s59, s58, 17\n"
  "s_add_u32 s44, s22, s59\n"
  "s_addc_u32 s45, s23, 0\n"
  "s_and_b32 s45, s45, 0xffff\n"
  "s_mov_b32 s46, 0x20000\n"
  "s_mov_b32 s47, 0x20000\n"
  "s_add_u32 s48, s24, s59\n"
  "s_addc_u32 s49, s25, 0\n"
  "s_and_b32 s49, s49, 0xffff\n"
  "s_mov_b32 s50, 0x20000\n"
  "s_mov_b32 s51, 0x20000\n"
  "s_mul_i32 s59, s58, 0xd000\n"
  "s_add_u32 s52, s30, s59\n"
  "s_addc_u32 s53, s31, 0\n"
  "s_and_b32 s53, s53, 0xffff\n"
  "s_mov_b32 s54, 0xd000\n"
  "s_mov_b32 s55, 0x20000\n"
  "s_and_b32 s59, s58, 0xfc\n"
  "s_mul_i32 s59, s59, 0xd000\n"
  "s_add_u32 s76, s30, s59\n"
  "s_addc_u32 s77, s31, 0\n"
  "s_and_b32 s77, s77, 0xffff\n"
  "s_mov_b32 s78, 0x34000\n"
  "s_mov_b32 s79, 0x20000\n"
  "s_mov_b32 s68, 0x1000\n"
  "s_mov_b32 s69, 0x2000\n"
  "s_mov_b32 s34, 0x3e0293ee\n"
  "s_and_b32 s88, s57, 3\n"
  "s_lshl_b32 s61, s88, 8\n"
  "s_lshr_b32 s59, s57, 2\n"
  "s_lshl_b32 s59, s59, 19\n"
  "s_add_u32 s59, s59, s61\n"
  "s_sub_u32 s62, 0x80000, s61\n"
  "s_add_u32 s72, s28, s59\n"
  "s_addc_u32 s73, s29, 0\n"
  "s_and_b32 s73, s73, 0xffff\n"
  "s_mov_b32 s74, s62\n"
  "s_mov_b32 s75, 0x20000\n"
  "s_add_u32 s61, s61, s60\n"
  "s_mul_i32 s59, s56, 0x34000\n"
  "s_add_u32 s59, s59, s61\n"
  "s_sub_u32 s62, 0x34000, s61\n"
  "s_add_u32 s80, s32, s59\n"
  "s_addc_u32 s81, s33, 0\n"
  "s_and_b32 s81, s81, 0xffff\n"
  "s_mov_b32 s82, s62\n"
  "s_mov_b32 s83, 0x20000\n"
  "v_lshrrev_b32 v244, 4, v254\n"
  "v_bfe_u32 v245, v254, 3, 1\n"
  "v_and_b32 v247, 7, v254\n"
  "v_lshlrev_b32 v247, 6, v247\n"
  "v_lshl_or_b32 v245, v245, 10, v247\n"
  "v_bfe_u32 v246, v254, 2, 1\n"
  "v_mul_u32_u24 v246, 3, v246\n"
  "v_xor_b32 v247, v244, v246\n"
  "v_lshl_or_b32 v130, v247, 4, v245\n"
  "v_add_u32 v128, s64, v130\n"
  "v_add_u32 v131, 0x10000, v130\n"
  "v_add_u32 v129, 0x10000, v128\n"
  "v_bfe_u32 v247, v254, 4, 1\n"
  "v_lshrrev_b32 v248, 5, v254\n"
  "v_lshl_or_b32 v248, v247, 1, v248\n"
  "v_xor_b32 v248, v248, v246\n"
  "v_lshl_or_b32 v216, v248, 4, v245\n"
  "s_lshl_b32 s59, s19, 11\n"
  "s_add_u32 s59, s59, 0x18000\n"
  "v_add_u32 v216, s59, v216\n"
  "v_xor_b32 v217, 0x800, v216\n"
  "v_xor_b32 v218, 0x1000, v216\n"
  "v_xor_b32 v219, 0x1800, v216\n"
  "v_and_b32 v248, 3, v254\n"
  "v_lshlrev_b32 v248, 3, v248\n"
  "v_lshl_or_b32 v248, v247, 5, v248\n"
  "v_bfe_u32 v247, v254, 2, 2\n"
  "v_lshl_or_b32 v248, v247, 6, v248\n"
  "v_lshl_or_b32 v222, v244, 10, v248\n"
  "v_xor_b32 v225, 32, v222\n"
  "v_add_u32 v223, 0x10000, v222\n"
  "v_add_u32 v224, 0x20000, v222\n"
  "v_add_u32 v226, 0x10000, v225\n"
  "v_add_u32 v227, 0x20000, v225\n"
  "s_lshr_b32 s58, s19, 1\n"
  "s_lshl_b32 s58, s58, 13\n"
  "s_and_b32 s59, s19, 1\n"
  "s_lshl_b32 s59, s59, 9\n"
  "s_or_b32 s58, s58, s59\n"
  "v_add_u32 v228, s58, v222\n"
  "v_add_u32 v229, s58, v223\n"
  "v_add_u32 v230, s58, v224\n"
  "v_add_u32 v231, s58, v225\n"
  "v_add_u32 v232, s58, v226\n"
  "v_add_u32 v233, s58, v227\n"
  "s_add_u32 s84, s30, 0x2000000\n"
  "s_addc_u32 s85, s31, 0\n"
  "s_and_b32 s85, s85, 0xffff\n"
  "s_mov_b32 s86, 0x9000\n"
  "s_mov_b32 s87, 0x20000\n"
  "s_lshl_b32 s98, s56, 7\n"
  "s_lshl_b32 s99, s56, 12\n"
  "s_add_u32 s99, s99, 0x1000\n"
  "v_lshlrev_b32 v251, 4, v254\n"
  "s_mul_i32 s59, s19, 0x3000\n"
  "v_add_u32 v250, s59, v251\n"
  "v_mov_b32 v244, 0x3f803f80\n"
  "v_accvgpr_write_b32 a128, v244\n"
  "v_accvgpr_write_b32 a129, v244\n"
  "v_accvgpr_write_b32 a130, v244\n"
  "v_accvgpr_write_b32 a131, v244\n"
  "s_bfe_u32 s71, s70, 0x30004\n"
  "s_add_u32 m0, s65, 0x11000\n"
  "s_lshl_b32 s66, s71, 19\n"
  "buffer_load_dwordx4 v136, s[40:43], s66 offen lds\n"
  "buffer_load_dwordx4 v137, s[40:43], s66 offen offset:1024 lds\n"
  "buffer_load_dwordx4 v138, s[40:43], s66 offen offset:2048 lds\n"
  "buffer_load_dwordx4 v139, s[40:43], s66 offen offset:3072 lds\n"
  "s_sleep 8\n"
  "s_mov_b64 exec, 0\n"
  "s_cmp_eq_u32 s19, 0\n"
  "s_cbranch_scc1 L_st0\n"
  "s_cmp_eq_u32 s19, 1\n"
  "s_cbranch_scc1 L_st1\n"
  "s_cmp_eq_u32 s19, 2\n"
  "s_cbranch_scc1 L_st2\n"
  "s_cmp_eq_u32 s19, 3\n"
  "s_cbranch_scc1 L_st3\n"
  "L_stx:\n"
  "s_mov_b64 exec, -1\n"
  "L_warm:\n"
  "s_waitcnt vmcnt(4)\n"
  "s_barrier\n"
  "ds_read_b64_tr_b16 a[144:145], v222 offset:26624\n"
  "ds_read_b64_tr_b16 a[146:147], v222 offset:26880\n"
  "ds_read_b64_tr_b16 a[148:149], v225 offset:26624\n"
  "ds_read_b64_tr_b16 a[150:151], v225 offset:26880\n"
  "ds_read_b128 a[224:227], v128\n"
  "ds_read_b64_tr_b16 a[152:153], v222 offset:27136\n"
  "ds_read_b128 a[228:231], v128 offset:2048\n"
  "ds_read_b64_tr_b16 a[154:155], v222 offset:27392\n"
  "ds_read_b128 a[232:235], v128 offset:4096\n"
  "s_waitcnt lgkmcnt(4)\n"
  "v_mfma_f32_16x16x32_bf16 v[0:3], a[144:147], a[224:227], 0\n"
  "s_cmp_lt_u32 s19, 2\n"
  "s_cbranch_scc0 L_o3\n"
  "s_add_u32 m0, s97, 0xa800\n"
  "s_lshl_b32 s67, s71, 7\n"
  "s_add_u32 s67, s67, s96\n"
  "buffer_load_dwordx4 v132, s[36:39], s67 offen lds\n"
  "L_o3:\n"
  "s_nop 4\n"
  "s_waitcnt lgkmcnt(0)\n"
  "v_mfma_f32_16x16x32_bf16 v[32:35], a[144:147], a[228:231], 0\n"
  "ds_read_b64_tr_b16 a[156:157], v225 offset:27136\n"
  "ds_read_b64_tr_b16 a[158:159], v225 offset:27392\n"
  "s_nop 3\n"
  "v_mfma_f32_16x16x32_bf16 v[64:67], a[144:147], a[232:235], 0\n"
  "s_add_u32 m0, s64, 0xa800\n"
  "s_lshl_b32 s66, s71, 7\n"
  "ds_read_b64_tr_b16 a[160:161], v222 offset:34816\n"
  "s_nop 3\n"
  "v_mfma_f32_16x16x32_bf16 v[4:7], a[148:151], a[224:227], 0\n"
  "buffer_load_dwordx4 v132, s[36:39], s66 offen lds\n"
  "ds_read_b64_tr_b16 a[162:163], v222 offset:35072\n"
  "s_nop 3\n"
  "v_mfma_f32_16x16x32_bf16 v[36:39], a[148:151], a[228:231], 0\n"
  "buffer_load_dwordx4 v133, s[36:39], s66 offen offset:1024 lds\n"
  "ds_read_b64_tr_b16 a[164:165], v225 offset:34816\n"
  "s_nop 3\n"
  "v_mfma_f32_16x16x32_bf16 v[68:71], a[148:151], a[232:235], 0\n"
  "ds_read_b64_tr_b16 a[166:167], v225 offset:35072\n"
  "ds_read_b64_tr_b16 a[168:169], v222 offset:35328\n"
  "s_nop 3\n"
  "v_mfma_f32_16x16x32_bf16 v[8:11], a[152:155], a[224:227], 0\n"
  "buffer_load_dwordx4 v134, s[36:39], s66 offen offset:2048 lds\n"
  "ds_read_b64_tr_b16 a[170:171], v222 offset:35584\n"
  "s_nop 3\n"
  "v_mfma_f32_16x16x32_bf16 v[40:43], a[152:155], a[228:231], 0\n"
  "ds_read_b64_tr_b16 a[172:173], v225 offset:35328\n"
  "ds_read_b64_tr_b16 a[174:175], v225 offset:35584\n"
  "s_nop 3\n"
  "v_mfma_f32_16x16x32_bf16 v[72:75], a[152:155], a[232:235], 0\n"
  "buffer_load_dwordx4 v135, s[36:39], s66 offen offset:3072 lds\n"
  "s_add_u32 m0, s64, 0xb800\n"
  "s_nop 3\n"
  "s_waitcnt lgkmcnt(2)\n"
  "v_mfma_f32_16x16x32_bf16 v[12:15], a[156:159], a[224:227], 0\n"
  "s_lshl_b32 s67, s71, 7\n"
  "s_or_b32 s67, s67, 0x8000\n"
  "s_nop 3\n"
  "v_mfma_f32_16x16x32_bf16 v[44:47], a[156:159], a[228:231], 0\n"
  "buffer_load_dwordx4 v132, s[36:39], s67 offen lds\n"
  "ds_read_b64_tr_b16 a[176:177], v228 offset:26624\n"
  "s_nop 3\n"
  "v_mfma_f32_16x16x32_bf16 v[76:79], a[156:159], a[232:235], 0\n"
  "ds_read_b64_tr_b16 a[178:179], v228 offset:26880\n"
  "ds_read_b128 a[236:239], v130 offset:24576\n"
  "s_nop 3\n"
  "v_mfma_f32_16x16x32_bf16 v[16:19], a[160:163], a[224:227], 0\n"
  "ds_read_b64_tr_b16 a[180:181], v231 offset:26624\n"
  "ds_read_b64_tr_b16 a[182:183], v231 offset:26880\n"
  "s_nop 3\n"
  "v_mfma_f32_16x16x32_bf16 v[48:51], a[160:163], a[228:231], 0\n"
  "buffer_load_dwordx4 v133, s[36:39], s67 offen offset:1024 lds\n"
  "s_bfe_u32 s71, s70, 0x30008\n"
  "s_nop 3\n"
  "v_mfma_f32_16x16x32_bf16 v[80:83], a[160:163], a[232:235], 0\n"
  "s_add_u32 m0, s65, 0x1b800\n"
  "s_lshl_b32 s66, s71, 19\n"
  "s_nop 3\n"
  "v_mfma_f32_16x16x32_bf16 v[20:23], a[164:167], a[224:227], 0\n"
  "ds_read_b64_tr_b16 a[184:185], v222 offset:30720\n"
  "buffer_load_dwordx4 v136, s[40:43], s66 offen lds\n"
  "s_nop 3\n"
  "v_mfma_f32_16x16x32_bf16 v[52:55], a[164:167], a[228:231], 0\n"
  "ds_read_b64_tr_b16 a[186:187], v222 offset:30976\n"
  "ds_read_b128 a[240:243], v128 offset:512\n"
  "s_nop 3\n"
  "v_mfma_f32_16x16x32_bf16 v[84:87], a[164:167], a[232:235], 0\n"
  "buffer_load_dwordx4 v137, s[40:43], s66 offen offset:1024 lds\n"
  "ds_read_b64_tr_b16 a[188:189], v225 offset:30720\n"
  "s_nop 3\n"
  "v_mfma_f32_16x16x32_bf16 v[24:27], a[168:171], a[224:227], 0\n"
  "ds_read_b128 a[244:247], v128 offset:2560\n"
  "ds_read_b64_tr_b16 a[190:191], v225 offset:30976\n"
  "s_nop 3\n"
  "v_mfma_f32_16x16x32_bf16 v[56:59], a[168:171], a[228:231], 0\n"
  "ds_read_b128 a[248:251], v128 offset:4608\n"
  "ds_read_b64_tr_b16 a[192:193], v222 offset:31232\n"
  "s_nop 3\n"
  "v_mfma_f32_16x16x32_bf16 v[88:91], a[168:171], a[232:235], 0\n"
  "buffer_load_dwordx4 v138, s[40:43], s66 offen offset:2048 lds\n"
  "ds_read_b64_tr_b16 a[194:195], v222 offset:31488\n"
  "s_nop 3\n"
  "s_waitcnt lgkmcnt(3)\n"
  "v_mfma_f32_16x16x32_bf16 v[28:31], a[172:175], a[224:227], 0\n"
  "ds_read_b64_tr_b16 a[196:197], v225 offset:31232\n"
  "ds_read_b64_tr_b16 a[198:199], v225 offset:31488\n"
  "s_nop 3\n"
  "v_mfma_f32_16x16x32_bf16 v[60:63], a[172:175], a[228:231], 0\n"
  "buffer_load_dwordx4 v139, s[40:43], s66 offen offset:3072 lds\n"
  "ds_read_b64_tr_b16 a[200:201], v222 offset:38912\n"
  "s_nop 3\n"
  "v_mfma_f32_16x16x32_bf16 v[92:95], a[172:175], a[232:235], 0\n"
  "ds_read_b64_tr_b16 a[202:203], v222 offset:39168\n"
  "ds_read_b64_tr_b16 a[204:205], v225 offset:38912\n"
  "s_nop 3\n"
  "v_mfma_f32_16x16x32_bf16 v[96:99], a[176:179], a[236:239], 0\n"
  "s_cmp_lt_u32 s19, 2\n"
  "s_cbranch_scc0 L_o4\n"
  "s_add_u32 m0, s97, 0x15000\n"
  "s_lshl_b32 s67, s71, 7\n"
  "s_add_u32 s67, s67, s96\n"
  "buffer_load_dwordx4 v132, s[36:39], s67 offen lds\n"
  "L_o4:\n"
  "s_nop 3\n"
  "v_mfma_f32_16x16x32_bf16 v[100:103], a[180:183], a[236:239], 0\n"
  "s_add_u32 m0, s64, 0x15000\n"
  "s_lshl_b32 s66, s71, 7\n"
  "s_nop 3\n"
  "v_mfma_f32_16x16x32_bf16 v[0:3], a[184:187], a[240:243], v[0:3]\n"
  "ds_read_b64_tr_b16 a[206:207], v225 offset:39168\n"
  "ds_read_b64_tr_b16 a[208:209], v222 offset:39424\n"
  "s_nop 2\n"
  "v_mfma_f32_16x16x32_bf16 v[32:35], a[184:187], a[244:247], v[32:35]\n"
  "buffer_load_dwordx4 v132, s[36:39], s66 offen lds\n"
  "ds_read_b64_tr_b16 a[210:211], v222 offset:39680\n"
  "s_nop 2\n"
  "s_waitcnt lgkmcnt(3)\n"
  "v_mfma_f32_16x16x32_bf16 v[64:67], a[184:187], a[248:251], v[64:67]\n"
  "s_nop 2\n"
  "v_mfma_f32_16x16x32_bf16 v[4:7], a[188:191], a[240:243], v[4:7]\n"
  "buffer_load_dwordx4 v133, s[36:39], s66 offen offset:1024 lds\n"
  "ds_read_b64_tr_b16 a[212:213], v225 offset:39424\n"
  "s_nop 2\n"
  "v_mfma_f32_16x16x32_bf16 v[36:39], a[188:191], a[244:247], v[36:39]\n"
  "ds_read_b64_tr_b16 a[214:215], v225 offset:39680\n"
  "s_nop 2\n"
  "v_mfma_f32_16x16x32_bf16 v[68:71], a[188:191], a[248:251], v[68:71]\n"
  "buffer_load_dwordx4 v134, s[36:39], s66 offen offset:2048 lds\n"
  "s_nop 2\n"
  "v_mfma_f32_16x16x32_bf16 v[8:11], a[192:195], a[240:243], v[8:11]\n"
  "ds_read_b64_tr_b16 a[216:217], v228 offset:30720\n"
  "ds_read_b64_tr_b16 a[218:219], v228 offset:30976\n"
  "s_nop 2\n"
  "v_mfma_f32_16x16x32_bf16 v[40:43], a[192:195], a[244:247], v[40:43]\n"
  "ds_read_b64_tr_b16 a[220:221], v231 offset:30720\n"
  "ds_read_b64_tr_b16 a[222:223], v231 offset:30976\n"
  "s_nop 2\n"
  "v_mfma_f32_16x16x32_bf16 v[72:75], a[192:195], a[248:251], v[72:75]\n"
  "ds_read_b128 a[252:255], v130 offset:25088\n"
  "buffer_load_dwordx4 v135, s[36:39], s66 offen offset:3072 lds\n"
  "s_nop 2\n"
  "v_mfma_f32_16x16x32_bf16 v[12:15], a[196:199], a[240:243], v[12:15]\n"
  "s_add_u32 m0, s64, 0x16000\n"
  "s_lshl_b32 s67, s71, 7\n"
  "s_nop 2\n"
  "v_mfma_f32_16x16x32_bf16 v[44:47], a[196:199], a[244:247], v[44:47]\n"
  "s_or_b32 s67, s67, 0x8000\n"
  "buffer_load_dwordx4 v132, s[36:39], s67 offen lds\n"
  "s_nop 2\n"
  "v_mfma_f32_16x16x32_bf16 v[76:79], a[196:199], a[248:251], v[76:79]\n"
  "s_nop 2\n"
  "v_mfma_f32_16x16x32_bf16 v[16:19], a[200:203], a[240:243], v[16:19]\n"
  "buffer_load_dwordx4 v133, s[36:39], s67 offen offset:1024 lds\n"
  "s_nop 2\n"
  "v_mfma_f32_16x16x32_bf16 v[48:51], a[200:203], a[244:247], v[48:51]\n"
  "s_nop 2\n"
  "v_mfma_f32_16x16x32_bf16 v[80:83], a[200:203], a[248:251], v[80:83]\n"
  "s_nop 2\n"
  "s_waitcnt lgkmcnt(0)\n"
  "v_mfma_f32_16x16x32_bf16 v[20:23], a[204:207], a[240:243], v[20:23]\n"
  "s_nop 2\n"
  "v_mfma_f32_16x16x32_bf16 v[52:55], a[204:207], a[244:247], v[52:55]\n"
  "s_waitcnt vmcnt(10)\n"
  "s_barrier\n"
  "s_cmp_eq_u32 s19, 2\n"
  "s_cbranch_scc0 L_o10\n"
  "s_mov_b32 s92, s28\n"
  "s_and_b32 s93, s29, 0xffff\n"
  "s_mov_b32 s94, 0x400000\n"
  "s_mov_b32 s95, 0x20000\n"
  "v_lshlrev_b32 v188, 7, v254\n"
  "s_lshl_b32 s58, s56, 3\n"
  "s_lshr_b32 s59, s57, 2\n"
  "s_add_u32 s58, s58, s59\n"
  "s_mul_i32 s58, s58, 3\n"
  "s_add_u32 s59, s88, 1\n"
  "s_lshr_b32 s59, s59, 1\n"
  "s_add_u32 s58, s58, s59\n"
  "s_mul_i32 s58, s58, 0x6000\n"
  "s_add_u32 s58, s58, 0\n"
  "s_cmp_eq_u32 s88, 1\n"
  "s_cselect_b32 s58, 0x40000000, s58\n"
  "buffer_load_dword v183, v188, s[92:95], s58 offen\n"
  "L_o10:\n"
  "s_nop 2\n"
  "v_mfma_f32_16x16x32_bf16 v[84:87], a[204:207], a[248:251], v[84:87]\n"
  "s_bfe_u32 s71, s70, 0x3000c\n"
  "ds_read_b64_tr_b16 a[144:145], v223 offset:4096\n"
  "s_add_u32 m0, s65, 0x6800\n"
  "s_nop 2\n"
  "v_mfma_f32_16x16x32_bf16 v[24:27], a[208:211], a[240:243], v[24:27]\n"
  "ds_read_b64_tr_b16 a[146:147], v223 offset:4352\n"
  "ds_read_b128 a[224:227], v128 offset:43008\n"
  "s_lshl_b32 s66, s71, 19\n"
  "s_nop 2\n"
  "v_mfma_f32_16x16x32_bf16 v[56:59], a[208:211], a[244:247], v[56:59]\n"
  "buffer_load_dwordx4 v136, s[40:43], s66 offen lds\n"
  "ds_read_b128 a[228:231], v128 offset:45056\n"
  "ds_read_b128 a[232:235], v128 offset:47104\n"
  "s_nop 2\n"
  "v_mfma_f32_16x16x32_bf16 v[88:91], a[208:211], a[248:251], v[88:91]\n"
  "ds_read_b64_tr_b16 a[148:149], v226 offset:4096\n"
  "ds_read_b64_tr_b16 a[150:151], v226 offset:4352\n"
  "s_nop 2\n"
  "v_mfma_f32_16x16x32_bf16 v[28:31], a[212:215], a[240:243], v[28:31]\n"
  "buffer_load_dwordx4 v137, s[40:43], s66 offen offset:1024 lds\n"
  "ds_read_b64_tr_b16 a[152:153], v223 offset:4608\n"
  "s_nop 2\n"
  "v_mfma_f32_16x16x32_bf16 v[60:63], a[212:215], a[244:247], v[60:63]\n"
  "ds_read_b64_tr_b16 a[154:155], v223 offset:4864\n"
  "ds_read_b64_tr_b16 a[156:157], v226 offset:4608\n"
  "s_nop 2\n"
  "v_mfma_f32_16x16x32_bf16 v[92:95], a[212:215], a[248:251], v[92:95]\n"
  "ds_read_b64_tr_b16 a[158:159], v226 offset:4864\n"
  "ds_read_b64_tr_b16 a[160:161], v223 offset:12288\n"
  "s_nop 2\n"
  "v_mfma_f32_16x16x32_bf16 v[96:99], a[216:219], a[252:255], v[96:99]\n"
  "ds_read_b64_tr_b16 a[162:163], v223 offset:12544\n"
  "ds_read_b64_tr_b16 a[164:165], v226 offset:12288\n"
  "s_nop 2\n"
  "v_mfma_f32_16x16x32_bf16 v[100:103], a[220:223], a[252:255], v[100:103]\n"
  "buffer_load_dwordx4 v138, s[40:43], s66 offen offset:2048 lds\n"
  "ds_read_b64_tr_b16 a[166:167], v226 offset:12544\n"
  "s_nop 1\n"
  "s_waitcnt lgkmcnt(3)\n"
  "v_mfma_f32_16x16x32_bf16 v[0:3], a[144:147], a[224:227], v[0:3]\n"
  "ds_read_b64_tr_b16 a[168:169], v223 offset:12800\n"
  "ds_read_b64_tr_b16 a[170:171], v223 offset:13056\n"
  "s_nop 1\n"
  "v_mfma_f32_16x16x32_bf16 v[32:35], a[144:147], a[228:231], v[32:35]\n"
  "s_nop 1\n"
  "v_mfma_f32_16x16x32_bf16 v[64:67], a[144:147], a[232:235], v[64:67]\n"
  "s_nop 1\n"
  "v_mfma_f32_16x16x32_bf16 v[4:7], a[148:151], a[224:227], v[4:7]\n"
  "buffer_load_dwordx4 v139, s[40:43], s66 offen offset:3072 lds\n"
  "ds_read_b64_tr_b16 a[172:173], v226 offset:12800\n"
  "s_nop 1\n"
  "v_mfma_f32_16x16x32_bf16 v[36:39], a[148:151], a[228:231], v[36:39]\n"
  "ds_read_b64_tr_b16 a[174:175], v226 offset:13056\n"
  "s_nop 1\n"
  "v_mfma_f32_16x16x32_bf16 v[68:71], a[148:151], a[232:235], v[68:71]\n"
  "s_nop 1\n"
  "v_mfma_f32_16x16x32_bf16 v[8:11], a[152:155], a[224:227], v[8:11]\n"
  "ds_read_b64_tr_b16 a[176:177], v229 offset:4096\n"
  "ds_read_b64_tr_b16 a[178:179], v229 offset:4352\n"
  "s_nop 1\n"
  "v_mfma_f32_16x16x32_bf16 v[40:43], a[152:155], a[228:231], v[40:43]\n"
  "ds_read_b128 a[236:239], v131 offset:2048\n"
  "ds_read_b64_tr_b16 a[180:181], v232 offset:4096\n"
  "s_nop 1\n"
  "v_mfma_f32_16x16x32_bf16 v[72:75], a[152:155], a[232:235], v[72:75]\n"
  "s_cmp_lt_u32 s19, 2\n"
  "s_cbranch_scc0 L_o5\n"
  "s_add_u32 m0, s97, 0\n"
  "s_lshl_b32 s67, s71, 7\n"
  "s_add_u32 s67, s67, s96\n"
  "buffer_load_dwordx4 v132, s[36:39], s67 offen lds\n"
  "L_o5:\n"
  "s_nop 1\n"
  "v_mfma_f32_16x16x32_bf16 v[12:15], a[156:159], a[224:227], v[12:15]\n"
  "ds_read_b64_tr_b16 a[182:183], v232 offset:4352\n"
  "ds_read_b64_tr_b16 a[184:185], v223 offset:8192\n"
  "s_nop 1\n"
  "v_mfma_f32_16x16x32_bf16 v[44:47], a[156:159], a[228:231], v[44:47]\n"
  "s_add_u32 m0, s64, 0\n"
  "s_lshl_b32 s66, s71, 7\n"
  "s_nop 1\n"
  "v_mfma_f32_16x16x32_bf16 v[76:79], a[156:159], a[232:235], v[76:79]\n"
  "ds_read_b64_tr_b16 a[186:187], v223 offset:8448\n"
  "ds_read_b128 a[240:243], v128 offset:43520\n"
  "s_nop 1\n"
  "s_waitcnt lgkmcnt(3)\n"
  "v_mfma_f32_16x16x32_bf16 v[16:19], a[160:163], a[224:227], v[16:19]\n"
  "ds_read_b64_tr_b16 a[188:189], v226 offset:8192\n"
  "ds_read_b64_tr_b16 a[190:191], v226 offset:8448\n"
  "s_nop 1\n"
  "v_mfma_f32_16x16x32_bf16 v[48:51], a[160:163], a[228:231], v[48:51]\n"
  "buffer_load_dwordx4 v132, s[36:39], s66 offen lds\n"
  "ds_read_b128 a[244:247], v128 offset:45568\n"
  "s_nop 1\n"
  "v_mfma_f32_16x16x32_bf16 v[80:83], a[160:163], a[232:235], v[80:83]\n"
  "ds_read_b64_tr_b16 a[192:193], v223 offset:8704\n"
  "ds_read_b64_tr_b16 a[194:195], v223 offset:8960\n"
  "s_nop 1\n"
  "v_mfma_f32_16x16x32_bf16 v[20:23], a[164:167], a[224:227], v[20:23]\n"
  "ds_read_b128 a[248:251], v128 offset:47616\n"
  "s_nop 1\n"
  "v_mfma_f32_16x16x32_bf16 v[52:55], a[164:167], a[228:231], v[52:55]\n"
  "s_nop 1\n"
  "v_mfma_f32_16x16x32_bf16 v[84:87], a[164:167], a[232:235], v[84:87]\n"
  "buffer_load_dwordx4 v133, s[36:39], s66 offen offset:1024 lds\n"
  "ds_read_b64_tr_b16 a[196:197], v226 offset:8704\n"
  "s_nop 1\n"
  "v_mfma_f32_16x16x32_bf16 v[24:27], a[168:171], a[224:227], v[24:27]\n"
  "ds_read_b64_tr_b16 a[198:199], v226 offset:8960\n"
  "s_nop 1\n"
  "v_mfma_f32_16x16x32_bf16 v[56:59], a[168:171], a[228:231], v[56:59]\n"
  "s_nop 1\n"
  "v_mfma_f32_16x16x32_bf16 v[88:91], a[168:171], a[232:235], v[88:91]\n"
  "ds_read_b64_tr_b16 a[200:201], v223 offset:16384\n"
  "ds_read_b64_tr_b16 a[202:203], v223 offset:16640\n"
  "s_nop 1\n"
  "v_mfma_f32_16x16x32_bf16 v[28:31], a[172:175], a[224:227], v[28:31]\n"
  "s_nop 1\n"
  "v_mfma_f32_16x16x32_bf16 v[60:63], a[172:175], a[228:231], v[60:63]\n"
  "buffer_load_dwordx4 v134, s[36:39], s66 offen offset:2048 lds\n"
  "s_nop 1\n"
  "v_mfma_f32_16x16x32_bf16 v[92:95], a[172:175], a[232:235], v[92:95]\n"
  "ds_read_b64_tr_b16 a[204:205], v226 offset:16384\n"
  "ds_read_b64_tr_b16 a[206:207], v226 offset:16640\n"
  "s_nop 1\n"
  "v_mfma_f32_16x16x32_bf16 v[96:99], a[176:179], a[236:239], v[96:99]\n"
  "s_nop 0\n"
  "v_mfma_f32_16x16x32_bf16 v[100:103], a[180:183], a[236:239], v[100:103]\n"
  "s_nop 0\n"
  "s_waitcnt lgkmcnt(2)\n"
  "v_mfma_f32_16x16x32_bf16 v[0:3], a[184:187], a[240:243], v[0:3]\n"
  "ds_read_b64_tr_b16 a[208:209], v223 offset:16896\n"
  "ds_read_b64_tr_b16 a[210:211], v223 offset:17152\n"
  "s_nop 0\n"
  "v_mfma_f32_16x16x32_bf16 v[32:35], a[184:187], a[244:247], v[32:35]\n"
  "buffer_load_dwordx4 v135, s[36:39], s66 offen offset:3072 lds\n"
  "s_nop 0\n"
  "v_mfma_f32_16x16x32_bf16 v[64:67], a[184:187], a[248:251], v[64:67]\n"
  "s_add_u32 m0, s64, 0x1000\n"
  "s_lshl_b32 s67, s71, 7\n"
  "s_nop 0\n"
  "v_mfma_f32_16x16x32_bf16 v[4:7], a[188:191], a[240:243], v[4:7]\n"
  "ds_read_b64_tr_b16 a[212:213], v226 offset:16896\n"
  "ds_read_b64_tr_b16 a[214:215], v226 offset:17152\n"
  "s_nop 0\n"
  "v_mfma_f32_16x16x32_bf16 v[36:39], a[188:191], a[244:247], v[36:39]\n"
  "s_or_b32 s67, s67, 0x8000\n"
  "s_nop 0\n"
  "v_mfma_f32_16x16x32_bf16 v[68:71], a[188:191], a[248:251], v[68:71]\n"
  "buffer_load_dwordx4 v132, s[36:39], s67 offen lds\n"
  "s_nop 0\n"
  "v_mfma_f32_16x16x32_bf16 v[8:11], a[192:195], a[240:243], v[8:11]\n"
  "ds_read_b64_tr_b16 a[216:217], v229 offset:8192\n"
  "ds_read_b64_tr_b16 a[218:219], v229 offset:8448\n"
  "s_nop 0\n"
  "v_mfma_f32_16x16x32_bf16 v[40:43], a[192:195], a[244:247], v[40:43]\n"
  "ds_read_b64_tr_b16 a[220:221], v232 offset:8192\n"
  "ds_read_b64_tr_b16 a[222:223], v232 offset:8448\n"
  "s_nop 0\n"
  "v_mfma_f32_16x16x32_bf16 v[72:75], a[192:195], a[248:251], v[72:75]\n"
  "ds_read_b128 a[252:255], v131 offset:2560\n"
  "s_nop 0\n"
  "v_mfma_f32_16x16x32_bf16 v[12:15], a[196:199], a[240:243], v[12:15]\n"
  "s_nop 0\n"
  "v_mfma_f32_16x16x32_bf16 v[44:47], a[196:199], a[244:247], v[44:47]\n"
  "buffer_load_dwordx4 v133, s[36:39], s67 offen offset:1024 lds\n"
  "s_nop 0\n"
  "v_mfma_f32_16x16x32_bf16 v[76:79], a[196:199], a[248:251], v[76:79]\n"
  "s_nop 0\n"
  "v_mfma_f32_16x16x32_bf16 v[16:19], a[200:203], a[240:243], v[16:19]\n"
  "s_nop 0\n"
  "v_mfma_f32_16x16x32_bf16 v[48:51], a[200:203], a[244:247], v[48:51]\n"
  "L_st0:\n"
  "s_cbranch_execz L_st4\n"
  "s_nop 0\n"
  "v_mfma_f32_16x16x32_bf16 v[80:83], a[200:203], a[248:251], v[80:83]\n"
  "s_nop 0\n"
  "s_waitcnt lgkmcnt(0)\n"
  "v_mfma_f32_16x16x32_bf16 v[20:23], a[204:207], a[240:243], v[20:23]\n"
  "s_nop 0\n"
  "v_mfma_f32_16x16x32_bf16 v[52:55], a[204:207], a[244:247], v[52:55]\n"
  "s_waitcnt vmcnt(10)\n"
  "s_barrier\n"
  "s_cmp_eq_u32 s19, 3\n"
  "s_cbranch_scc0 L_o11\n"
  "v_readlane_b32 s89, v252, s57\n"
  "s_nop 3\n"
  "s_add_u32 s89, s89, 1\n"
  "L_nonce:\n"
  "s_add_u32 s89, s89, 1\n"
  "s_nop 1\n"
  "v_subrev_u32 v189, s89, v184\n"
  "v_cmp_gt_u32 vcc, 2, v189\n"
  "s_nop 4\n"
  "s_cbranch_vccnz L_nonce\n"
  "v_mov_b32 v189, s89\n"
  "s_lshl_b32 s58, s57, 2\n"
  "s_add_u32 s58, s58, s98\n"
  "v_lshlrev_b32 v188, 20, v254\n"
  "buffer_store_dword v189, v188, s[84:87], s58 offen sc1\n"
  "L_o11:\n"
  "s_cmp_eq_u32 s19, 2\n"
  "s_cbranch_scc0 L_o12\n"
  "s_mov_b32 s92, s28\n"
  "s_and_b32 s93, s29, 0xffff\n"
  "s_mov_b32 s94, 0x400000\n"
  "s_mov_b32 s95, 0x20000\n"
  "v_lshlrev_b32 v188, 7, v254\n"
  "s_lshl_b32 s58, s56, 3\n"
  "s_lshr_b32 s59, s57, 2\n"
  "s_add_u32 s58, s58, s59\n"
  "s_mul_i32 s58, s58, 3\n"
  "s_add_u32 s59, s88, 1\n"
  "s_lshr_b32 s59, s59, 1\n"
  "s_add_u32 s58, s58, s59\n"
  "s_mul_i32 s58, s58, 0x6000\n"
  "s_add_u32 s58, s58, 0x2000\n"
  "s_cmp_eq_u32 s88, 1\n"
  "s_cselect_b32 s58, 0x40000000, s58\n"
  "buffer_load_dword v183, v188, s[92:95], s58 offen\n"
  "L_o12:\n"
  "s_cmp_eq_u32 s19, 1\n"
  "s_cbranch_scc0 L_o13\n"
  "s_mov_b32 s92, s28\n"
  "s_and_b32 s93, s29, 0xffff\n"
  "s_mov_b32 s94, 0x400000\n"
  "s_mov_b32 s95, 0x20000\n"
  "v_lshlrev_b32 v188, 7, v254\n"
  "s_lshl_b32 s58, s56, 3\n"
  "s_lshr_b32 s59, s57, 2\n"
  "s_add_u32 s58, s58, s59\n"
  "s_mul_i32 s58, s58, 3\n"
  "s_add_u32 s59, s88, 1\n"
  "s_lshr_b32 s59, s59, 1\n"
  "s_add_u32 s58, s58, s59\n"
  "s_mul_i32 s58, s58, 0x6000\n"
  "s_add_u32 s58, s58, 0x4000\n"
  "s_cmp_eq_u32 s88, 1\n"
  "s_cselect_b32 s58, 0x40000000, s58\n"
  "buffer_load_dword v183, v188, s[92:95], s58 offen\n"
  "L_o13:\n"
  "s_nop 0\n"
  "v_mfma_f32_16x16x32_bf16 v[84:87], a[204:207], a[248:251], v[84:87]\n"
  "s_bfe_u32 s71, s70, 0x30010\n"
  "ds_read_b64_tr_b16 a[144:145], v223 offset:47104\n"
  "s_add_u32 m0, s65, 0x11000\n"
  "s_nop 0\n"
  "v_mfma_f32_16x16x32_bf16 v[24:27], a[208:211], a[240:243], v[24:27]\n"
  "ds_read_b64_tr_b16 a[146:147], v223 offset:47360\n"
  "ds_read_b128 a[224:227], v129 offset:20480\n"
  "s_lshl_b32 s66, s71, 19\n"
  "s_nop 0\n"
  "v_mfma_f32_16x16x32_bf16 v[56:59], a[208:211], a[244:247], v[56:59]\n"
  "buffer_load_dwordx4 v136, s[40:43], s66 offen lds\n"
  "ds_read_b128 a[228:231], v129 offset:22528\n"
  "ds_read_b128 a[232:235], v129 offset:24576\n"
  "s_nop 0\n"
  "v_mfma_f32_16x16x32_bf16 v[88:91], a[208:211], a[248:251], v[88:91]\n"
  "ds_read_b64_tr_b16 a[148:149], v226 offset:47104\n"
  "ds_read_b64_tr_b16 a[150:151], v226 offset:47360\n"
  "s_nop 0\n"
  "v_mfma_f32_16x16x32_bf16 v[28:31], a[212:215], a[240:243], v[28:31]\n"
  "buffer_load_dwordx4 v137, s[40:43], s66 offen offset:1024 lds\n"
  "ds_read_b64_tr_b16 a[152:153], v223 offset:47616\n"
  "s_nop 0\n"
  "v_mfma_f32_16x16x32_bf16 v[60:63], a[212:215], a[244:247], v[60:63]\n"
  "ds_read_b64_tr_b16 a[154:155], v223 offset:47872\n"
  "ds_read_b64_tr_b16 a[156:157], v226 offset:47616\n"
  "v_mfma_f32_16x16x32_bf16 v[92:95], a[212:215], a[248:251], v[92:95]\n"
  "ds_read_b64_tr_b16 a[158:159], v226 offset:47872\n"
  "ds_read_b64_tr_b16 a[160:161], v223 offset:55296\n"
  "v_mfma_f32_16x16x32_bf16 v[96:99], a[216:219], a[252:255], v[96:99]\n"
  "ds_read_b64_tr_b16 a[162:163], v223 offset:55552\n"
  "ds_read_b64_tr_b16 a[164:165], v226 offset:55296\n"
  "v_mfma_f32_16x16x32_bf16 v[100:103], a[220:223], a[252:255], v[100:103]\n"
  "buffer_load_dwordx4 v138, s[40:43], s66 offen offset:2048 lds\n"
  "ds_read_b64_tr_b16 a[166:167], v226 offset:55552\n"
  "s_waitcnt lgkmcnt(7)\n"
  "v_mfma_f32_16x16x32_bf16 v[0:3], a[144:147], a[224:227], v[0:3]\n"
  "ds_read_b64_tr_b16 a[168:169], v223 offset:55808\n"
  "ds_read_b64_tr_b16 a[170:171], v223 offset:56064\n"
  "v_mfma_f32_16x16x32_bf16 v[32:35], a[144:147], a[228:231], v[32:35]\n"
  "v_mfma_f32_16x16x32_bf16 v[64:67], a[144:147], a[232:235], v[64:67]\n"
  "v_mfma_f32_16x16x32_bf16 v[4:7], a[148:151], a[224:227], v[4:7]\n"
  "buffer_load_dwordx4 v139, s[40:43], s66 offen offset:3072 lds\n"
  "ds_read_b64_tr_b16 a[172:173], v226 offset:55808\n"
  "v_mfma_f32_16x16x32_bf16 v[36:39], a[148:151], a[228:231], v[36:39]\n"
  "ds_read_b64_tr_b16 a[174:175], v226 offset:56064\n"
  "v_mfma_f32_16x16x32_bf16 v[68:71], a[148:151], a[232:235], v[68:71]\n"
  "s_waitcnt lgkmcnt(5)\n"
  "v_mfma_f32_16x16x32_bf16 v[8:11], a[152:155], a[224:227], v[8:11]\n"
  "ds_read_b64_tr_b16 a[176:177], v229 offset:47104\n"
  "ds_read_b64_tr_b16 a[178:179], v229 offset:47360\n"
  "v_mfma_f32_16x16x32_bf16 v[40:43], a[152:155], a[228:231], v[40:43]\n"
  "ds_read_b128 a[236:239], v131 offset:45056\n"
  "ds_read_b64_tr_b16 a[180:181], v232 offset:47104\n"
  "v_mfma_f32_16x16x32_bf16 v[72:75], a[152:155], a[232:235], v[72:75]\n"
  "s_cmp_lt_u32 s19, 2\n"
  "s_cbranch_scc0 L_o6\n"
  "s_add_u32 m0, s97, 0xa800\n"
  "s_lshl_b32 s67, s71, 7\n"
  "s_add_u32 s67, s67, s96\n"
  "buffer_load_dwordx4 v132, s[36:39], s67 offen lds\n"
  "L_o6:\n"
  "v_mfma_f32_16x16x32_bf16 v[12:15], a[156:159], a[224:227], v[12:15]\n"
  "ds_read_b64_tr_b16 a[182:183], v232 offset:47360\n"
  "ds_read_b64_tr_b16 a[184:185], v223 offset:51200\n"
  "v_mfma_f32_16x16x32_bf16 v[44:47], a[156:159], a[228:231], v[44:47]\n"
  "s_add_u32 m0, s64, 0xa800\n"
  "s_lshl_b32 s66, s71, 7\n"
  "v_mfma_f32_16x16x32_bf16 v[76:79], a[156:159], a[232:235], v[76:79]\n"
  "ds_read_b64_tr_b16 a[186:187], v223 offset:51456\n"
  "ds_read_b128 a[240:243], v129 offset:20992\n"
  "v_mfma_f32_16x16x32_bf16 v[16:19], a[160:163], a[224:227], v[16:19]\n"
  "ds_read_b64_tr_b16 a[188:189], v226 offset:51200\n"
  "ds_read_b64_tr_b16 a[190:191], v226 offset:51456\n"
  "v_mfma_f32_16x16x32_bf16 v[48:51], a[160:163], a[228:231], v[48:51]\n"
  "buffer_load_dwordx4 v132, s[36:39], s66 offen lds\n"
  "ds_read_b128 a[244:247], v129 offset:23040\n"
  "v_mfma_f32_16x16x32_bf16 v[80:83], a[160:163], a[232:235], v[80:83]\n"
  "ds_read_b64_tr_b16 a[192:193], v223 offset:51712\n"
  "ds_read_b64_tr_b16 a[194:195], v223 offset:51968\n"
  "s_waitcnt lgkmcnt(7)\n"
  "v_mfma_f32_16x16x32_bf16 v[20:23], a[164:167], a[224:227], v[20:23]\n"
  "ds_read_b128 a[248:251], v129 offset:25088\n"
  "v_mfma_f32_16x16x32_bf16 v[52:55], a[164:167], a[228:231], v[52:55]\n"
  "v_mfma_f32_16x16x32_bf16 v[84:87], a[164:167], a[232:235], v[84:87]\n"
  "buffer_load_dwordx4 v133, s[36:39], s66 offen offset:1024 lds\n"
  "ds_read_b64_tr_b16 a[196:197], v226 offset:51712\n"
  "v_mfma_f32_16x16x32_bf16 v[24:27], a[168:171], a[224:227], v[24:27]\n"
  "ds_read_b64_tr_b16 a[198:199], v226 offset:51968\n"
  "v_mfma_f32_16x16x32_bf16 v[56:59], a[168:171], a[228:231], v[56:59]\n"
  "v_mfma_f32_16x16x32_bf16 v[88:91], a[168:171], a[232:235], v[88:91]\n"
  "ds_read_b64_tr_b16 a[200:201], v223 offset:59392\n"
  "ds_read_b64_tr_b16 a[202:203], v223 offset:59648\n"
  "v_mfma_f32_16x16x32_bf16 v[28:31], a[172:175], a[224:227], v[28:31]\n"
  "v_mfma_f32_16x16x32_bf16 v[60:63], a[172:175], a[228:231], v[60:63]\n"
  "buffer_load_dwordx4 v134, s[36:39], s66 offen offset:2048 lds\n"
  "v_mfma_f32_16x16x32_bf16 v[92:95], a[172:175], a[232:235], v[92:95]\n"
  "ds_read_b64_tr_b16 a[204:205], v226 offset:59392\n"
  "ds_read_b64_tr_b16 a[206:207], v226 offset:59648\n"
  "v_mfma_f32_16x16x32_bf16 v[96:99], a[176:179], a[236:239], v[96:99]\n"
  "v_mfma_f32_16x16x32_bf16 v[100:103], a[180:183], a[236:239], v[100:103]\n"
  "s_waitcnt lgkmcnt(5)\n"
  "v_mfma_f32_16x16x32_bf16 v[0:3], a[184:187], a[240:243], v[0:3]\n"
  "ds_read_b64_tr_b16 a[208:209], v223 offset:59904\n"
  "ds_read_b64_tr_b16 a[210:211], v223 offset:60160\n"
  "v_mfma_f32_16x16x32_bf16 v[32:35], a[184:187], a[244:247], v[32:35]\n"
  "buffer_load_dwordx4 v135, s[36:39], s66 offen offset:3072 lds\n"
  "v_mfma_f32_16x16x32_bf16 v[64:67], a[184:187], a[248:251], v[64:67]\n"
  "s_add_u32 m0, s64, 0xb800\n"
  "s_lshl_b32 s67, s71, 7\n"
  "v_mfma_f32_16x16x32_bf16 v[4:7], a[188:191], a[240:243], v[4:7]\n"
  "ds_read_b64_tr_b16 a[212:213], v226 offset:59904\n"
  "ds_read_b64_tr_b16 a[214:215], v226 offset:60160\n"
  "v_mfma_f32_16x16x32_bf16 v[36:39], a[188:191], a[244:247], v[36:39]\n"
  "s_or_b32 s67, s67, 0x8000\n"
  "v_mfma_f32_16x16x32_bf16 v[68:71], a[188:191], a[248:251], v[68:71]\n"
  "buffer_load_dwordx4 v132, s[36:39], s67 offen lds\n"
  "v_mfma_f32_16x16x32_bf16 v[8:11], a[192:195], a[240:243], v[8:11]\n"
  "ds_read_b64_tr_b16 a[216:217], v229 offset:51200\n"
  "ds_read_b64_tr_b16 a[218:219], v229 offset:51456\n"
  "v_mfma_f32_16x16x32_bf16 v[40:43], a[192:195], a[244:247], v[40:43]\n"
  "ds_read_b64_tr_b16 a[220:221], v232 offset:51200\n"
  "ds_read_b64_tr_b16 a[222:223], v232 offset:51456\n"
  "v_mfma_f32_16x16x32_bf16 v[72:75], a[192:195], a[248:251], v[72:75]\n"
  "ds_read_b128 a[252:255], v131 offset:45568\n"
  "s_waitcnt lgkmcnt(6)\n"
  "v_mfma_f32_16x16x32_bf16 v[12:15], a[196:199], a[240:243], v[12:15]\n"
  "v_mfma_f32_16x16x32_bf16 v[44:47], a[196:199], a[244:247], v[44:47]\n"
  "buffer_load_dwordx4 v133, s[36:39], s67 offen offset:1024 lds\n"
  "v_mfma_f32_16x16x32_bf16 v[76:79], a[196:199], a[248:251], v[76:79]\n"
  "v_mfma_f32_16x16x32_bf16 v[16:19], a[200:203], a[240:243], v[16:19]\n"
  "v_mfma_f32_16x16x32_bf16 v[48:51], a[200:203], a[244:247], v[48:51]\n"
  "v_mfma_f32_16x16x32_bf16 v[80:83], a[200:203], a[248:251], v[80:83]\n"
  "v_mfma_f32_16x16x32_bf16 v[20:23], a[204:207], a[240:243], v[20:23]\n"
  "v_mfma_f32_16x16x32_bf16 v[52:55], a[204:207], a[244:247], v[52:55]\n"
  "s_waitcnt vmcnt(10) lgkmcnt(0)\n"
  "s_barrier\n"
  "s_bfe_u32 s71, s70, 0x30014\n"
  "v_mfma_f32_16x16x32_bf16 v[84:87], a[204:207], a[248:251], v[84:87]\n"
  "ds_read_b64_tr_b16 a[144:145], v222 offset:26624\n"
  "s_add_u32 m0, s65, 0x1b800\n"
  "ds_read_b64_tr_b16 a[146:147], v222 offset:26880\n"
  "v_mfma_f32_16x16x32_bf16 v[24:27], a[208:211], a[240:243], v[24:27]\n"
  "ds_read_b128 a[224:227], v128\n"
  "s_lshl_b32 s66, s71, 19\n"
  "buffer_load_dwordx4 v136, s[40:43], s66 offen lds\n"
  "v_mfma_f32_16x16x32_bf16 v[56:59], a[208:211], a[244:247], v[56:59]\n"
  "ds_read_b128 a[228:231], v128 offset:2048\n"
  "ds_read_b128 a[232:235], v128 offset:4096\n"
  "ds_read_b64_tr_b16 a[148:149], v225 offset:26624\n"
  "v_mfma_f32_16x16x32_bf16 v[88:91], a[208:211], a[248:251], v[88:91]\n"
  "ds_read_b64_tr_b16 a[150:151], v225 offset:26880\n"
  "buffer_load_dwordx4 v137, s[40:43], s66 offen offset:1024 lds\n"
  "v_mfma_f32_16x16x32_bf16 v[28:31], a[212:215], a[240:243], v[28:31]\n"
  "ds_read_b64_tr_b16 a[152:153], v222 offset:27136\n"
  "ds_read_b64_tr_b16 a[154:155], v222 offset:27392\n"
  "v_mfma_f32_16x16x32_bf16 v[60:63], a[212:215], a[244:247], v[60:63]\n"
  "buffer_load_dwordx4 v138, s[40:43], s66 offen offset:2048 lds\n"
  "ds_read_b64_tr_b16 a[156:157], v225 offset:27136\n"
  "v_mfma_f32_16x16x32_bf16 v[92:95], a[212:215], a[248:251], v[92:95]\n"
  "ds_read_b64_tr_b16 a[158:159], v225 offset:27392\n"
  "ds_read_b64_tr_b16 a[160:161], v222 offset:34816\n"
  "v_mfma_f32_16x16x32_bf16 v[96:99], a[216:219], a[252:255], v[96:99]\n"
  "ds_read_b64_tr_b16 a[162:163], v222 offset:35072\n"
  "ds_read_b64_tr_b16 a[164:165], v225 offset:34816\n"
  "v_mfma_f32_16x16x32_bf16 v[100:103], a[220:223], a[252:255], v[100:103]\n"
  "ds_read_b64_tr_b16 a[166:167], v225 offset:35072\n"
  "s_waitcnt lgkmcnt(8)\n"
  "v_mfma_f32_16x16x32_bf16 v[0:3], a[144:147], a[224:227], v[0:3]\n"
  "buffer_load_dwordx4 v139, s[40:43], s66 offen offset:3072 lds\n"
  "ds_read_b64_tr_b16 a[168:169], v222 offset:35328\n"
  "v_mfma_f32_16x16x32_bf16 v[32:35], a[144:147], a[228:231], v[32:35]\n"
  "ds_read_b64_tr_b16 a[170:171], v222 offset:35584\n"
  "v_mfma_f32_16x16x32_bf16 v[64:67], a[144:147], a[232:235], v[64:67]\n"
  "v_mfma_f32_16x16x32_bf16 v[4:7], a[148:151], a[224:227], v[4:7]\n"
  "s_cmp_lt_u32 s19, 2\n"
  "s_cbranch_scc0 L_o7\n"
  "s_add_u32 m0, s97, 0x15000\n"
  "s_lshl_b32 s67, s71, 7\n"
  "s_add_u32 s67, s67, s96\n"
  "buffer_load_dwordx4 v132, s[36:39], s67 offen lds\n"
  "L_o7:\n"
  "v_mfma_f32_16x16x32_bf16 v[36:39], a[148:151], a[228:231], v[36:39]\n"
  "s_add_u32 m0, s64, 0x15000\n"
  "s_lshl_b32 s66, s71, 7\n"
  "v_mfma_f32_16x16x32_bf16 v[68:71], a[148:151], a[232:235], v[68:71]\n"
  "ds_read_b64_tr_b16 a[172:173], v225 offset:35328\n"
  "ds_read_b64_tr_b16 a[174:175], v225 offset:35584\n"
  "s_waitcnt lgkmcnt(2)\n"
  "v_mfma_f32_16x16x32_bf16 v[8:11], a[152:155], a[224:227], v[8:11]\n"
  "ds_read_b64_tr_b16 a[176:177], v228 offset:26624\n"
  "ds_read_b64_tr_b16 a[178:179], v228 offset:26880\n"
  "v_mfma_f32_16x16x32_bf16 v[40:43], a[152:155], a[228:231], v[40:43]\n"
  "buffer_load_dwordx4 v132, s[36:39], s66 offen lds\n"
  "ds_read_b128 a[236:239], v130 offset:24576\n"
  "v_mfma_f32_16x16x32_bf16 v[72:75], a[152:155], a[232:235], v[72:75]\n"
  "ds_read_b64_tr_b16 a[180:181], v231 offset:26624\n"
  "ds_read_b64_tr_b16 a[182:183], v231 offset:26880\n"
  "v_mfma_f32_16x16x32_bf16 v[12:15], a[156:159], a[224:227], v[12:15]\n"
  "ds_read_b64_tr_b16 a[184:185], v222 offset:30720\n"
  "ds_read_b64_tr_b16 a[186:187], v222 offset:30976\n"
  "v_mfma_f32_16x16x32_bf16 v[44:47], a[156:159], a[228:231], v[44:47]\n"
  "buffer_load_dwordx4 v133, s[36:39], s66 offen offset:1024 lds\n"
  "ds_read_b128 a[240:243], v128 offset:512\n"
  "v_mfma_f32_16x16x32_bf16 v[76:79], a[156:159], a[232:235], v[76:79]\n"
  "ds_read_b64_tr_b16 a[188:189], v225 offset:30720\n"
  "ds_read_b64_tr_b16 a[190:191], v225 offset:30976\n"
  "v_mfma_f32_16x16x32_bf16 v[16:19], a[160:163], a[224:227], v[16:19]\n"
  "ds_read_b128 a[244:247], v128 offset:2560\n"
  "v_mfma_f32_16x16x32_bf16 v[48:51], a[160:163], a[228:231], v[48:51]\n"
  "buffer_load_dwordx4 v134, s[36:39], s66 offen offset:2048 lds\n"
  "v_mfma_f32_16x16x32_bf16 v[80:83], a[160:163], a[232:235], v[80:83]\n"
  "ds_read_b128 a[248:251], v128 offset:4608\n"
  "ds_read_b64_tr_b16 a[192:193], v222 offset:31232\n"
  "v_mfma_f32_16x16x32_bf16 v[20:23], a[164:167], a[224:227], v[20:23]\n"
  "ds_read_b64_tr_b16 a[194:195], v222 offset:31488\n"
  "v_mfma_f32_16x16x32_bf16 v[52:55], a[164:167], a[228:231], v[52:55]\n"
  "v_mfma_f32_16x16x32_bf16 v[84:87], a[164:167], a[232:235], v[84:87]\n"
  "buffer_load_dwordx4 v135, s[36:39], s66 offen offset:3072 lds\n"
  "s_add_u32 m0, s64, 0x16000\n"
  "v_mfma_f32_16x16x32_bf16 v[24:27], a[168:171], a[224:227], v[24:27]\n"
  "s_lshl_b32 s67, s71, 7\n"
  "s_or_b32 s67, s67, 0x8000\n"
  "v_mfma_f32_16x16x32_bf16 v[56:59], a[168:171], a[228:231], v[56:59]\n"
  "ds_read_b64_tr_b16 a[196:197], v225 offset:31232\n"
  "ds_read_b64_tr_b16 a[198:199], v225 offset:31488\n"
  "v_mfma_f32_16x16x32_bf16 v[88:91], a[168:171], a[232:235], v[88:91]\n"
  "buffer_load_dwordx4 v132, s[36:39], s67 offen lds\n"
  "ds_read_b64_tr_b16 a[200:201], v222 offset:38912\n"
  "s_waitcnt lgkmcnt(3)\n"
  "v_mfma_f32_16x16x32_bf16 v[28:31], a[172:175], a[224:227], v[28:31]\n"
  "ds_read_b64_tr_b16 a[202:203], v222 offset:39168\n"
  "v_mfma_f32_16x16x32_bf16 v[60:63], a[172:175], a[228:231], v[60:63]\n"
  "v_mfma_f32_16x16x32_bf16 v[92:95], a[172:175], a[232:235], v[92:95]\n"
  "ds_read_b64_tr_b16 a[204:205], v225 offset:38912\n"
  "ds_read_b64_tr_b16 a[206:207], v225 offset:39168\n"
  "v_mfma_f32_16x16x32_bf16 v[96:99], a[176:179], a[236:239], v[96:99]\n"
  "buffer_load_dwordx4 v133, s[36:39], s67 offen offset:1024 lds\n"
  "s_add_u32 m0, s65, 0x20000\n"
  "v_mfma_f32_16x16x32_bf16 v[100:103], a[180:183], a[236:239], v[100:103]\n"
  "v_mfma_f32_16x16x32_bf16 v[0:3], a[184:187], a[240:243], v[0:3]\n"
  "ds_read_b64_tr_b16 a[208:209], v222 offset:39424\n"
  "ds_read_b64_tr_b16 a[210:211], v222 offset:39680\n"
  "v_mfma_f32_16x16x32_bf16 v[32:35], a[184:187], a[244:247], v[32:35]\n"
  "buffer_load_dwordx4 v234, s[44:47], 0 offen nt lds\n"
  "v_mfma_f32_16x16x32_bf16 v[64:67], a[184:187], a[248:251], v[64:67]\n"
  "v_mfma_f32_16x16x32_bf16 v[4:7], a[188:191], a[240:243], v[4:7]\n"
  "ds_read_b64_tr_b16 a[212:213], v225 offset:39424\n"
  "ds_read_b64_tr_b16 a[214:215], v225 offset:39680\n"
  "v_mfma_f32_16x16x32_bf16 v[36:39], a[188:191], a[244:247], v[36:39]\n"
  "buffer_load_dwordx4 v235, s[44:47], 0 offen offset:1024 nt lds\n"
  "v_mfma_f32_16x16x32_bf16 v[68:71], a[188:191], a[248:251], v[68:71]\n"
  "v_mfma_f32_16x16x32_bf16 v[8:11], a[192:195], a[240:243], v[8:11]\n"
  "ds_read_b64_tr_b16 a[216:217], v228 offset:30720\n"
  "ds_read_b64_tr_b16 a[218:219], v228 offset:30976\n"
  "v_mfma_f32_16x16x32_bf16 v[40:43], a[192:195], a[244:247], v[40:43]\n"
  "ds_read_b64_tr_b16 a[220:221], v231 offset:30720\n"
  "ds_read_b64_tr_b16 a[222:223], v231 offset:30976\n"
  "v_mfma_f32_16x16x32_bf16 v[72:75], a[192:195], a[248:251], v[72:75]\n"
  "ds_read_b128 a[252:255], v130 offset:25088\n"
  "buffer_load_dwordx4 v236, s[44:47], 0 offen offset:2048 nt lds\n"
  "s_waitcnt lgkmcnt(6)\n"
  "v_mfma_f32_16x16x32_bf16 v[12:15], a[196:199], a[240:243], v[12:15]\n"
  "v_mfma_f32_16x16x32_bf16 v[44:47], a[196:199], a[244:247], v[44:47]\n"
  "v_mfma_f32_16x16x32_bf16 v[76:79], a[196:199], a[248:251], v[76:79]\n"
  "buffer_load_dwordx4 v237, s[44:47], 0 offen offset:3072 nt lds\n"
  "v_mfma_f32_16x16x32_bf16 v[16:19], a[200:203], a[240:243], v[16:19]\n"
  "v_mfma_f32_16x16x32_bf16 v[48:51], a[200:203], a[244:247], v[48:51]\n"
  "v_mfma_f32_16x16x32_bf16 v[80:83], a[200:203], a[248:251], v[80:83]\n"
  "v_mfma_f32_16x16x32_bf16 v[20:23], a[204:207], a[240:243], v[20:23]\n"
  "v_mfma_f32_16x16x32_bf16 v[52:55], a[204:207], a[244:247], v[52:55]\n"
  "s_waitcnt vmcnt(14) lgkmcnt(0)\n"
  "s_barrier\n"
  "s_bfe_u32 s71, s70, 0x30018\n"
  "v_mfma_f32_16x16x32_bf16 v[84:87], a[204:207], a[248:251], v[84:87]\n"
  "ds_read_b64_tr_b16 a[144:145], v223 offset:4096\n"
  "s_add_u32 m0, s65, 0x6800\n"
  "ds_read_b64_tr_b16 a[146:147], v223 offset:4352\n"
  "v_mfma_f32_16x16x32_bf16 v[24:27], a[208:211], a[240:243], v[24:27]\n"
  "ds_read_b128 a[224:227], v128 offset:43008\n"
  "s_lshl_b32 s67, s71, 19\n"
  "buffer_load_dwordx4 v136, s[40:43], s67 offen lds\n"
  "v_mfma_f32_16x16x32_bf16 v[56:59], a[208:211], a[244:247], v[56:59]\n"
  "ds_read_b128 a[228:231], v128 offset:45056\n"
  "ds_read_b128 a[232:235], v128 offset:47104\n"
  "ds_read_b64_tr_b16 a[148:149], v226 offset:4096\n"
  "v_mfma_f32_16x16x32_bf16 v[88:91], a[208:211], a[248:251], v[88:91]\n"
  "ds_read_b64_tr_b16 a[150:151], v226 offset:4352\n"
  "buffer_load_dwordx4 v137, s[40:43], s67 offen offset:1024 lds\n"
  "v_mfma_f32_16x16x32_bf16 v[28:31], a[212:215], a[240:243], v[28:31]\n"
  "ds_read_b64_tr_b16 a[152:153], v223 offset:4608\n"
  "ds_read_b64_tr_b16 a[154:155], v223 offset:4864\n"
  "v_mfma_f32_16x16x32_bf16 v[60:63], a[212:215], a[244:247], v[60:63]\n"
  "ds_read_b64_tr_b16 a[156:157], v226 offset:4608\n"
  "ds_read_b64_tr_b16 a[158:159], v226 offset:4864\n"
  "v_mfma_f32_16x16x32_bf16 v[92:95], a[212:215], a[248:251], v[92:95]\n"
  "ds_read_b64_tr_b16 a[160:161], v223 offset:12288\n"
  "ds_read_b64_tr_b16 a[162:163], v223 offset:12544\n"
  "v_mfma_f32_16x16x32_bf16 v[96:99], a[216:219], a[252:255], v[96:99]\n"
  "ds_read_b64_tr_b16 a[164:165], v226 offset:12288\n"
  "ds_read_b64_tr_b16 a[166:167], v226 offset:12544\n"
  "v_mfma_f32_16x16x32_bf16 v[100:103], a[220:223], a[252:255], v[100:103]\n"
  "buffer_load_dwordx4 v138, s[40:43], s67 offen offset:2048 lds\n"
  "s_waitcnt lgkmcnt(8)\n"
  "v_mfma_f32_16x16x32_bf16 v[0:3], a[144:147], a[224:227], v[0:3]\n"
  "ds_read_b64_tr_b16 a[168:169], v223 offset:12800\n"
  "ds_read_b64_tr_b16 a[170:171], v223 offset:13056\n"
  "v_mfma_f32_16x16x32_bf16 v[32:35], a[144:147], a[228:231], v[32:35]\n"
  "v_mfma_f32_16x16x32_bf16 v[64:67], a[144:147], a[232:235], v[64:67]\n"
  "v_mfma_f32_16x16x32_bf16 v[4:7], a[148:151], a[224:227], v[4:7]\n"
  "buffer_load_dwordx4 v139, s[40:43], s67 offen offset:3072 lds\n"
  "ds_read_b64_tr_b16 a[172:173], v226 offset:12800\n"
  "v_mfma_f32_16x16x32_bf16 v[36:39], a[148:151], a[228:231], v[36:39]\n"
  "ds_read_b64_tr_b16 a[174:175], v226 offset:13056\n"
  "v_mfma_f32_16x16x32_bf16 v[68:71], a[148:151], a[232:235], v[68:71]\n"
  "s_waitcnt lgkmcnt(4)\n"
  "v_mfma_f32_16x16x32_bf16 v[8:11], a[152:155], a[224:227], v[8:11]\n"
  "ds_read_b64_tr_b16 a[176:177], v229 offset:4096\n"
  "ds_read_b64_tr_b16 a[178:179], v229 offset:4352\n"
  "v_mfma_f32_16x16x32_bf16 v[40:43], a[152:155], a[228:231], v[40:43]\n"
  "ds_read_b128 a[236:239], v131 offset:2048\n"
  "ds_read_b64_tr_b16 a[180:181], v232 offset:4096\n"
  "v_mfma_f32_16x16x32_bf16 v[72:75], a[152:155], a[232:235], v[72:75]\n"
  "s_cmp_lt_u32 s19, 2\n"
  "s_cbranch_scc0 L_o8\n"
  "s_add_u32 m0, s97, 0\n"
  "s_lshl_b32 s66, s71, 7\n"
  "s_add_u32 s66, s66, s96\n"
  "buffer_load_dwordx4 v132, s[36:39], s66 offen lds\n"
  "L_o8:\n"
  "v_mfma_f32_16x16x32_bf16 v[12:15], a[156:159], a[224:227], v[12:15]\n"
  "ds_read_b64_tr_b16 a[182:183], v232 offset:4352\n"
  "ds_read_b64_tr_b16 a[184:185], v223 offset:8192\n"
  "v_mfma_f32_16x16x32_bf16 v[44:47], a[156:159], a[228:231], v[44:47]\n"
  "s_add_u32 m0, s64, 0\n"
  "s_lshl_b32 s67, s71, 7\n"
  "v_mfma_f32_16x16x32_bf16 v[76:79], a[156:159], a[232:235], v[76:79]\n"
  "ds_read_b64_tr_b16 a[186:187], v223 offset:8448\n"
  "ds_read_b128 a[240:243], v128 offset:43520\n"
  "v_mfma_f32_16x16x32_bf16 v[16:19], a[160:163], a[224:227], v[16:19]\n"
  "ds_read_b64_tr_b16 a[188:189], v226 offset:8192\n"
  "ds_read_b64_tr_b16 a[190:191], v226 offset:8448\n"
  "v_mfma_f32_16x16x32_bf16 v[48:51], a[160:163], a[228:231], v[48:51]\n"
  "buffer_load_dwordx4 v132, s[36:39], s67 offen lds\n"
  "ds_read_b128 a[244:247], v128 offset:45568\n"
  "v_mfma_f32_16x16x32_bf16 v[80:83], a[160:163], a[232:235], v[80:83]\n"
  "ds_read_b64_tr_b16 a[192:193], v223 offset:8704\n"
  "ds_read_b64_tr_b16 a[194:195], v223 offset:8960\n"
  "v_mfma_f32_16x16x32_bf16 v[20:23], a[164:167], a[224:227], v[20:23]\n"
  "ds_read_b128 a[248:251], v128 offset:47616\n"
  "v_mfma_f32_16x16x32_bf16 v[52:55], a[164:167], a[228:231], v[52:55]\n"
  "v_mfma_f32_16x16x32_bf16 v[84:87], a[164:167], a[232:235], v[84:87]\n"
  "buffer_load_dwordx4 v133, s[36:39], s67 offen offset:1024 lds\n"
  "ds_read_b64_tr_b16 a[196:197], v226 offset:8704\n"
  "s_waitcnt lgkmcnt(6)\n"
  "v_mfma_f32_16x16x32_bf16 v[24:27], a[168:171], a[224:227], v[24:27]\n"
  "ds_read_b64_tr_b16 a[198:199], v226 offset:8960\n"
  "v_mfma_f32_16x16x32_bf16 v[56:59], a[168:171], a[228:231], v[56:59]\n"
  "v_mfma_f32_16x16x32_bf16 v[88:91], a[168:171], a[232:235], v[88:91]\n"
  "ds_read_b64_tr_b16 a[200:201], v223 offset:16384\n"
  "ds_read_b64_tr_b16 a[202:203], v223 offset:16640\n"
  "v_mfma_f32_16x16x32_bf16 v[28:31], a[172:175], a[224:227], v[28:31]\n"
  "v_mfma_f32_16x16x32_bf16 v[60:63], a[172:175], a[228:231], v[60:63]\n"
  "buffer_load_dwordx4 v134, s[36:39], s67 offen offset:2048 lds\n"
  "v_mfma_f32_16x16x32_bf16 v[92:95], a[172:175], a[232:235], v[92:95]\n"
  "ds_read_b64_tr_b16 a[204:205], v226 offset:16384\n"
  "ds_read_b64_tr_b16 a[206:207], v226 offset:16640\n"
  "v_mfma_f32_16x16x32_bf16 v[96:99], a[176:179], a[236:239], v[96:99]\n"
  "v_mfma_f32_16x16x32_bf16 v[100:103], a[180:183], a[236:239], v[100:103]\n"
  "v_mfma_f32_16x16x32_bf16 v[0:3], a[184:187], a[240:243], v[0:3]\n"
  "ds_read_b64_tr_b16 a[208:209], v223 offset:16896\n"
  "ds_read_b64_tr_b16 a[210:211], v223 offset:17152\n"
  "s_waitcnt lgkmcnt(6)\n"
  "v_mfma_f32_16x16x32_bf16 v[32:35], a[184:187], a[244:247], v[32:35]\n"
  "buffer_load_dwordx4 v135, s[36:39], s67 offen offset:3072 lds\n"
  "v_mfma_f32_16x16x32_bf16 v[64:67], a[184:187], a[248:251], v[64:67]\n"
  "s_add_u32 m0, s64, 0x1000\n"
  "s_lshl_b32 s66, s71, 7\n"
  "v_mfma_f32_16x16x32_bf16 v[4:7], a[188:191], a[240:243], v[4:7]\n"
  "ds_read_b64_tr_b16 a[212:213], v226 offset:16896\n"
  "ds_read_b64_tr_b16 a[214:215], v226 offset:17152\n"
  "v_mfma_f32_16x16x32_bf16 v[36:39], a[188:191], a[244:247], v[36:39]\n"
  "s_or_b32 s66, s66, 0x8000\n"
  "v_mfma_f32_16x16x32_bf16 v[68:71], a[188:191], a[248:251], v[68:71]\n"
  "buffer_load_dwordx4 v132, s[36:39], s66 offen lds\n"
  "v_mfma_f32_16x16x32_bf16 v[8:11], a[192:195], a[240:243], v[8:11]\n"
  "ds_read_b64_tr_b16 a[216:217], v229 offset:8192\n"
  "ds_read_b64_tr_b16 a[218:219], v229 offset:8448\n"
  "v_mfma_f32_16x16x32_bf16 v[40:43], a[192:195], a[244:247], v[40:43]\n"
  "ds_read_b64_tr_b16 a[220:221], v232 offset:8192\n"
  "ds_read_b64_tr_b16 a[222:223], v232 offset:8448\n"
  "v_mfma_f32_16x16x32_bf16 v[72:75], a[192:195], a[248:251], v[72:75]\n"
  "ds_read_b128 a[252:255], v131 offset:2560\n"
  "v_mfma_f32_16x16x32_bf16 v[12:15], a[196:199], a[240:243], v[12:15]\n"
  "v_mfma_f32_16x16x32_bf16 v[44:47], a[196:199], a[244:247], v[44:47]\n"
  "buffer_load_dwordx4 v133, s[36:39], s66 offen offset:1024 lds\n"
  "v_mfma_f32_16x16x32_bf16 v[76:79], a[196:199], a[248:251], v[76:79]\n"
  "s_waitcnt lgkmcnt(5)\n"
  "v_mfma_f32_16x16x32_bf16 v[16:19], a[200:203], a[240:243], v[16:19]\n"
  "v_mfma_f32_16x16x32_bf16 v[48:51], a[200:203], a[244:247], v[48:51]\n"
  "v_mfma_f32_16x16x32_bf16 v[80:83], a[200:203], a[248:251], v[80:83]\n"
  "v_mfma_f32_16x16x32_bf16 v[20:23], a[204:207], a[240:243], v[20:23]\n"
  "v_mfma_f32_16x16x32_bf16 v[52:55], a[204:207], a[244:247], v[52:55]\n"
  "s_waitcnt vmcnt(14) lgkmcnt(0)\n"
  "s_barrier\n"
  "s_bfe_u32 s71, s70, 0x3001c\n"
  "v_mfma_f32_16x16x32_bf16 v[84:87], a[204:207], a[248:251], v[84:87]\n"
  "ds_read_b64_tr_b16 a[144:145], v223 offset:47104\n"
  "s_add_u32 m0, s65, 0x11000\n"
  "ds_read_b64_tr_b16 a[146:147], v223 offset:47360\n"
  "v_mfma_f32_16x16x32_bf16 v[24:27], a[208:211], a[240:243], v[24:27]\n"
  "ds_read_b128 a[224:227], v129 offset:20480\n"
  "s_lshl_b32 s67, s71, 19\n"
  "buffer_load_dwordx4 v136, s[40:43], s67 offen lds\n"
  "v_mfma_f32_16x16x32_bf16 v[56:59], a[208:211], a[244:247], v[56:59]\n"
  "ds_read_b128 a[228:231], v129 offset:22528\n"
  "ds_read_b128 a[232:235], v129 offset:24576\n"
  "ds_read_b64_tr_b16 a[148:149], v226 offset:47104\n"
  "v_mfma_f32_16x16x32_bf16 v[88:91], a[208:211], a[248:251], v[88:91]\n"
  "ds_read_b64_tr_b16 a[150:151], v226 offset:47360\n"
  "buffer_load_dwordx4 v137, s[40:43], s67 offen offset:1024 lds\n"
  "v_mfma_f32_16x16x32_bf16 v[28:31], a[212:215], a[240:243], v[28:31]\n"
  "ds_read_b64_tr_b16 a[152:153], v223 offset:47616\n"
  "ds_read_b64_tr_b16 a[154:155], v223 offset:47872\n"
  "v_mfma_f32_16x16x32_bf16 v[60:63], a[212:215], a[244:247], v[60:63]\n"
  "buffer_load_dwordx4 v138, s[40:43], s67 offen offset:2048 lds\n"
  "ds_read_b64_tr_b16 a[156:157], v226 offset:47616\n"
  "v_mfma_f32_16x16x32_bf16 v[92:95], a[212:215], a[248:251], v[92:95]\n"
  "ds_read_b64_tr_b16 a[158:159], v226 offset:47872\n"
  "ds_read_b64_tr_b16 a[160:161], v223 offset:55296\n"
  "v_mfma_f32_16x16x32_bf16 v[96:99], a[216:219], a[252:255], v[96:99]\n"
  "ds_read_b64_tr_b16 a[162:163], v223 offset:55552\n"
  "ds_read_b64_tr_b16 a[164:165], v226 offset:55296\n"
  "v_mfma_f32_16x16x32_bf16 v[100:103], a[220:223], a[252:255], v[100:103]\n"
  "ds_read_b64_tr_b16 a[166:167], v226 offset:55552\n"
  "s_waitcnt lgkmcnt(8)\n"
  "v_mfma_f32_16x16x32_bf16 v[0:3], a[144:147], a[224:227], v[0:3]\n"
  "buffer_load_dwordx4 v139, s[40:43], s67 offen offset:3072 lds\n"
  "ds_read_b64_tr_b16 a[168:169], v223 offset:55808\n"
  "v_mfma_f32_16x16x32_bf16 v[32:35], a[144:147], a[228:231], v[32:35]\n"
  "ds_read_b64_tr_b16 a[170:171], v223 offset:56064\n"
  "v_mfma_f32_16x16x32_bf16 v[64:67], a[144:147], a[232:235], v[64:67]\n"
  "v_mfma_f32_16x16x32_bf16 v[4:7], a[148:151], a[224:227], v[4:7]\n"
  "s_cmp_lt_u32 s19, 2\n"
  "s_cbranch_scc0 L_o9\n"
  "s_add_u32 m0, s97, 0xa800\n"
  "s_lshl_b32 s66, s71, 7\n"
  "s_add_u32 s66, s66, s96\n"
  "buffer_load_dwordx4 v132, s[36:39], s66 offen lds\n"
  "L_o9:\n"
  "v_mfma_f32_16x16x32_bf16 v[36:39], a[148:151], a[228:231], v[36:39]\n"
  "s_add_u32 m0, s64, 0xa800\n"
  "s_lshl_b32 s67, s71, 7\n"
  "v_mfma_f32_16x16x32_bf16 v[68:71], a[148:151], a[232:235], v[68:71]\n"
  "ds_read_b64_tr_b16 a[172:173], v226 offset:55808\n"
  "ds_read_b64_tr_b16 a[174:175], v226 offset:56064\n"
  "s_waitcnt lgkmcnt(2)\n"
  "v_mfma_f32_16x16x32_bf16 v[8:11], a[152:155], a[224:227], v[8:11]\n"
  "ds_read_b64_tr_b16 a[176:177], v229 offset:47104\n"
  "ds_read_b64_tr_b16 a[178:179], v229 offset:47360\n"
  "v_mfma_f32_16x16x32_bf16 v[40:43], a[152:155], a[228:231], v[40:43]\n"
  "buffer_load_dwordx4 v132, s[36:39], s67 offen lds\n"
  "ds_read_b128 a[236:239], v131 offset:45056\n"
  "v_mfma_f32_16x16x32_bf16 v[72:75], a[152:155], a[232:235], v[72:75]\n"
  "ds_read_b64_tr_b16 a[180:181], v232 offset:47104\n"
  "ds_read_b64_tr_b16 a[182:183], v232 offset:47360\n"
  "v_mfma_f32_16x16x32_bf16 v[12:15], a[156:159], a[224:227], v[12:15]\n"
  "ds_read_b64_tr_b16 a[184:185], v223 offset:51200\n"
  "ds_read_b64_tr_b16 a[186:187], v223 offset:51456\n"
  "v_mfma_f32_16x16x32_bf16 v[44:47], a[156:159], a[228:231], v[44:47]\n"
  "buffer_load_dwordx4 v133, s[36:39], s67 offen offset:1024 lds\n"
  "ds_read_b128 a[240:243], v129 offset:20992\n"
  "v_mfma_f32_16x16x32_bf16 v[76:79], a[156:159], a[232:235], v[76:79]\n"
  "ds_read_b64_tr_b16 a[188:189], v226 offset:51200\n"
  "ds_read_b64_tr_b16 a[190:191], v226 offset:51456\n"
  "v_mfma_f32_16x16x32_bf16 v[16:19], a[160:163], a[224:227], v[16:19]\n"
  "ds_read_b128 a[244:247], v129 offset:23040\n"
  "v_mfma_f32_16x16x32_bf16 v[48:51], a[160:163], a[228:231], v[48:51]\n"
  "buffer_load_dwordx4 v134, s[36:39], s67 offen offset:2048 lds\n"
  "v_mfma_f32_16x16x32_bf16 v[80:83], a[160:163], a[232:235], v[80:83]\n"
  "ds_read_b128 a[248:251], v129 offset:25088\n"
  "ds_read_b64_tr_b16 a[192:193], v223 offset:51712\n"
  "v_mfma_f32_16x16x32_bf16 v[20:23], a[164:167], a[224:227], v[20:23]\n"
  "ds_read_b64_tr_b16 a[194:195], v223 offset:51968\n"
  "v_mfma_f32_16x16x32_bf16 v[52:55], a[164:167], a[228:231], v[52:55]\n"
  "v_mfma_f32_16x16x32_bf16 v[84:87], a[164:167], a[232:235], v[84:87]\n"
  "buffer_load_dwordx4 v135, s[36:39], s67 offen offset:3072 lds\n"
  "s_add_u32 m0, s64, 0xb800\n"
  "v_mfma_f32_16x16x32_bf16 v[24:27], a[168:171], a[224:227], v[24:27]\n"
  "s_lshl_b32 s66, s71, 7\n"
  "s_or_b32 s66, s66, 0x8000\n"
  "v_mfma_f32_16x16x32_bf16 v[56:59], a[168:171], a[228:231], v[56:59]\n"
  "ds_read_b64_tr_b16 a[196:197], v226 offset:51712\n"
  "ds_read_b64_tr_b16 a[198:199], v226 offset:51968\n"
  "v_mfma_f32_16x16x32_bf16 v[88:91], a[168:171], a[232:235], v[88:91]\n"
  "buffer_load_dwordx4 v132, s[36:39], s66 offen lds\n"
  "ds_read_b64_tr_b16 a[200:201], v223 offset:59392\n"
  "s_waitcnt lgkmcnt(3)\n"
  "v_mfma_f32_16x16x32_bf16 v[28:31], a[172:175], a[224:227], v[28:31]\n"
  "ds_read_b64_tr_b16 a[202:203], v223 offset:59648\n"
  "v_mfma_f32_16x16x32_bf16 v[60:63], a[172:175], a[228:231], v[60:63]\n"
  "v_mfma_f32_16x16x32_bf16 v[92:95], a[172:175], a[232:235], v[92:95]\n"
  "ds_read_b64_tr_b16 a[204:205], v226 offset:59392\n"
  "ds_read_b64_tr_b16 a[206:207], v226 offset:59648\n"
  "L_st1:\n"
  "s_cbranch_execz L_st5\n"
  "v_mfma_f32_16x16x32_bf16 v[96:99], a[176:179], a[236:239], v[96:99]\n"
  "buffer_load_dwordx4 v133, s[36:39], s66 offen offset:1024 lds\n"
  "s_add_u32 m0, s65, 0x24000\n"
  "v_mfma_f32_16x16x32_bf16 v[100:103], a[180:183], a[236:239], v[100:103]\n"
  "s_mov_b32 s67, 0x4000\n"
  "v_mfma_f32_16x16x32_bf16 v[0:3], a[184:187], a[240:243], v[0:3]\n"
  "ds_read_b64_tr_b16 a[208:209], v223 offset:59904\n"
  "ds_read_b64_tr_b16 a[210:211], v223 offset:60160\n"
  "v_mfma_f32_16x16x32_bf16 v[32:35], a[184:187], a[244:247], v[32:35]\n"
  "buffer_load_dwordx4 v234, s[44:47], s67 offen nt lds\n"
  "v_mfma_f32_16x16x32_bf16 v[64:67], a[184:187], a[248:251], v[64:67]\n"
  "v_mfma_f32_16x16x32_bf16 v[4:7], a[188:191], a[240:243], v[4:7]\n"
  "ds_read_b64_tr_b16 a[212:213], v226 offset:59904\n"
  "ds_read_b64_tr_b16 a[214:215], v226 offset:60160\n"
  "v_mfma_f32_16x16x32_bf16 v[36:39], a[188:191], a[244:247], v[36:39]\n"
  "buffer_load_dwordx4 v235, s[44:47], s67 offen offset:1024 nt lds\n"
  "v_mfma_f32_16x16x32_bf16 v[68:71], a[188:191], a[248:251], v[68:71]\n"
  "v_mfma_f32_16x16x32_bf16 v[8:11], a[192:195], a[240:243], v[8:11]\n"
  "ds_read_b64_tr_b16 a[216:217], v229 offset:51200\n"
  "ds_read_b64_tr_b16 a[218:219], v229 offset:51456\n"
  "v_mfma_f32_16x16x32_bf16 v[40:43], a[192:195], a[244:247], v[40:43]\n"
  "ds_read_b64_tr_b16 a[220:221], v232 offset:51200\n"
  "ds_read_b64_tr_b16 a[222:223], v232 offset:51456\n"
  "v_mfma_f32_16x16x32_bf16 v[72:75], a[192:195], a[248:251], v[72:75]\n"
  "ds_read_b128 a[252:255], v131 offset:45568\n"
  "buffer_load_dwordx4 v236, s[44:47], s67 offen offset:2048 nt lds\n"
  "s_waitcnt lgkmcnt(6)\n"
  "v_mfma_f32_16x16x32_bf16 v[12:15], a[196:199], a[240:243], v[12:15]\n"
  "v_mfma_f32_16x16x32_bf16 v[44:47], a[196:199], a[244:247], v[44:47]\n"
  "v_mfma_f32_16x16x32_bf16 v[76:79], a[196:199], a[248:251], v[76:79]\n"
  "buffer_load_dwordx4 v237, s[44:47], s67 offen offset:3072 nt lds\n"
  "v_mfma_f32_16x16x32_bf16 v[16:19], a[200:203], a[240:243], v[16:19]\n"
  "v_mfma_f32_16x16x32_bf16 v[48:51], a[200:203], a[244:247], v[48:51]\n"
  "v_mfma_f32_16x16x32_bf16 v[80:83], a[200:203], a[248:251], v[80:83]\n"
  "v_mfma_f32_16x16x32_bf16 v[20:23], a[204:207], a[240:243], v[20:23]\n"
  "v_mfma_f32_16x16x32_bf16 v[52:55], a[204:207], a[244:247], v[52:55]\n"
  "s_waitcnt vmcnt(14) lgkmcnt(0)\n"
  "s_barrier\n"
  "ds_read_b64_tr_b16 a[144:145], v222 offset:26624\n"
  "v_mfma_f32_16x16x32_bf16 v[84:87], a[204:207], a[248:251], v[84:87]\n"
  "s_add_u32 m0, s65, 0x18000\n"
  "ds_read_b64_tr_b16 a[146:147], v222 offset:26880\n"
  "ds_read_b128 a[224:227], v128\n"
  "v_mfma_f32_16x16x32_bf16 v[24:27], a[208:211], a[240:243], v[24:27]\n"
  "s_mov_b32 s66, 0x8000\n"
  "buffer_load_dwordx4 v234, s[44:47], s66 offen nt lds\n"
  "ds_read_b128 a[228:231], v128 offset:2048\n"
  "v_mfma_f32_16x16x32_bf16 v[56:59], a[208:211], a[244:247], v[56:59]\n"
  "ds_read_b128 a[232:235], v128 offset:4096\n"
  "ds_read_b64_tr_b16 a[148:149], v225 offset:26624\n"
  "v_mfma_f32_16x16x32_bf16 v[88:91], a[208:211], a[248:251], v[88:91]\n"
  "ds_read_b64_tr_b16 a[150:151], v225 offset:26880\n"
  "ds_read_b64_tr_b16 a[152:153], v222 offset:27136\n"
  "v_mfma_f32_16x16x32_bf16 v[28:31], a[212:215], a[240:243], v[28:31]\n"
  "ds_read_b64_tr_b16 a[154:155], v222 offset:27392\n"
  "ds_read_b64_tr_b16 a[156:157], v225 offset:27136\n"
  "v_mfma_f32_16x16x32_bf16 v[60:63], a[212:215], a[244:247], v[60:63]\n"
  "ds_read_b64_tr_b16 a[158:159], v225 offset:27392\n"
  "ds_read_b64_tr_b16 a[160:161], v222 offset:34816\n"
  "v_mfma_f32_16x16x32_bf16 v[92:95], a[212:215], a[248:251], v[92:95]\n"
  "ds_read_b64_tr_b16 a[162:163], v222 offset:35072\n"
  "ds_read_b64_tr_b16 a[164:165], v225 offset:34816\n"
  "v_mfma_f32_16x16x32_bf16 v[96:99], a[216:219], a[252:255], v[96:99]\n"
  "ds_read_b64_tr_b16 a[166:167], v225 offset:35072\n"
  "v_mfma_f32_16x16x32_bf16 v[100:103], a[220:223], a[252:255], v[100:103]\n"
  "s_waitcnt lgkmcnt(8)\n"
  "v_mfma_f32_16x16x32_bf16 v[0:3], a[144:147], a[224:227], v[0:3]\n"
  "ds_read_b64_tr_b16 a[168:169], v222 offset:35328\n"
  "ds_read_b64_tr_b16 a[170:171], v222 offset:35584\n"
  "v_mfma_f32_16x16x32_bf16 v[32:35], a[144:147], a[228:231], v[32:35]\n"
  "v_mfma_f32_16x16x32_bf16 v[64:67], a[144:147], a[232:235], v[64:67]\n"
  "v_mfma_f32_16x16x32_bf16 v[4:7], a[148:151], a[224:227], v[4:7]\n"
  "buffer_load_dwordx4 v235, s[44:47], s66 offen offset:1024 nt lds\n"
  "ds_read_b64_tr_b16 a[172:173], v225 offset:35328\n"
  "v_mfma_f32_16x16x32_bf16 v[36:39], a[148:151], a[228:231], v[36:39]\n"
  "ds_read_b64_tr_b16 a[174:175], v225 offset:35584\n"
  "v_mfma_f32_16x16x32_bf16 v[68:71], a[148:151], a[232:235], v[68:71]\n"
  "s_waitcnt lgkmcnt(4)\n"
  "v_mfma_f32_16x16x32_bf16 v[8:11], a[152:155], a[224:227], v[8:11]\n"
  "ds_read_b64_tr_b16 a[176:177], v228 offset:26624\n"
  "ds_read_b64_tr_b16 a[178:179], v228 offset:26880\n"
  "v_mfma_f32_16x16x32_bf16 v[40:43], a[152:155], a[228:231], v[40:43]\n"
  "ds_read_b128 a[236:239], v130 offset:24576\n"
  "ds_read_b64_tr_b16 a[180:181], v231 offset:26624\n"
  "v_mfma_f32_16x16x32_bf16 v[72:75], a[152:155], a[232:235], v[72:75]\n"
  "ds_read_b64_tr_b16 a[182:183], v231 offset:26880\n"
  "ds_read_b64_tr_b16 a[184:185], v222 offset:30720\n"
  "v_mfma_f32_16x16x32_bf16 v[12:15], a[156:159], a[224:227], v[12:15]\n"
  "ds_read_b64_tr_b16 a[186:187], v222 offset:30976\n"
  "ds_read_b128 a[240:243], v128 offset:512\n"
  "v_mfma_f32_16x16x32_bf16 v[44:47], a[156:159], a[228:231], v[44:47]\n"
  "v_mfma_f32_16x16x32_bf16 v[76:79], a[156:159], a[232:235], v[76:79]\n"
  "ds_read_b128 a[244:247], v128 offset:2560\n"
  "ds_read_b64_tr_b16 a[188:189], v225 offset:30720\n"
  "v_mfma_f32_16x16x32_bf16 v[16:19], a[160:163], a[224:227], v[16:19]\n"
  "ds_read_b64_tr_b16 a[190:191], v225 offset:30976\n"
  "v_mfma_f32_16x16x32_bf16 v[48:51], a[160:163], a[228:231], v[48:51]\n"
  "ds_read_b128 a[248:251], v128 offset:4608\n"
  "v_mfma_f32_16x16x32_bf16 v[80:83], a[160:163], a[232:235], v[80:83]\n"
  "ds_read_b64_tr_b16 a[192:193], v222 offset:31232\n"
  "ds_read_b64_tr_b16 a[194:195], v222 offset:31488\n"
  "v_mfma_f32_16x16x32_bf16 v[20:23], a[164:167], a[224:227], v[20:23]\n"
  "v_mfma_f32_16x16x32_bf16 v[52:55], a[164:167], a[228:231], v[52:55]\n"
  "buffer_load_dwordx4 v236, s[44:47], s66 offen offset:2048 nt lds\n"
  "v_mfma_f32_16x16x32_bf16 v[84:87], a[164:167], a[232:235], v[84:87]\n"
  "ds_read_b64_tr_b16 a[196:197], v225 offset:31232\n"
  "ds_read_b64_tr_b16 a[198:199], v225 offset:31488\n"
  "s_waitcnt lgkmcnt(6)\n"
  "v_mfma_f32_16x16x32_bf16 v[24:27], a[168:171], a[224:227], v[24:27]\n"
  "v_mfma_f32_16x16x32_bf16 v[56:59], a[168:171], a[228:231], v[56:59]\n"
  "v_mfma_f32_16x16x32_bf16 v[88:91], a[168:171], a[232:235], v[88:91]\n"
  "ds_read_b64_tr_b16 a[200:201], v222 offset:38912\n"
  "ds_read_b64_tr_b16 a[202:203], v222 offset:39168\n"
  "v_mfma_f32_16x16x32_bf16 v[28:31], a[172:175], a[224:227], v[28:31]\n"
  "v_mfma_f32_16x16x32_bf16 v[60:63], a[172:175], a[228:231], v[60:63]\n"
  "v_mfma_f32_16x16x32_bf16 v[92:95], a[172:175], a[232:235], v[92:95]\n"
  "ds_read_b64_tr_b16 a[204:205], v225 offset:38912\n"
  "ds_read_b64_tr_b16 a[206:207], v225 offset:39168\n"
  "v_mfma_f32_16x16x32_bf16 v[96:99], a[176:179], a[236:239], v[96:99]\n"
  "v_mfma_f32_16x16x32_bf16 v[100:103], a[180:183], a[236:239], v[100:103]\n"
  "v_mfma_f32_16x16x32_bf16 v[0:3], a[184:187], a[240:243], v[0:3]\n"
  "ds_read_b64_tr_b16 a[208:209], v222 offset:39424\n"
  "ds_read_b64_tr_b16 a[210:211], v222 offset:39680\n"
  "v_mfma_f32_16x16x32_bf16 v[32:35], a[184:187], a[244:247], v[32:35]\n"
  "s_waitcnt lgkmcnt(6)\n"
  "v_mfma_f32_16x16x32_bf16 v[64:67], a[184:187], a[248:251], v[64:67]\n"
  "v_mfma_f32_16x16x32_bf16 v[4:7], a[188:191], a[240:243], v[4:7]\n"
  "buffer_load_dwordx4 v237, s[44:47], s66 offen offset:3072 nt lds\n"
  "ds_read_b64_tr_b16 a[212:213], v225 offset:39424\n"
  "v_mfma_f32_16x16x32_bf16 v[36:39], a[188:191], a[244:247], v[36:39]\n"
  "ds_read_b64_tr_b16 a[214:215], v225 offset:39680\n"
  "v_mfma_f32_16x16x32_bf16 v[68:71], a[188:191], a[248:251], v[68:71]\n"
  "v_mfma_f32_16x16x32_bf16 v[8:11], a[192:195], a[240:243], v[8:11]\n"
  "ds_read_b64_tr_b16 a[216:217], v228 offset:30720\n"
  "ds_read_b64_tr_b16 a[218:219], v228 offset:30976\n"
  "v_mfma_f32_16x16x32_bf16 v[40:43], a[192:195], a[244:247], v[40:43]\n"
  "ds_read_b64_tr_b16 a[220:221], v231 offset:30720\n"
  "ds_read_b64_tr_b16 a[222:223], v231 offset:30976\n"
  "v_mfma_f32_16x16x32_bf16 v[72:75], a[192:195], a[248:251], v[72:75]\n"
  "ds_read_b128 a[252:255], v130 offset:25088\n"
  "v_mfma_f32_16x16x32_bf16 v[12:15], a[196:199], a[240:243], v[12:15]\n"
  "v_mfma_f32_16x16x32_bf16 v[44:47], a[196:199], a[244:247], v[44:47]\n"
  "v_mfma_f32_16x16x32_bf16 v[76:79], a[196:199], a[248:251], v[76:79]\n"
  "s_waitcnt lgkmcnt(6)\n"
  "v_mfma_f32_16x16x32_bf16 v[16:19], a[200:203], a[240:243], v[16:19]\n"
  "v_mfma_f32_16x16x32_bf16 v[48:51], a[200:203], a[244:247], v[48:51]\n"
  "v_mfma_f32_16x16x32_bf16 v[80:83], a[200:203], a[248:251], v[80:83]\n"
  "v_mfma_f32_16x16x32_bf16 v[20:23], a[204:207], a[240:243], v[20:23]\n"
  "v_mfma_f32_16x16x32_bf16 v[52:55], a[204:207], a[244:247], v[52:55]\n"
  "s_waitcnt vmcnt(8) lgkmcnt(0)\n"
  "s_barrier\n"
  "s_add_u32 m0, s65, 0\n"
  "v_mfma_f32_16x16x32_bf16 v[84:87], a[204:207], a[248:251], v[84:87]\n"
  "buffer_load_dwordx4 v238, s[48:51], 0 offen nt lds\n"
  "ds_read_b64_tr_b16 a[144:145], v229 offset:4096\n"
  "ds_read_b64_tr_b16 a[146:147], v229 offset:4352\n"
  "v_mfma_f32_16x16x32_bf16 v[24:27], a[208:211], a[240:243], v[24:27]\n"
  "buffer_load_dwordx4 v239, s[48:51], 0 offen offset:1024 nt lds\n"
  "ds_read_b128 a[224:227], v131 offset:2048\n"
  "ds_read_b64_tr_b16 a[148:149], v229 offset:8192\n"
  "v_mfma_f32_16x16x32_bf16 v[56:59], a[208:211], a[244:247], v[56:59]\n"
  "buffer_load_dwordx4 v238, s[48:51], 0 offen offset:2048 nt lds\n"
  "ds_read_b64_tr_b16 a[150:151], v229 offset:8448\n"
  "ds_read_b128 a[228:231], v131 offset:2560\n"
  "v_mfma_f32_16x16x32_bf16 v[88:91], a[208:211], a[248:251], v[88:91]\n"
  "buffer_load_dwordx4 v239, s[48:51], 0 offen offset:3072 nt lds\n"
  "ds_read_b64_tr_b16 a[152:153], v232 offset:4096\n"
  "ds_read_b64_tr_b16 a[154:155], v232 offset:4352\n"
  "v_mfma_f32_16x16x32_bf16 v[28:31], a[212:215], a[240:243], v[28:31]\n"
  "ds_read_b64_tr_b16 a[156:157], v232 offset:8192\n"
  "ds_read_b64_tr_b16 a[158:159], v232 offset:8448\n"
  "ds_read_b64_tr_b16 a[160:161], v223 offset:4096\n"
  "v_mfma_f32_16x16x32_bf16 v[60:63], a[212:215], a[244:247], v[60:63]\n"
  "s_add_u32 m0, s65, 0x4000\n"
  "ds_read_b64_tr_b16 a[162:163], v223 offset:4352\n"
  "ds_read_b128 a[232:235], v128 offset:43008\n"
  "v_mfma_f32_16x16x32_bf16 v[92:95], a[212:215], a[248:251], v[92:95]\n"
  "s_mov_b32 s66, 0x4000\n"
  "ds_read_b64_tr_b16 a[164:165], v223 offset:8192\n"
  "buffer_load_dwordx4 v238, s[48:51], s66 offen nt lds\n"
  "v_mfma_f32_16x16x32_bf16 v[96:99], a[216:219], a[252:255], v[96:99]\n"
  "ds_read_b64_tr_b16 a[166:167], v223 offset:8448\n"
  "ds_read_b128 a[236:239], v128 offset:43520\n"
  "ds_read_b64_tr_b16 a[168:169], v226 offset:4096\n"
  "v_mfma_f32_16x16x32_bf16 v[100:103], a[220:223], a[252:255], v[100:103]\n"
  "ds_read_b64_tr_b16 a[170:171], v226 offset:4352\n"
  "ds_read_b64_tr_b16 a[172:173], v226 offset:8192\n"
  "ds_read_b64_tr_b16 a[174:175], v226 offset:8448\n"
  "s_waitcnt lgkmcnt(8)\n"
  "v_mfma_f32_16x16x32_bf16 v[96:99], a[144:147], a[224:227], v[96:99]\n"
  "ds_read_b64_tr_b16 a[176:177], v223 offset:4608\n"
  "ds_read_b64_tr_b16 a[178:179], v223 offset:4864\n"
  "ds_read_b64_tr_b16 a[180:181], v223 offset:8704\n"
  "v_mfma_f32_16x16x32_bf16 v[96:99], a[148:151], a[228:231], v[96:99]\n"
  "ds_read_b64_tr_b16 a[182:183], v223 offset:8960\n"
  "ds_read_b64_tr_b16 a[184:185], v226 offset:4608\n"
  "ds_read_b64_tr_b16 a[186:187], v226 offset:4864\n"
  "v_mfma_f32_16x16x32_bf16 v[100:103], a[152:155], a[224:227], v[100:103]\n"
  "ds_read_b64_tr_b16 a[188:189], v226 offset:8704\n"
  "ds_read_b64_tr_b16 a[190:191], v226 offset:8960\n"
  "ds_read_b64_tr_b16 a[192:193], v223 offset:12288\n"
  "v_mfma_f32_16x16x32_bf16 v[100:103], a[156:159], a[228:231], v[100:103]\n"
  "ds_read_b64_tr_b16 a[194:195], v223 offset:12544\n"
  "ds_read_b64_tr_b16 a[196:197], v223 offset:16384\n"
  "s_waitcnt lgkmcnt(10)\n"
  "v_mfma_f32_16x16x32_bf16 v[0:3], a[160:163], a[232:235], v[0:3]\n"
  "ds_read_b64_tr_b16 a[198:199], v223 offset:16640\n"
  "ds_read_b64_tr_b16 a[200:201], v226 offset:12288\n"
  "v_mfma_f32_16x16x32_bf16 v[0:3], a[164:167], a[236:239], v[0:3]\n"
  "ds_read_b64_tr_b16 a[202:203], v226 offset:12544\n"
  "ds_read_b64_tr_b16 a[204:205], v226 offset:16384\n"
  "v_mfma_f32_16x16x32_bf16 v[4:7], a[168:171], a[232:235], v[4:7]\n"
  "v_mul_f32 v96, s34, v96\n"
  "v_mul_f32 v97, s34, v97\n"
  "v_mul_f32 v98, s34, v98\n"
  "v_mfma_f32_16x16x32_bf16 v[4:7], a[172:175], a[236:239], v[4:7]\n"
  "v_mul_f32 v99, s34, v99\n"
  "v_mul_f32 v100, s34, v100\n"
  "v_mul_f32 v101, s34, v101\n"
  "s_waitcnt lgkmcnt(4)\n"
  "v_mfma_f32_16x16x32_bf16 v[8:11], a[176:179], a[232:235], v[8:11]\n"
  "ds_read_b64_tr_b16 a[206:207], v226 offset:16640\n"
  "v_mul_f32 v102, s34, v102\n"
  "v_mul_f32 v103, s34, v103\n"
  "v_mfma_f32_16x16x32_bf16 v[8:11], a[180:183], a[236:239], v[8:11]\n"
  "ds_read_b64_tr_b16 a[208:209], v223 offset:12800\n"
  "ds_read_b64_tr_b16 a[210:211], v223 offset:13056\n"
  "v_cvt_pk_bf16_f32 v104, v96, v97\n"
  "v_mfma_f32_16x16x32_bf16 v[12:15], a[184:187], a[232:235], v[12:15]\n"
  "ds_read_b64_tr_b16 a[212:213], v223 offset:16896\n"
  "ds_read_b64_tr_b16 a[214:215], v223 offset:17152\n"
  "v_cvt_pk_bf16_f32 v106, v100, v101\n"
  "v_mfma_f32_16x16x32_bf16 v[12:15], a[188:191], a[236:239], v[12:15]\n"
  "ds_read_b64_tr_b16 a[216:217], v226 offset:12800\n"
  "ds_read_b64_tr_b16 a[218:219], v226 offset:13056\n"
  "v_cvt_pk_bf16_f32 v105, v98, v99\n"
  "v_mfma_f32_16x16x32_bf16 v[16:19], a[192:195], a[232:235], v[16:19]\n"
  "ds_read_b64_tr_b16 a[220:221], v226 offset:16896\n"
  "ds_read_b64_tr_b16 a[222:223], v226 offset:17152\n"
  "v_cvt_pk_bf16_f32 v107, v102, v103\n"
  "s_waitcnt lgkmcnt(6)\n"
  "v_mfma_f32_16x16x32_bf16 v[16:19], a[196:199], a[236:239], v[16:19]\n"
  "ds_read_b128 a[240:243], v128 offset:45056\n"
  "v_permlane16_swap_b32 v104, v106\n"
  "v_permlane16_swap_b32 v105, v107\n"
  "v_mfma_f32_16x16x32_bf16 v[20:23], a[200:203], a[232:235], v[20:23]\n"
  "buffer_load_dwordx4 v239, s[48:51], s66 offen offset:1024 nt lds\n"
  "ds_read_b128 a[244:247], v128 offset:45568\n"
  "v_add_u32 v253, 0x16000, v251\n"
  "v_mfma_f32_16x16x32_bf16 v[20:23], a[204:207], a[236:239], v[20:23]\n"
  "v_add_u32 v96, s97, v253\n"
  "ds_write_b128 v96, v[104:107]\n"
  "v_mul_f32 v0, s34, v0\n"
  "v_mfma_f32_16x16x32_bf16 v[24:27], a[208:211], a[232:235], v[24:27]\n"
  "v_mul_f32 v1, s34, v1\n"
  "v_mul_f32 v2, s34, v2\n"
  "v_mul_f32 v3, s34, v3\n"
  "s_waitcnt lgkmcnt(2)\n"
  "v_mfma_f32_16x16x32_bf16 v[24:27], a[212:215], a[236:239], v[24:27]\n"
  "v_mul_f32 v4, s34, v4\n"
  "v_mul_f32 v5, s34, v5\n"
  "v_mul_f32 v6, s34, v6\n"
  "v_mfma_f32_16x16x32_bf16 v[28:31], a[216:219], a[232:235], v[28:31]\n"
  "v_mul_f32 v7, s34, v7\n"
  "v_cvt_pk_bf16_f32 v200, v0, v1\n"
  "v_cvt_pk_bf16_f32 v202, v4, v5\n"
  "v_mfma_f32_16x16x32_bf16 v[28:31], a[220:223], a[236:239], v[28:31]\n"
  "v_cvt_pk_bf16_f32 v201, v2, v3\n"
  "v_cvt_pk_bf16_f32 v203, v6, v7\n"
  "v_permlane16_swap_b32 v200, v202\n"
  "v_mfma_f32_16x16x32_bf16 v[32:35], a[160:163], a[240:243], v[32:35]\n"
  "v_permlane16_swap_b32 v201, v203\n"
  "v_mul_f32 v8, s34, v8\n"
  "v_mul_f32 v9, s34, v9\n"
  "s_waitcnt lgkmcnt(0)\n"
  "v_mfma_f32_16x16x32_bf16 v[32:35], a[164:167], a[244:247], v[32:35]\n"
  "v_mul_f32 v10, s34, v10\n"
  "v_mul_f32 v11, s34, v11\n"
  "v_mul_f32 v12, s34, v12\n"
  "v_mfma_f32_16x16x32_bf16 v[36:39], a[168:171], a[240:243], v[36:39]\n"
  "v_mul_f32 v13, s34, v13\n"
  "v_mul_f32 v14, s34, v14\n"
  "v_mul_f32 v15, s34, v15\n"
  "v_mfma_f32_16x16x32_bf16 v[36:39], a[172:175], a[244:247], v[36:39]\n"
  "v_cvt_pk_bf16_f32 v204, v8, v9\n"
  "v_cvt_pk_bf16_f32 v206, v12, v13\n"
  "v_cvt_pk_bf16_f32 v205, v10, v11\n"
  "v_mfma_f32_16x16x32_bf16 v[40:43], a[176:179], a[240:243], v[40:43]\n"
  "v_cvt_pk_bf16_f32 v207, v14, v15\n"
  "v_permlane16_swap_b32 v204, v206\n"
  "s_nop 0\n"
  "v_permlane16_swap_b32 v205, v207\n"
  "v_mfma_f32_16x16x32_bf16 v[40:43], a[180:183], a[244:247], v[40:43]\n"
  "v_mul_f32 v16, s34, v16\n"
  "v_mul_f32 v17, s34, v17\n"
  "v_mul_f32 v18, s34, v18\n"
  "v_mfma_f32_16x16x32_bf16 v[44:47], a[184:187], a[240:243], v[44:47]\n"
  "v_mul_f32 v19, s34, v19\n"
  "v_mul_f32 v20, s34, v20\n"
  "v_mul_f32 v21, s34, v21\n"
  "v_mfma_f32_16x16x32_bf16 v[44:47], a[188:191], a[244:247], v[44:47]\n"
  "v_mul_f32 v22, s34, v22\n"
  "v_mul_f32 v23, s34, v23\n"
  "v_cvt_pk_bf16_f32 v208, v16, v17\n"
  "v_mfma_f32_16x16x32_bf16 v[48:51], a[192:195], a[240:243], v[48:51]\n"
  "v_cvt_pk_bf16_f32 v210, v20, v21\n"
  "v_cvt_pk_bf16_f32 v209, v18, v19\n"
  "v_cvt_pk_bf16_f32 v211, v22, v23\n"
  "v_mfma_f32_16x16x32_bf16 v[48:51], a[196:199], a[244:247], v[48:51]\n"
  "ds_read_b128 a[248:251], v128 offset:47104\n"
  "v_permlane16_swap_b32 v208, v210\n"
  "v_permlane16_swap_b32 v209, v211\n"
  "v_mfma_f32_16x16x32_bf16 v[52:55], a[200:203], a[240:243], v[52:55]\n"
  "buffer_load_dwordx4 v238, s[48:51], s66 offen offset:2048 nt lds\n"
  "ds_read_b128 a[252:255], v128 offset:47616\n"
  "v_mul_f32 v24, s34, v24\n"
  "v_mfma_f32_16x16x32_bf16 v[52:55], a[204:207], a[244:247], v[52:55]\n"
  "v_mul_f32 v25, s34, v25\n"
  "v_mul_f32 v26, s34, v26\n"
  "v_mul_f32 v27, s34, v27\n"
  "v_mfma_f32_16x16x32_bf16 v[56:59], a[208:211], a[240:243], v[56:59]\n"
  "v_mul_f32 v28, s34, v28\n"
  "v_mul_f32 v29, s34, v29\n"
  "v_mul_f32 v30, s34, v30\n"
  "v_mfma_f32_16x16x32_bf16 v[56:59], a[212:215], a[244:247], v[56:59]\n"
  "v_mul_f32 v31, s34, v31\n"
  "v_cvt_pk_bf16_f32 v212, v24, v25\n"
  "v_cvt_pk_bf16_f32 v214, v28, v29\n"
  "v_mfma_f32_16x16x32_bf16 v[60:63], a[216:219], a[240:243], v[60:63]\n"
  "v_cvt_pk_bf16_f32 v213, v26, v27\n"
  "v_cvt_pk_bf16_f32 v215, v30, v31\n"
  "v_permlane16_swap_b32 v212, v214\n"
  "v_mfma_f32_16x16x32_bf16 v[60:63], a[220:223], a[244:247], v[60:63]\n"
  "v_permlane16_swap_b32 v213, v215\n"
  "v_mul_f32 v32, s34, v32\n"
  "v_mul_f32 v33, s34, v33\n"
  "s_waitcnt lgkmcnt(0)\n"
  "v_mfma_f32_16x16x32_bf16 v[64:67], a[160:163], a[248:251], v[64:67]\n"
  "v_mul_f32 v34, s34, v34\n"
  "v_mul_f32 v35, s34, v35\n"
  "v_mul_f32 v36, s34, v36\n"
  "v_mfma_f32_16x16x32_bf16 v[64:67], a[164:167], a[252:255], v[64:67]\n"
  "v_mul_f32 v37, s34, v37\n"
  "v_mul_f32 v38, s34, v38\n"
  "v_mul_f32 v39, s34, v39\n"
  "v_mfma_f32_16x16x32_bf16 v[68:71], a[168:171], a[248:251], v[68:71]\n"
  "s_barrier\n"
  "v_cvt_pk_bf16_f32 v112, v32, v33\n"
  "v_mfma_f32_16x16x32_bf16 v[68:71], a[172:175], a[252:255], v[68:71]\n"
  "v_cvt_pk_bf16_f32 v114, v36, v37\n"
  "v_cvt_pk_bf16_f32 v113, v34, v35\n"
  "v_cvt_pk_bf16_f32 v115, v38, v39\n"
  "v_mfma_f32_16x16x32_bf16 v[72:75], a[176:179], a[248:251], v[72:75]\n"
  "v_permlane16_swap_b32 v112, v114\n"
  "v_permlane16_swap_b32 v113, v115\n"
  "v_mul_f32 v40, s34, v40\n"
  "v_mfma_f32_16x16x32_bf16 v[72:75], a[180:183], a[252:255], v[72:75]\n"
  "v_mul_f32 v41, s34, v41\n"
  "v_mul_f32 v42, s34, v42\n"
  "v_mul_f32 v43, s34, v43\n"
  "v_mfma_f32_16x16x32_bf16 v[76:79], a[184:187], a[248:251], v[76:79]\n"
  "v_mul_f32 v44, s34, v44\n"
  "v_mul_f32 v45, s34, v45\n"
  "v_mul_f32 v46, s34, v46\n"
  "v_mfma_f32_16x16x32_bf16 v[76:79], a[188:191], a[252:255], v[76:79]\n"
  "v_mul_f32 v47, s34, v47\n"
  "v_cvt_pk_bf16_f32 v116, v40, v41\n"
  "v_cvt_pk_bf16_f32 v118, v44, v45\n"
  "v_mfma_f32_16x16x32_bf16 v[80:83], a[192:195], a[248:251], v[80:83]\n"
  "v_cvt_pk_bf16_f32 v117, v42, v43\n"
  "v_cvt_pk_bf16_f32 v119, v46, v47\n"
  "v_permlane16_swap_b32 v116, v118\n"
  "v_mfma_f32_16x16x32_bf16 v[80:83], a[196:199], a[252:255], v[80:83]\n"
  "ds_read_b128 a[64:67], v216 offset:32768\n"
  "v_permlane16_swap_b32 v117, v119\n"
  "v_mul_f32 v48, s34, v48\n"
  "v_mfma_f32_16x16x32_bf16 v[84:87], a[200:203], a[248:251], v[84:87]\n"
  "buffer_load_dwordx4 v239, s[48:51], s66 offen offset:3072 nt lds\n"
  "ds_read_b128 a[80:83], v217 offset:32768\n"
  "v_mul_f32 v49, s34, v49\n"
  "v_mfma_f32_16x16x32_bf16 v[84:87], a[204:207], a[252:255], v[84:87]\n"
  "ds_read_b128 a[68:71], v216 offset:33280\n"
  "v_mul_f32 v50, s34, v50\n"
  "v_mul_f32 v51, s34, v51\n"
  "v_mfma_f32_16x16x32_bf16 v[88:91], a[208:211], a[248:251], v[88:91]\n"
  "ds_read_b128 a[84:87], v217 offset:33280\n"
  "v_mul_f32 v52, s34, v52\n"
  "v_mul_f32 v53, s34, v53\n"
  "v_mfma_f32_16x16x32_bf16 v[88:91], a[212:215], a[252:255], v[88:91]\n"
  "ds_read_b128 a[72:75], v216 offset:40960\n"
  "v_mul_f32 v54, s34, v54\n"
  "v_mul_f32 v55, s34, v55\n"
  "v_mfma_f32_16x16x32_bf16 v[92:95], a[216:219], a[248:251], v[92:95]\n"
  "ds_read_b128 a[88:91], v217 offset:40960\n"
  "v_cvt_pk_bf16_f32 v120, v48, v49\n"
  "v_cvt_pk_bf16_f32 v122, v52, v53\n"
  "v_mfma_f32_16x16x32_bf16 v[92:95], a[220:223], a[252:255], v[92:95]\n"
  "ds_read_b128 a[76:79], v216 offset:41472\n"
  "v_cvt_pk_bf16_f32 v121, v50, v51\n"
  "v_cvt_pk_bf16_f32 v123, v54, v55\n"
  "s_waitcnt lgkmcnt(3)\n"
  "v_mfma_f32_16x16x32_bf16 v[128:131], a[64:67], v[200:203], 0\n"
  "ds_read_b128 a[92:95], v217 offset:41472\n"
  "v_permlane16_swap_b32 v120, v122\n"
  "v_permlane16_swap_b32 v121, v123\n"
  "v_mfma_f32_16x16x32_bf16 v[132:135], a[80:83], v[200:203], 0\n"
  "ds_read_b128 a[48:51], v253 offset:24576\n"
  "v_mul_f32 v56, s34, v56\n"
  "v_mul_f32 v57, s34, v57\n"
  "v_mfma_f32_16x16x32_bf16 v[128:131], a[68:71], v[204:207], v[128:131]\n"
  "ds_read_b128 a[52:55], v253 offset:25600\n"
  "v_mul_f32 v58, s34, v58\n"
  "v_mul_f32 v59, s34, v59\n"
  "v_mfma_f32_16x16x32_bf16 v[132:135], a[84:87], v[204:207], v[132:135]\n"
  "ds_read_b128 a[56:59], v253 offset:26624\n"
  "v_mul_f32 v60, s34, v60\n"
  "v_mul_f32 v61, s34, v61\n"
  "s_waitcnt lgkmcnt(3)\n"
  "v_mfma_f32_16x16x32_bf16 v[128:131], a[72:75], v[208:211], v[128:131]\n"
  "ds_read_b128 a[60:63], v253 offset:27648\n"
  "v_mul_f32 v62, s34, v62\n"
  "v_mul_f32 v63, s34, v63\n"
  "v_mfma_f32_16x16x32_bf16 v[132:135], a[88:91], v[208:211], v[132:135]\n"
  "v_cvt_pk_bf16_f32 v124, v56, v57\n"
  "v_cvt_pk_bf16_f32 v126, v60, v61\n"
  "v_cvt_pk_bf16_f32 v125, v58, v59\n"
  "v_mfma_f32_16x16x32_bf16 v[128:131], a[76:79], v[212:215], v[128:131]\n"
  "v_cvt_pk_bf16_f32 v127, v62, v63\n"
  "v_permlane16_swap_b32 v124, v126\n"
  "s_nop 0\n"
  "v_permlane16_swap_b32 v125, v127\n"
  "v_mfma_f32_16x16x32_bf16 v[132:135], a[92:95], v[212:215], v[132:135]\n"
  "v_mul_f32 v64, s34, v64\n"
  "v_mul_f32 v65, s34, v65\n"
  "v_mul_f32 v66, s34, v66\n"
  "s_waitcnt lgkmcnt(0)\n"
  "v_mfma_f32_16x16x32_bf16 v[176:179], a[64:67], a[48:51], 0\n"
  "v_mul_f32 v67, s34, v67\n"
  "v_mul_f32 v68, s34, v68\n"
  "v_mul_f32 v69, s34, v69\n"
  "v_mfma_f32_16x16x32_bf16 v[176:179], a[68:71], a[52:55], v[176:179]\n"
  "v_mul_f32 v70, s34, v70\n"
  "v_mul_f32 v71, s34, v71\n"
  "v_cvt_pk_bf16_f32 v96, v64, v65\n"
  "v_mfma_f32_16x16x32_bf16 v[176:179], a[72:75], a[56:59], v[176:179]\n"
  "s_add_u32 m0, s65, 0x8000\n"
  "s_mov_b32 s67, 0x8000\n"
  "buffer_load_dwordx4 v238, s[48:51], s67 offen nt lds\n"
  "v_mfma_f32_16x16x32_bf16 v[176:179], a[76:79], a[60:63], v[176:179]\n"
  "v_cvt_pk_bf16_f32 v98, v68, v69\n"
  "v_cvt_pk_bf16_f32 v97, v66, v67\n"
  "v_cvt_pk_bf16_f32 v99, v70, v71\n"
  "v_mfma_f32_16x16x32_bf16 v[136:139], a[64:67], v[112:115], 0\n"
  "v_permlane16_swap_b32 v96, v98\n"
  "v_permlane16_swap_b32 v97, v99\n"
  "v_mul_f32 v72, s34, v72\n"
  "v_mfma_f32_16x16x32_bf16 v[140:143], a[80:83], v[112:115], 0\n"
  "v_mul_f32 v73, s34, v73\n"
  "v_mul_f32 v74, s34, v74\n"
  "v_mul_f32 v75, s34, v75\n"
  "v_mfma_f32_16x16x32_bf16 v[136:139], a[68:71], v[116:119], v[136:139]\n"
  "v_mul_f32 v76, s34, v76\n"
  "v_mul_f32 v77, s34, v77\n"
  "v_mul_f32 v78, s34, v78\n"
  "v_mfma_f32_16x16x32_bf16 v[140:143], a[84:87], v[116:119], v[140:143]\n"
  "v_mul_f32 v79, s34, v79\n"
  "v_cvt_pk_bf16_f32 v100, v72, v73\n"
  "v_cvt_pk_bf16_f32 v102, v76, v77\n"
  "v_mfma_f32_16x16x32_bf16 v[136:139], a[72:75], v[120:123], v[136:139]\n"
  "v_cvt_pk_bf16_f32 v101, v74, v75\n"
  "v_cvt_pk_bf16_f32 v103, v78, v79\n"
  "v_permlane16_swap_b32 v100, v102\n"
  "v_mfma_f32_16x16x32_bf16 v[140:143], a[88:91], v[120:123], v[140:143]\n"
  "v_permlane16_swap_b32 v101, v103\n"
  "v_mul_f32 v80, s34, v80\n"
  "v_mul_f32 v81, s34, v81\n"
  "v_mfma_f32_16x16x32_bf16 v[136:139], a[76:79], v[124:127], v[136:139]\n"
  "v_mul_f32 v82, s34, v82\n"
  "v_mul_f32 v83, s34, v83\n"
  "v_mul_f32 v84, s34, v84\n"
  "v_mfma_f32_16x16x32_bf16 v[140:143], a[92:95], v[124:127], v[140:143]\n"
  "v_mul_f32 v85, s34, v85\n"
  "v_mul_f32 v86, s34, v86\n"
  "v_mul_f32 v87, s34, v87\n"
  "v_mfma_f32_16x16x32_bf16 v[144:147], a[64:67], v[96:99], 0\n"
  "v_cvt_pk_bf16_f32 v104, v80, v81\n"
  "v_cvt_pk_bf16_f32 v106, v84, v85\n"
  "v_cvt_pk_bf16_f32 v105, v82, v83\n"
  "v_mfma_f32_16x16x32_bf16 v[148:151], a[80:83], v[96:99], 0\n"
  "ds_read_b128 a[196:199], v218 offset:32768\n"
  "v_cvt_pk_bf16_f32 v107, v86, v87\n"
  "v_permlane16_swap_b32 v104, v106\n"
  "v_mfma_f32_16x16x32_bf16 v[144:147], a[68:71], v[100:103], v[144:147]\n"
  "ds_read_b128 a[212:215], v219 offset:32768\n"
  "v_permlane16_swap_b32 v105, v107\n"
  "v_mul_f32 v88, s34, v88\n"
  "v_mfma_f32_16x16x32_bf16 v[148:151], a[84:87], v[100:103], v[148:151]\n"
  "ds_read_b128 a[200:203], v218 offset:33280\n"
  "v_mul_f32 v89, s34, v89\n"
  "v_mul_f32 v90, s34, v90\n"
  "v_mfma_f32_16x16x32_bf16 v[144:147], a[72:75], v[104:107], v[144:147]\n"
  "ds_read_b128 a[216:219], v219 offset:33280\n"
  "v_mul_f32 v91, s34, v91\n"
  "v_mul_f32 v92, s34, v92\n"
  "v_mfma_f32_16x16x32_bf16 v[148:151], a[88:91], v[104:107], v[148:151]\n"
  "v_mul_f32 v93, s34, v93\n"
  "v_mul_f32 v94, s34, v94\n"
  "v_mul_f32 v95, s34, v95\n"
  "v_cvt_pk_bf16_f32 v108, v88, v89\n"
  "v_cvt_pk_bf16_f32 v110, v92, v93\n"
  "v_cvt_pk_bf16_f32 v109, v90, v91\n"
  "v_cvt_pk_bf16_f32 v111, v94, v95\n"
  "v_permlane16_swap_b32 v108, v110\n"
  "s_nop 0\n"
  "v_permlane16_swap_b32 v109, v111\n"
  "ds_read_b128 a[204:207], v218 offset:40960\n"
  "s_nop 0\n"
  "v_mfma_f32_16x16x32_bf16 v[144:147], a[76:79], v[108:111], v[144:147]\n"
  "buffer_load_dwordx4 v239, s[48:51], s67 offen offset:1024 nt lds\n"
  "ds_read_b128 a[220:223], v219 offset:40960\n"
  "v_max3_f32 v244, v128, v129, v130\n"
  "v_mfma_f32_16x16x32_bf16 v[148:151], a[92:95], v[108:111], v[148:151]\n"
  "v_max3_f32 v244, v244, v131, v132\n"
  "v_max3_f32 v244, v244, v133, v134\n"
  "v_max_f32 v244, v244, v135\n"
  "v_max3_f32 v180, v176, v177, v178\n"
  "v_mov_b32 v245, v244\n"
  "v_max_f32 v180, v180, v179\n"
  "s_nop 0\n"
  "v_permlane16_swap_b32 v244, v245\n"
  "v_mov_b32 v181, v180\n"
  "v_max_f32 v244, v244, v245\n"
  "s_nop 0\n"
  "v_permlane16_swap_b32 v180, v181\n"
  "v_mov_b32 v245, v244\n"
  "v_max_f32 v180, v180, v181\n"
  "s_nop 0\n"
  "v_permlane32_swap_b32 v244, v245\n"
  "v_mov_b32 v181, v180\n"
  "v_max_f32 v244, v244, v245\n"
  "s_nop 0\n"
  "v_permlane32_swap_b32 v180, v181\n"
  "v_sub_f32 v184, 0xc2000000, v244\n"
  "v_max_f32 v180, v180, v181\n"
  "v_sub_f32 v185, 0xc2000000, v244\n"
  "s_lshl_b32 s58, s19, 2\n"
  "v_add_u32 v181, s58, v253\n"
  "v_sub_f32 v186, 0xc2000000, v244\n"
  "v_sub_f32 v187, 0xc2000000, v244\n"
  "ds_read_b128 a[208:211], v218 offset:41472\n"
  "ds_write_b32 v181, v180 offset:28672\n"
  "s_waitcnt lgkmcnt(2)\n"
  "v_mfma_f32_16x16x32_bf16 v[152:155], a[196:199], v[200:203], v[184:187]\n"
  "ds_read_b128 a[224:227], v219 offset:41472\n"
  "v_max3_f32 v246, v136, v137, v138\n"
  "v_max3_f32 v246, v246, v139, v140\n"
  "v_mfma_f32_16x16x32_bf16 v[156:159], a[212:215], v[200:203], v[184:187]\n"
  "s_lshr_b32 s58, s19, 1\n"
  "s_lshl_b32 s58, s58, 12\n"
  "s_and_b32 s59, s19, 1\n"
  "s_lshl_b32 s59, s59, 8\n"
  "s_or_b32 s58, s58, s59\n"
  "v_add_u32 v220, s58, v222\n"
  "v_xor_b32 v221, 0x100, v220\n"
  "v_xor_b32 v228, 0x1000, v220\n"
  "v_xor_b32 v229, 0x1100, v220\n"
  "v_xor_b32 v230, 32, v220\n"
  "v_xor_b32 v231, 0x120, v220\n"
  "v_xor_b32 v232, 0x1020, v220\n"
  "v_xor_b32 v233, 0x1120, v220\n"
  "v_mfma_f32_16x16x32_bf16 v[152:155], a[200:203], v[204:207], v[152:155]\n"
  "s_waitcnt vmcnt(6) lgkmcnt(0)\n"
  "s_barrier\n"
  "v_max3_f32 v246, v246, v141, v142\n"
  "v_mfma_f32_16x16x32_bf16 v[156:159], a[216:219], v[204:207], v[156:159]\n"
  "v_max_f32 v246, v246, v143\n"
  "v_mov_b32 v247, v246\n"
  "s_nop 1\n"
  "v_permlane16_swap_b32 v246, v247\n"
  "v_mfma_f32_16x16x32_bf16 v[152:155], a[204:207], v[208:211], v[152:155]\n"
  "v_max_f32 v246, v246, v247\n"
  "v_mov_b32 v247, v246\n"
  "s_nop 1\n"
  "v_permlane32_swap_b32 v246, v247\n"
  "v_mfma_f32_16x16x32_bf16 v[156:159], a[220:223], v[208:211], v[156:159]\n"
  "v_max_f32 v246, v246, v247\n"
  "v_sub_f32 v188, 0xc2000000, v246\n"
  "v_sub_f32 v189, 0xc2000000, v246\n"
  "v_mfma_f32_16x16x32_bf16 v[152:155], a[208:211], v[212:215], v[152:155]\n"
  "v_sub_f32 v190, 0xc2000000, v246\n"
  "v_sub_f32 v191, 0xc2000000, v246\n"
  "v_max3_f32 v248, v144, v145, v146\n"
  "v_mfma_f32_16x16x32_bf16 v[156:159], a[224:227], v[212:215], v[156:159]\n"
  "ds_read_b64_tr_b16 a[96:97], v220\n"
  "v_max3_f32 v248, v248, v147, v148\n"
  "ds_read_b64_tr_b16 a[98:99], v221\n"
  "v_mfma_f32_16x16x32_bf16 v[160:163], a[196:199], v[112:115], v[188:191]\n"
  "v_max3_f32 v248, v248, v149, v150\n"
  "ds_read_b64_tr_b16 a[100:101], v230\n"
  "v_max_f32 v248, v248, v151\n"
  "v_mfma_f32_16x16x32_bf16 v[164:167], a[212:215], v[112:115], v[188:191]\n"
  "ds_read_b64_tr_b16 a[102:103], v231\n"
  "v_mov_b32 v249, v248\n"
  "ds_read_b64_tr_b16 a[104:105], v220 offset:512\n"
  "v_mfma_f32_16x16x32_bf16 v[160:163], a[200:203], v[116:119], v[160:163]\n"
  "v_permlane16_swap_b32 v248, v249\n"
  "ds_read_b64_tr_b16 a[106:107], v221 offset:512\n"
  "v_max_f32 v248, v248, v249\n"
  "v_mfma_f32_16x16x32_bf16 v[164:167], a[216:219], v[116:119], v[164:167]\n"
  "ds_read_b64_tr_b16 a[108:109], v230 offset:512\n"
  "v_mov_b32 v249, v248\n"
  "ds_read_b64_tr_b16 a[110:111], v231 offset:512\n"
  "L_st2:\n"
  "s_cbranch_execz L_st6\n"
  "v_mfma_f32_16x16x32_bf16 v[160:163], a[204:207], v[120:123], v[160:163]\n"
  "v_permlane32_swap_b32 v248, v249\n"
  "ds_read_b64_tr_b16 a[112:113], v220 offset:8192\n"
  "v_max_f32 v248, v248, v249\n"
  "v_mfma_f32_16x16x32_bf16 v[164:167], a[220:223], v[120:123], v[164:167]\n"
  "ds_read_b64_tr_b16 a[114:115], v221 offset:8192\n"
  "v_sub_f32 v192, 0xc2000000, v248\n"
  "ds_read_b64_tr_b16 a[116:117], v230 offset:8192\n"
  "v_mfma_f32_16x16x32_bf16 v[160:163], a[208:211], v[124:127], v[160:163]\n"
  "buffer_load_dwordx4 v238, s[48:51], s67 offen offset:2048 nt lds\n"
  "v_sub_f32 v193, 0xc2000000, v248\n"
  "ds_read_b64_tr_b16 a[118:119], v231 offset:8192\n"
  "v_mfma_f32_16x16x32_bf16 v[164:167], a[224:227], v[124:127], v[164:167]\n"
  "v_sub_f32 v194, 0xc2000000, v248\n"
  "v_sub_f32 v195, 0xc2000000, v248\n"
  "ds_read_b64_tr_b16 a[120:121], v220 offset:8704\n"
  "s_nop 0\n"
  "v_mfma_f32_16x16x32_bf16 v[168:171], a[196:199], v[96:99], v[192:195]\n"
  "ds_read_b64_tr_b16 a[122:123], v221 offset:8704\n"
  "ds_read_b64_tr_b16 a[124:125], v230 offset:8704\n"
  "ds_read_b64_tr_b16 a[126:127], v231 offset:8704\n"
  "v_mfma_f32_16x16x32_bf16 v[172:175], a[212:215], v[96:99], v[192:195]\n"
  "v_add_f32 v128, v184, v128\n"
  "v_add_f32 v129, v184, v129\n"
  "v_add_f32 v130, v184, v130\n"
  "v_mfma_f32_16x16x32_bf16 v[168:171], a[200:203], v[100:103], v[168:171]\n"
  "v_add_f32 v131, v184, v131\n"
  "v_exp_f32 v128, v128\n"
  "v_mfma_f32_16x16x32_bf16 v[172:175], a[216:219], v[100:103], v[172:175]\n"
  "v_add_f32 v132, v184, v132\n"
  "v_exp_f32 v129, v129\n"
  "v_mfma_f32_16x16x32_bf16 v[168:171], a[204:207], v[104:107], v[168:171]\n"
  "v_cvt_pk_bf16_f32 v128, v128, v129\n"
  "v_exp_f32 v130, v130\n"
  "v_mfma_f32_16x16x32_bf16 v[172:175], a[220:223], v[104:107], v[172:175]\n"
  "v_add_f32 v133, v184, v133\n"
  "v_exp_f32 v131, v131\n"
  "v_mfma_f32_16x16x32_bf16 v[168:171], a[208:211], v[108:111], v[168:171]\n"
  "v_exp_f32 v132, v132\n"
  "v_exp_f32 v133, v133\n"
  "v_cvt_pk_bf16_f32 v129, v130, v131\n"
  "v_cvt_pk_bf16_f32 v130, v132, v133\n"
  "v_mfma_f32_16x16x32_bf16 v[172:175], a[224:227], v[108:111], v[172:175]\n"
  "s_waitcnt lgkmcnt(0)\n"
  "v_add_f32 v134, v184, v134\n"
  "v_exp_f32 v134, v134\n"
  "v_add_f32 v135, v184, v135\n"
  "v_exp_f32 v135, v135\n"
  "s_nop 0\n"
  "v_cvt_pk_bf16_f32 v131, v134, v135\n"
  "s_nop 1\n"
  "v_mfma_f32_16x16x32_bf16 v[0:3], a[96:99], v[128:131], 0\n"
  "v_add_f32 v136, v188, v136\n"
  "v_add_f32 v137, v188, v137\n"
  "v_mfma_f32_16x16x32_bf16 a[32:35], a[128:131], v[128:131], 0\n"
  "v_exp_f32 v136, v136\n"
  "v_mfma_f32_16x16x32_bf16 v[4:7], a[100:103], v[128:131], 0\n"
  "v_add_f32 v138, v188, v138\n"
  "v_exp_f32 v137, v137\n"
  "v_mfma_f32_16x16x32_bf16 v[8:11], a[104:107], v[128:131], 0\n"
  "v_add_f32 v139, v188, v139\n"
  "v_exp_f32 v138, v138\n"
  "v_mfma_f32_16x16x32_bf16 v[12:15], a[108:111], v[128:131], 0\n"
  "v_cvt_pk_bf16_f32 v136, v136, v137\n"
  "v_exp_f32 v139, v139\n"
  "v_mfma_f32_16x16x32_bf16 v[16:19], a[112:115], v[128:131], 0\n"
  "v_add_f32 v140, v188, v140\n"
  "v_cvt_pk_bf16_f32 v137, v138, v139\n"
  "buffer_load_dwordx4 v239, s[48:51], s67 offen offset:3072 nt lds\n"
  "v_mfma_f32_16x16x32_bf16 v[20:23], a[116:119], v[128:131], 0\n"
  "v_add_f32 v141, v188, v141\n"
  "v_exp_f32 v140, v140\n"
  "v_mfma_f32_16x16x32_bf16 v[24:27], a[120:123], v[128:131], 0\n"
  "v_exp_f32 v141, v141\n"
  "s_nop 0\n"
  "v_cvt_pk_bf16_f32 v138, v140, v141\n"
  "v_add_f32 v142, v188, v142\n"
  "v_exp_f32 v142, v142\n"
  "v_add_f32 v143, v188, v143\n"
  "v_exp_f32 v143, v143\n"
  "s_nop 0\n"
  "v_cvt_pk_bf16_f32 v139, v142, v143\n"
  "v_mfma_f32_16x16x32_bf16 v[28:31], a[124:127], v[128:131], 0\n"
  "ds_read_b128 v[180:183], v253 offset:28672\n"
  "s_waitcnt lgkmcnt(0)\n"
  "v_max3_f32 v180, v180, v181, v182\n"
  "v_max_f32 v180, v180, v183\n"
  "v_mfma_f32_16x16x32_bf16 v[32:35], a[96:99], v[136:139], 0\n"
  "v_sub_f32 v196, 0xc2000000, v180\n"
  "v_sub_f32 v197, 0xc2000000, v180\n"
  "v_sub_f32 v198, 0xc2000000, v180\n"
  "v_mfma_f32_16x16x32_bf16 a[36:39], a[128:131], v[136:139], 0\n"
  "v_sub_f32 v199, 0xc2000000, v180\n"
  "v_add_f32 v176, v196, v176\n"
  "v_add_f32 v177, v196, v177\n"
  "v_mfma_f32_16x16x32_bf16 v[36:39], a[100:103], v[136:139], 0\n"
  "v_add_f32 v178, v196, v178\n"
  "v_add_f32 v179, v196, v179\n"
  "v_mfma_f32_16x16x32_bf16 v[40:43], a[104:107], v[136:139], 0\n"
  "v_exp_f32 v176, v176\n"
  "v_mfma_f32_16x16x32_bf16 v[44:47], a[108:111], v[136:139], 0\n"
  "v_exp_f32 v177, v177\n"
  "v_mfma_f32_16x16x32_bf16 v[48:51], a[112:115], v[136:139], 0\n"
  "v_exp_f32 v178, v178\n"
  "v_mfma_f32_16x16x32_bf16 v[52:55], a[116:119], v[136:139], 0\n"
  "v_exp_f32 v179, v179\n"
  "v_mfma_f32_16x16x32_bf16 v[56:59], a[120:123], v[136:139], 0\n"
  "v_cvt_pk_bf16_f32 v176, v176, v177\n"
  "v_cvt_pk_bf16_f32 v177, v178, v179\n"
  "v_mfma_f32_16x16x32_bf16 v[60:63], a[124:127], v[136:139], 0\n"
  "v_add_f32 v144, v192, v144\n"
  "v_add_f32 v145, v192, v145\n"
  "v_mfma_f32_16x16x16_bf16 a[0:3], a[96:97], v[176:177], 0\n"
  "v_exp_f32 v144, v144\n"
  "v_mfma_f32_16x16x16_bf16 a[4:7], a[100:101], v[176:177], 0\n"
  "s_add_u32 m0, s65, 0xc000\n"
  "v_exp_f32 v145, v145\n"
  "v_mfma_f32_16x16x16_bf16 a[8:11], a[104:105], v[176:177], 0\n"
  "s_mov_b32 s66, 0xc000\n"
  "buffer_load_dwordx4 v238, s[48:51], s66 offen nt lds\n"
  "v_add_f32 v146, v192, v146\n"
  "v_mfma_f32_16x16x16_bf16 a[12:15], a[108:109], v[176:177], 0\n"
  "v_add_f32 v147, v192, v147\n"
  "v_exp_f32 v146, v146\n"
  "v_mfma_f32_16x16x16_bf16 a[16:19], a[112:113], v[176:177], 0\n"
  "v_cvt_pk_bf16_f32 v144, v144, v145\n"
  "v_exp_f32 v147, v147\n"
  "v_mfma_f32_16x16x16_bf16 a[20:23], a[116:117], v[176:177], 0\n"
  "v_add_f32 v148, v192, v148\n"
  "v_cvt_pk_bf16_f32 v145, v146, v147\n"
  "v_add_f32 v149, v192, v149\n"
  "v_mfma_f32_16x16x16_bf16 a[24:27], a[120:121], v[176:177], 0\n"
  "v_add_f32 v150, v192, v150\n"
  "v_exp_f32 v148, v148\n"
  "v_mfma_f32_16x16x16_bf16 a[28:31], a[124:125], v[176:177], 0\n"
  "v_exp_f32 v149, v149\n"
  "v_exp_f32 v150, v150\n"
  "v_cvt_pk_bf16_f32 v146, v148, v149\n"
  "v_add_f32 v151, v192, v151\n"
  "v_exp_f32 v151, v151\n"
  "s_nop 0\n"
  "v_cvt_pk_bf16_f32 v147, v150, v151\n"
  "v_mfma_f32_16x16x16_bf16 a[44:47], a[128:129], v[176:177], 0\n"
  "ds_read_b128 a[64:67], v216 offset:49152\n"
  "ds_read_b128 a[80:83], v217 offset:49152\n"
  "ds_read_b128 a[68:71], v216 offset:49664\n"
  "v_mfma_f32_16x16x32_bf16 v[64:67], a[96:99], v[144:147], 0\n"
  "ds_read_b128 a[84:87], v217 offset:49664\n"
  "ds_read_b128 a[72:75], v216 offset:57344\n"
  "ds_read_b128 a[88:91], v217 offset:57344\n"
  "v_mfma_f32_16x16x32_bf16 a[40:43], a[128:131], v[144:147], 0\n"
  "ds_read_b128 a[76:79], v216 offset:57856\n"
  "ds_read_b128 a[92:95], v217 offset:57856\n"
  "buffer_load_dwordx4 v239, s[48:51], s66 offen offset:1024 nt lds\n"
  "v_mfma_f32_16x16x32_bf16 v[68:71], a[100:103], v[144:147], 0\n"
  "v_exp_f32 v152, v152\n"
  "v_mfma_f32_16x16x32_bf16 v[72:75], a[104:107], v[144:147], 0\n"
  "v_exp_f32 v153, v153\n"
  "v_mfma_f32_16x16x32_bf16 v[76:79], a[108:111], v[144:147], 0\n"
  "v_exp_f32 v154, v154\n"
  "v_mfma_f32_16x16x32_bf16 v[80:83], a[112:115], v[144:147], 0\n"
  "v_exp_f32 v155, v155\n"
  "v_mfma_f32_16x16x32_bf16 v[84:87], a[116:119], v[144:147], 0\n"
  "v_exp_f32 v156, v156\n"
  "v_mfma_f32_16x16x32_bf16 v[88:91], a[120:123], v[144:147], 0\n"
  "v_exp_f32 v157, v157\n"
  "v_mfma_f32_16x16x32_bf16 v[92:95], a[124:127], v[144:147], 0\n"
  "s_waitcnt lgkmcnt(0)\n"
  "buffer_load_dwordx4 v238, s[48:51], s66 offen offset:2048 nt lds\n"
  "v_mfma_f32_16x16x32_bf16 v[128:131], a[64:67], v[200:203], v[184:187]\n"
  "v_exp_f32 v158, v158\n"
  "v_mfma_f32_16x16x32_bf16 v[132:135], a[80:83], v[200:203], v[184:187]\n"
  "v_exp_f32 v159, v159\n"
  "v_mfma_f32_16x16x32_bf16 v[128:131], a[68:71], v[204:207], v[128:131]\n"
  "v_exp_f32 v160, v160\n"
  "v_mfma_f32_16x16x32_bf16 v[132:135], a[84:87], v[204:207], v[132:135]\n"
  "v_exp_f32 v161, v161\n"
  "v_mfma_f32_16x16x32_bf16 v[128:131], a[72:75], v[208:211], v[128:131]\n"
  "v_exp_f32 v162, v162\n"
  "v_mfma_f32_16x16x32_bf16 v[132:135], a[88:91], v[208:211], v[132:135]\n"
  "v_exp_f32 v163, v163\n"
  "v_mfma_f32_16x16x32_bf16 v[128:131], a[76:79], v[212:215], v[128:131]\n"
  "buffer_load_dwordx4 v239, s[48:51], s66 offen offset:3072 nt lds\n"
  "ds_read_b64_tr_b16 a[96:97], v228\n"
  "v_mfma_f32_16x16x32_bf16 v[132:135], a[92:95], v[212:215], v[132:135]\n"
  "v_exp_f32 v164, v164\n"
  "v_mfma_f32_16x16x32_bf16 v[176:179], a[64:67], a[48:51], v[196:199]\n"
  "v_exp_f32 v165, v165\n"
  "v_mfma_f32_16x16x32_bf16 v[176:179], a[68:71], a[52:55], v[176:179]\n"
  "v_exp_f32 v166, v166\n"
  "v_mfma_f32_16x16x32_bf16 v[176:179], a[72:75], a[56:59], v[176:179]\n"
  "v_exp_f32 v167, v167\n"
  "v_mfma_f32_16x16x32_bf16 v[176:179], a[76:79], a[60:63], v[176:179]\n"
  "ds_read_b64_tr_b16 a[98:99], v229\n"
  "ds_read_b64_tr_b16 a[100:101], v232\n"
  "v_mfma_f32_16x16x32_bf16 v[136:139], a[64:67], v[112:115], v[188:191]\n"
  "s_add_u32 m0, s65, 0x10000\n"
  "ds_read_b64_tr_b16 a[102:103], v233\n"
  "s_add_u32 s66, s88, 0\n"
  "v_mfma_f32_16x16x32_bf16 v[140:143], a[80:83], v[112:115], v[188:191]\n"
  "ds_read_b64_tr_b16 a[104:105], v228 offset:512\n"
  "s_and_b32 s66, s66, 3\n"
  "ds_read_b64_tr_b16 a[106:107], v229 offset:512\n"
  "v_mfma_f32_16x16x32_bf16 v[136:139], a[68:71], v[116:119], v[136:139]\n"
  "s_lshl_b32 s66, s66, 17\n"
  "ds_read_b64_tr_b16 a[108:109], v232 offset:512\n"
  "buffer_load_dwordx4 v240, s[72:75], s66 offen lds\n"
  "v_mfma_f32_16x16x32_bf16 v[140:143], a[84:87], v[116:119], v[140:143]\n"
  "ds_read_b64_tr_b16 a[110:111], v233 offset:512\n"
  "ds_read_b64_tr_b16 a[112:113], v228 offset:8192\n"
  "ds_read_b64_tr_b16 a[114:115], v229 offset:8192\n"
  "v_mfma_f32_16x16x32_bf16 v[136:139], a[72:75], v[120:123], v[136:139]\n"
  "ds_read_b64_tr_b16 a[116:117], v232 offset:8192\n"
  "ds_read_b64_tr_b16 a[118:119], v233 offset:8192\n"
  "ds_read_b64_tr_b16 a[120:121], v228 offset:8704\n"
  "v_mfma_f32_16x16x32_bf16 v[140:143], a[88:91], v[120:123], v[140:143]\n"
  "ds_read_b64_tr_b16 a[122:123], v229 offset:8704\n"
  "ds_read_b64_tr_b16 a[124:125], v232 offset:8704\n"
  "v_mfma_f32_16x16x32_bf16 v[136:139], a[76:79], v[124:127], v[136:139]\n"
  "ds_read_b64_tr_b16 a[126:127], v233 offset:8704\n"
  "v_cvt_pk_bf16_f32 v152, v152, v153\n"
  "v_mfma_f32_16x16x32_bf16 v[140:143], a[92:95], v[124:127], v[140:143]\n"
  "v_exp_f32 v168, v168\n"
  "v_mfma_f32_16x16x32_bf16 v[144:147], a[64:67], v[96:99], v[192:195]\n"
  "v_exp_f32 v169, v169\n"
  "v_mfma_f32_16x16x32_bf16 v[148:151], a[80:83], v[96:99], v[192:195]\n"
  "buffer_load_dwordx4 v241, s[72:75], s66 offen offset:1024 lds\n"
  "v_cvt_pk_bf16_f32 v153, v154, v155\n"
  "v_mfma_f32_16x16x32_bf16 v[144:147], a[68:71], v[100:103], v[144:147]\n"
  "v_exp_f32 v170, v170\n"
  "v_mfma_f32_16x16x32_bf16 v[148:151], a[84:87], v[100:103], v[148:151]\n"
  "v_cvt_pk_bf16_f32 v154, v156, v157\n"
  "v_cvt_pk_bf16_f32 v155, v158, v159\n"
  "v_mfma_f32_16x16x32_bf16 v[144:147], a[72:75], v[104:107], v[144:147]\n"
  "v_exp_f32 v171, v171\n"
  "v_mfma_f32_16x16x32_bf16 v[148:151], a[88:91], v[104:107], v[148:151]\n"
  "v_exp_f32 v172, v172\n"
  "v_mfma_f32_16x16x32_bf16 v[144:147], a[76:79], v[108:111], v[144:147]\n"
  "v_exp_f32 v173, v173\n"
  "v_mfma_f32_16x16x32_bf16 v[148:151], a[92:95], v[108:111], v[148:151]\n"
  "s_waitcnt lgkmcnt(0)\n"
  "buffer_load_dwordx4 v242, s[72:75], s66 offen offset:2048 lds\n"
  "v_mfma_f32_16x16x32_bf16 v[0:3], a[96:99], v[152:155], v[0:3]\n"
  "v_exp_f32 v174, v174\n"
  "v_mfma_f32_16x16x32_bf16 a[32:35], a[128:131], v[152:155], a[32:35]\n"
  "v_exp_f32 v175, v175\n"
  "v_mfma_f32_16x16x32_bf16 v[4:7], a[100:103], v[152:155], v[4:7]\n"
  "v_exp_f32 v128, v128\n"
  "v_mfma_f32_16x16x32_bf16 v[8:11], a[104:107], v[152:155], v[8:11]\n"
  "v_cvt_pk_bf16_f32 v160, v160, v161\n"
  "v_cvt_pk_bf16_f32 v161, v162, v163\n"
  "v_mfma_f32_16x16x32_bf16 v[12:15], a[108:111], v[152:155], v[12:15]\n"
  "v_cvt_pk_bf16_f32 v162, v164, v165\n"
  "v_cvt_pk_bf16_f32 v163, v166, v167\n"
  "v_mfma_f32_16x16x32_bf16 v[16:19], a[112:115], v[152:155], v[16:19]\n"
  "v_exp_f32 v129, v129\n"
  "v_mfma_f32_16x16x32_bf16 v[20:23], a[116:119], v[152:155], v[20:23]\n"
  "v_exp_f32 v130, v130\n"
  "v_mfma_f32_16x16x32_bf16 v[24:27], a[120:123], v[152:155], v[24:27]\n"
  "buffer_load_dwordx4 v243, s[72:75], s66 offen offset:3072 lds\n"
  "v_cvt_pk_bf16_f32 v168, v168, v169\n"
  "v_mfma_f32_16x16x32_bf16 v[28:31], a[124:127], v[152:155], v[28:31]\n"
  "v_exp_f32 v131, v131\n"
  "v_mfma_f32_16x16x32_bf16 v[32:35], a[96:99], v[160:163], v[32:35]\n"
  "v_exp_f32 v132, v132\n"
  "v_mfma_f32_16x16x32_bf16 a[36:39], a[128:131], v[160:163], a[36:39]\n"
  "v_exp_f32 v133, v133\n"
  "v_mfma_f32_16x16x32_bf16 v[36:39], a[100:103], v[160:163], v[36:39]\n"
  "v_exp_f32 v134, v134\n"
  "v_mfma_f32_16x16x32_bf16 v[40:43], a[104:107], v[160:163], v[40:43]\n"
  "s_waitcnt vmcnt(12)\n"
  "s_barrier\n"
  "v_mfma_f32_16x16x32_bf16 v[44:47], a[108:111], v[160:163], v[44:47]\n"
  "v_cvt_pk_bf16_f32 v169, v170, v171\n"
  "v_cvt_pk_bf16_f32 v170, v172, v173\n"
  "ds_read_b128 a[64:67], v218 offset:49152\n"
  "v_mfma_f32_16x16x32_bf16 v[48:51], a[112:115], v[160:163], v[48:51]\n"
  "v_cvt_pk_bf16_f32 v171, v174, v175\n"
  "ds_read_b128 a[80:83], v219 offset:49152\n"
  "ds_read_b128 a[68:71], v218 offset:49664\n"
  "v_mfma_f32_16x16x32_bf16 v[52:55], a[116:119], v[160:163], v[52:55]\n"
  "ds_read_b128 a[84:87], v219 offset:49664\n"
  "ds_read_b128 a[72:75], v218 offset:57344\n"
  "v_mfma_f32_16x16x32_bf16 v[56:59], a[120:123], v[160:163], v[56:59]\n"
  "ds_read_b128 a[88:91], v219 offset:57344\n"
  "ds_read_b128 a[76:79], v218 offset:57856\n"
  "v_mfma_f32_16x16x32_bf16 v[60:63], a[124:127], v[160:163], v[60:63]\n"
  "ds_read_b128 a[92:95], v219 offset:57856\n"
  "v_cvt_pk_bf16_f32 v128, v128, v129\n"
  "v_mfma_f32_16x16x32_bf16 v[64:67], a[96:99], v[168:171], v[64:67]\n"
  "v_exp_f32 v135, v135\n"
  "v_mfma_f32_16x16x32_bf16 a[40:43], a[128:131], v[168:171], a[40:43]\n"
  "s_add_u32 m0, s65, 0x1c000\n"
  "s_mov_b32 s67, 0xc000\n"
  "v_mfma_f32_16x16x32_bf16 v[68:71], a[100:103], v[168:171], v[68:71]\n"
  "buffer_load_dwordx4 v234, s[44:47], s67 offen nt lds\n"
  "ds_read_b64_tr_b16 a[96:97], v220 offset:16384\n"
  "v_mfma_f32_16x16x32_bf16 v[72:75], a[104:107], v[168:171], v[72:75]\n"
  "v_exp_f32 v176, v176\n"
  "v_mfma_f32_16x16x32_bf16 v[76:79], a[108:111], v[168:171], v[76:79]\n"
  "v_exp_f32 v177, v177\n"
  "v_mfma_f32_16x16x32_bf16 v[80:83], a[112:115], v[168:171], v[80:83]\n"
  "v_exp_f32 v178, v178\n"
  "v_mfma_f32_16x16x32_bf16 v[84:87], a[116:119], v[168:171], v[84:87]\n"
  "v_exp_f32 v179, v179\n"
  "v_mfma_f32_16x16x32_bf16 v[88:91], a[120:123], v[168:171], v[88:91]\n"
  "v_exp_f32 v136, v136\n"
  "v_mfma_f32_16x16x32_bf16 v[92:95], a[124:127], v[168:171], v[92:95]\n"
  "s_waitcnt lgkmcnt(1)\n"
  "ds_read_b64_tr_b16 a[98:99], v221 offset:16384\n"
  "v_mfma_f32_16x16x32_bf16 v[152:155], a[64:67], v[200:203], v[184:187]\n"
  "v_exp_f32 v137, v137\n"
  "v_mfma_f32_16x16x32_bf16 v[156:159], a[80:83], v[200:203], v[184:187]\n"
  "v_exp_f32 v138, v138\n"
  "s_nop 2\n"
  "v_mfma_f32_16x16x32_bf16 v[152:155], a[68:71], v[204:207], v[152:155]\n"
  "v_exp_f32 v139, v139\n"
  "v_mfma_f32_16x16x32_bf16 v[156:159], a[84:87], v[204:207], v[156:159]\n"
  "v_exp_f32 v140, v140\n"
  "v_mfma_f32_16x16x32_bf16 v[152:155], a[72:75], v[208:211], v[152:155]\n"
  "buffer_load_dwordx4 v235, s[44:47], s67 offen offset:1024 nt lds\n"
  "ds_read_b64_tr_b16 a[100:101], v230 offset:16384\n"
  "v_mfma_f32_16x16x32_bf16 v[156:159], a[88:91], v[208:211], v[156:159]\n"
  "v_exp_f32 v141, v141\n"
  "v_mfma_f32_16x16x32_bf16 v[152:155], a[76:79], v[212:215], v[152:155]\n"
  "v_exp_f32 v142, v142\n"
  "v_mfma_f32_16x16x32_bf16 v[156:159], a[92:95], v[212:215], v[156:159]\n"
  "v_exp_f32 v143, v143\n"
  "v_mfma_f32_16x16x32_bf16 v[160:163], a[64:67], v[112:115], v[188:191]\n"
  "ds_read_b64_tr_b16 a[102:103], v231 offset:16384\n"
  "ds_read_b64_tr_b16 a[104:105], v220 offset:16896\n"
  "v_mfma_f32_16x16x32_bf16 v[164:167], a[80:83], v[112:115], v[188:191]\n"
  "ds_read_b64_tr_b16 a[106:107], v221 offset:16896\n"
  "ds_read_b64_tr_b16 a[108:109], v230 offset:16896\n"
  "s_nop 2\n"
  "v_mfma_f32_16x16x32_bf16 v[160:163], a[68:71], v[116:119], v[160:163]\n"
  "s_barrier\n"
  "ds_read_b64_tr_b16 a[110:111], v231 offset:16896\n"
  "v_mfma_f32_16x16x32_bf16 v[164:167], a[84:87], v[116:119], v[164:167]\n"
  "ds_read_b64_tr_b16 a[112:113], v220 offset:24576\n"
  "ds_read_b64_tr_b16 a[114:115], v221 offset:24576\n"
  "ds_read_b64_tr_b16 a[116:117], v230 offset:24576\n"
  "v_mfma_f32_16x16x32_bf16 v[160:163], a[72:75], v[120:123], v[160:163]\n"
  "ds_read_b64_tr_b16 a[118:119], v231 offset:24576\n"
  "ds_read_b64_tr_b16 a[120:121], v220 offset:25088\n"
  "v_mfma_f32_16x16x32_bf16 v[164:167], a[88:91], v[120:123], v[164:167]\n"
  "ds_read_b64_tr_b16 a[122:123], v221 offset:25088\n"
  "ds_read_b64_tr_b16 a[124:125], v230 offset:25088\n"
  "v_mfma_f32_16x16x32_bf16 v[160:163], a[76:79], v[124:127], v[160:163]\n"
  "ds_read_b64_tr_b16 a[126:127], v231 offset:25088\n"
  "buffer_load_dwordx4 v236, s[44:47], s67 offen offset:2048 nt lds\n"
  "v_mfma_f32_16x16x32_bf16 v[164:167], a[92:95], v[124:127], v[164:167]\n"
  "v_exp_f32 v144, v144\n"
  "v_mfma_f32_16x16x32_bf16 v[168:171], a[64:67], v[96:99], v[192:195]\n"
  "v_exp_f32 v145, v145\n"
  "v_mfma_f32_16x16x32_bf16 v[172:175], a[80:83], v[96:99], v[192:195]\n"
  "v_exp_f32 v146, v146\n"
  "s_nop 2\n"
  "v_mfma_f32_16x16x32_bf16 v[168:171], a[68:71], v[100:103], v[168:171]\n"
  "v_exp_f32 v147, v147\n"
  "v_mfma_f32_16x16x32_bf16 v[172:175], a[84:87], v[100:103], v[172:175]\n"
  "v_cvt_pk_bf16_f32 v129, v130, v131\n"
  "v_cvt_pk_bf16_f32 v130, v132, v133\n"
  "v_mfma_f32_16x16x32_bf16 v[168:171], a[72:75], v[104:107], v[168:171]\n"
  "v_cvt_pk_bf16_f32 v131, v134, v135\n"
  "v_cvt_pk_bf16_f32 v176, v176, v177\n"
  "v_mfma_f32_16x16x32_bf16 v[172:175], a[88:91], v[104:107], v[172:175]\n"
  "v_exp_f32 v148, v148\n"
  "v_mfma_f32_16x16x32_bf16 v[168:171], a[76:79], v[108:111], v[168:171]\n"
  "v_exp_f32 v149, v149\n"
  "v_mfma_f32_16x16x32_bf16 v[172:175], a[92:95], v[108:111], v[172:175]\n"
  "s_waitcnt lgkmcnt(0)\n"
  "v_cvt_pk_bf16_f32 v177, v178, v179\n"
  "v_mfma_f32_16x16x32_bf16 v[0:3], a[96:99], v[128:131], v[0:3]\n"
  "v_exp_f32 v150, v150\n"
  "v_mfma_f32_16x16x32_bf16 a[32:35], a[128:131], v[128:131], a[32:35]\n"
  "v_exp_f32 v151, v151\n"
  "s_nop 2\n"
  "v_mfma_f32_16x16x32_bf16 v[4:7], a[100:103], v[128:131], v[4:7]\n"
  "buffer_load_dwordx4 v237, s[44:47], s67 offen offset:3072 nt lds\n"
  "v_cvt_pk_bf16_f32 v136, v136, v137\n"
  "v_mfma_f32_16x16x32_bf16 v[8:11], a[104:107], v[128:131], v[8:11]\n"
  "v_exp_f32 v152, v152\n"
  "v_mfma_f32_16x16x32_bf16 v[12:15], a[108:111], v[128:131], v[12:15]\n"
  "v_exp_f32 v153, v153\n"
  "v_mfma_f32_16x16x32_bf16 v[16:19], a[112:115], v[128:131], v[16:19]\n"
  "v_exp_f32 v154, v154\n"
  "v_mfma_f32_16x16x32_bf16 v[20:23], a[116:119], v[128:131], v[20:23]\n"
  "v_exp_f32 v155, v155\n"
  "v_mfma_f32_16x16x32_bf16 v[24:27], a[120:123], v[128:131], v[24:27]\n"
  "v_exp_f32 v156, v156\n"
  "v_mfma_f32_16x16x32_bf16 v[28:31], a[124:127], v[128:131], v[28:31]\n"
  "v_exp_f32 v157, v157\n"
  "v_mfma_f32_16x16x16_bf16 a[0:3], a[96:97], v[176:177], a[0:3]\n"
  "v_exp_f32 v158, v158\n"
  "s_nop 2\n"
  "v_mfma_f32_16x16x16_bf16 a[4:7], a[100:101], v[176:177], a[4:7]\n"
  "v_exp_f32 v159, v159\n"
  "v_mfma_f32_16x16x16_bf16 a[8:11], a[104:105], v[176:177], a[8:11]\n"
  "v_exp_f32 v160, v160\n"
  "v_mfma_f32_16x16x16_bf16 a[12:15], a[108:109], v[176:177], a[12:15]\n"
  "s_add_u32 m0, s65, 0x20000\n"
  "s_mov_b32 s66, 0x10000\n"
  "v_mfma_f32_16x16x16_bf16 a[16:19], a[112:113], v[176:177], a[16:19]\n"
  "v_cvt_pk_bf16_f32 v137, v138, v139\n"
  "buffer_load_dwordx4 v234, s[44:47], s66 offen nt lds\n"
  "v_cvt_pk_bf16_f32 v138, v140, v141\n"
  "v_mfma_f32_16x16x16_bf16 a[20:23], a[116:117], v[176:177], a[20:23]\n"
  "v_cvt_pk_bf16_f32 v139, v142, v143\n"
  "v_cvt_pk_bf16_f32 v144, v144, v145\n"
  "v_mfma_f32_16x16x16_bf16 a[24:27], a[120:121], v[176:177], a[24:27]\n"
  "v_exp_f32 v161, v161\n"
  "v_mfma_f32_16x16x16_bf16 a[28:31], a[124:125], v[176:177], a[28:31]\n"
  "v_exp_f32 v162, v162\n"
  "v_mfma_f32_16x16x16_bf16 a[44:47], a[128:129], v[176:177], a[44:47]\n"
  "v_exp_f32 v163, v163\n"
  "s_nop 2\n"
  "v_mfma_f32_16x16x32_bf16 v[32:35], a[96:99], v[136:139], v[32:35]\n"
  "v_exp_f32 v164, v164\n"
  "v_mfma_f32_16x16x32_bf16 a[36:39], a[128:131], v[136:139], a[36:39]\n"
  "v_cvt_pk_bf16_f32 v145, v146, v147\n"
  "v_cvt_pk_bf16_f32 v146, v148, v149\n"
  "v_mfma_f32_16x16x32_bf16 v[36:39], a[100:103], v[136:139], v[36:39]\n"
  "v_exp_f32 v165, v165\n"
  "v_mfma_f32_16x16x32_bf16 v[40:43], a[104:107], v[136:139], v[40:43]\n"
  "ds_read_b128 a[64:67], v216\n"
  "v_cvt_pk_bf16_f32 v147, v150, v151\n"
  "v_mfma_f32_16x16x32_bf16 v[44:47], a[108:111], v[136:139], v[44:47]\n"
  "v_exp_f32 v166, v166\n"
  "v_mfma_f32_16x16x32_bf16 v[48:51], a[112:115], v[136:139], v[48:51]\n"
  "ds_read_b128 a[80:83], v217\n"
  "ds_read_b128 a[68:71], v216 offset:512\n"
  "v_mfma_f32_16x16x32_bf16 v[52:55], a[116:119], v[136:139], v[52:55]\n"
  "ds_read_b128 a[84:87], v217 offset:512\n"
  "ds_read_b128 a[72:75], v216 offset:8192\n"
  "v_mfma_f32_16x16x32_bf16 v[56:59], a[120:123], v[136:139], v[56:59]\n"
  "ds_read_b128 a[88:91], v217 offset:8192\n"
  "ds_read_b128 a[76:79], v216 offset:8704\n"
  "s_nop 2\n"
  "v_mfma_f32_16x16x32_bf16 v[60:63], a[124:127], v[136:139], v[60:63]\n"
  "ds_read_b128 a[92:95], v217 offset:8704\n"
  "v_cvt_pk_bf16_f32 v152, v152, v153\n"
  "v_mfma_f32_16x16x32_bf16 v[64:67], a[96:99], v[144:147], v[64:67]\n"
  "v_exp_f32 v167, v167\n"
  "v_mfma_f32_16x16x32_bf16 a[40:43], a[128:131], v[144:147], a[40:43]\n"
  "v_exp_f32 v168, v168\n"
  "v_mfma_f32_16x16x32_bf16 v[68:71], a[100:103], v[144:147], v[68:71]\n"
  "buffer_load_dwordx4 v235, s[44:47], s66 offen offset:1024 nt lds\n"
  "ds_read_b64_tr_b16 a[96:97], v228 offset:16384\n"
  "v_mfma_f32_16x16x32_bf16 v[72:75], a[104:107], v[144:147], v[72:75]\n"
  "v_exp_f32 v169, v169\n"
  "v_mfma_f32_16x16x32_bf16 v[76:79], a[108:111], v[144:147], v[76:79]\n"
  "ds_read_b64_tr_b16 a[98:99], v229 offset:16384\n"
  "ds_read_b64_tr_b16 a[100:101], v232 offset:16384\n"
  "v_mfma_f32_16x16x32_bf16 v[80:83], a[112:115], v[144:147], v[80:83]\n"
  "ds_read_b64_tr_b16 a[102:103], v233 offset:16384\n"
  "ds_read_b64_tr_b16 a[104:105], v228 offset:16896\n"
  "v_mfma_f32_16x16x32_bf16 v[84:87], a[116:119], v[144:147], v[84:87]\n"
  "v_exp_f32 v170, v170\n"
  "s_nop 2\n"
  "v_mfma_f32_16x16x32_bf16 v[88:91], a[120:123], v[144:147], v[88:91]\n"
  "ds_read_b64_tr_b16 a[106:107], v229 offset:16896\n"
  "ds_read_b64_tr_b16 a[108:109], v232 offset:16896\n"
  "v_mfma_f32_16x16x32_bf16 v[92:95], a[124:127], v[144:147], v[92:95]\n"
  "s_waitcnt lgkmcnt(7)\n"
  "ds_read_b64_tr_b16 a[110:111], v233 offset:16896\n"
  "v_mfma_f32_16x16x32_bf16 v[128:131], a[64:67], v[200:203], v[184:187]\n"
  "v_exp_f32 v171, v171\n"
  "v_mfma_f32_16x16x32_bf16 v[132:135], a[80:83], v[200:203], v[184:187]\n"
  "ds_read_b64_tr_b16 a[112:113], v228 offset:24576\n"
  "ds_read_b64_tr_b16 a[114:115], v229 offset:24576\n"
  "v_mfma_f32_16x16x32_bf16 v[128:131], a[68:71], v[204:207], v[128:131]\n"
  "v_exp_f32 v172, v172\n"
  "v_mfma_f32_16x16x32_bf16 v[132:135], a[84:87], v[204:207], v[132:135]\n"
  "ds_read_b64_tr_b16 a[116:117], v232 offset:24576\n"
  "ds_read_b64_tr_b16 a[118:119], v233 offset:24576\n"
  "v_mfma_f32_16x16x32_bf16 v[128:131], a[72:75], v[208:211], v[128:131]\n"
  "v_exp_f32 v173, v173\n"
  "v_mfma_f32_16x16x32_bf16 v[132:135], a[88:91], v[208:211], v[132:135]\n"
  "ds_read_b64_tr_b16 a[120:121], v228 offset:25088\n"
  "ds_read_b64_tr_b16 a[122:123], v229 offset:25088\n"
  "s_nop 2\n"
  "v_mfma_f32_16x16x32_bf16 v[128:131], a[76:79], v[212:215], v[128:131]\n"
  "ds_read_b64_tr_b16 a[124:125], v232 offset:25088\n"
  "ds_read_b64_tr_b16 a[126:127], v233 offset:25088\n"
  "v_mfma_f32_16x16x32_bf16 v[132:135], a[92:95], v[212:215], v[132:135]\n"
  "v_exp_f32 v174, v174\n"
  "v_mfma_f32_16x16x32_bf16 v[176:179], a[64:67], a[48:51], v[196:199]\n"
  "buffer_load_dwordx4 v236, s[44:47], s66 offen offset:2048 nt lds\n"
  "v_cvt_pk_bf16_f32 v153, v154, v155\n"
  "v_mfma_f32_16x16x32_bf16 v[176:179], a[68:71], a[52:55], v[176:179]\n"
  "v_exp_f32 v175, v175\n"
  "v_mfma_f32_16x16x32_bf16 v[176:179], a[72:75], a[56:59], v[176:179]\n"
  "v_cvt_pk_bf16_f32 v154, v156, v157\n"
  "v_cvt_pk_bf16_f32 v155, v158, v159\n"
  "v_mfma_f32_16x16x32_bf16 v[176:179], a[76:79], a[60:63], v[176:179]\n"
  "v_exp_f32 v128, v128\n"
  "v_mfma_f32_16x16x32_bf16 v[136:139], a[64:67], v[112:115], v[188:191]\n"
  "v_cvt_pk_bf16_f32 v160, v160, v161\n"
  "v_cvt_pk_bf16_f32 v161, v162, v163\n"
  "v_mfma_f32_16x16x32_bf16 v[140:143], a[80:83], v[112:115], v[188:191]\n"
  "v_exp_f32 v129, v129\n"
  "s_nop 2\n"
  "v_mfma_f32_16x16x32_bf16 v[136:139], a[68:71], v[116:119], v[136:139]\n"
  "v_cvt_pk_bf16_f32 v162, v164, v165\n"
  "v_cvt_pk_bf16_f32 v163, v166, v167\n"
  "v_mfma_f32_16x16x32_bf16 v[140:143], a[84:87], v[116:119], v[140:143]\n"
  "v_exp_f32 v130, v130\n"
  "v_mfma_f32_16x16x32_bf16 v[136:139], a[72:75], v[120:123], v[136:139]\n"
  "v_exp_f32 v176, v176\n"
  "v_mfma_f32_16x16x32_bf16 v[140:143], a[88:91], v[120:123], v[140:143]\n"
  "v_exp_f32 v131, v131\n"
  "v_mfma_f32_16x16x32_bf16 v[136:139], a[76:79], v[124:127], v[136:139]\n"
  "v_cvt_pk_bf16_f32 v168, v168, v169\n"
  "v_cvt_pk_bf16_f32 v169, v170, v171\n"
  "v_mfma_f32_16x16x32_bf16 v[140:143], a[92:95], v[124:127], v[140:143]\n"
  "v_cvt_pk_bf16_f32 v170, v172, v173\n"
  "v_cvt_pk_bf16_f32 v171, v174, v175\n"
  "v_mfma_f32_16x16x32_bf16 v[144:147], a[64:67], v[96:99], v[192:195]\n"
  "ds_read_b128 a[64:67], v218\n"
  "v_cvt_pk_bf16_f32 v128, v128, v129\n"
  "v_mfma_f32_16x16x32_bf16 v[148:151], a[80:83], v[96:99], v[192:195]\n"
  "v_exp_f32 v132, v132\n"
  "s_nop 2\n"
  "v_mfma_f32_16x16x32_bf16 v[144:147], a[68:71], v[100:103], v[144:147]\n"
  "ds_read_b128 a[80:83], v219\n"
  "v_cvt_pk_bf16_f32 v129, v130, v131\n"
  "v_mfma_f32_16x16x32_bf16 v[148:151], a[84:87], v[100:103], v[148:151]\n"
  "v_exp_f32 v133, v133\n"
  "v_mfma_f32_16x16x32_bf16 v[144:147], a[72:75], v[104:107], v[144:147]\n"
  "buffer_load_dwordx4 v237, s[44:47], s66 offen offset:3072 nt lds\n"
  "ds_read_b128 a[68:71], v218 offset:512\n"
  "v_mfma_f32_16x16x32_bf16 v[148:151], a[88:91], v[104:107], v[148:151]\n"
  "v_exp_f32 v134, v134\n"
  "v_mfma_f32_16x16x32_bf16 v[144:147], a[76:79], v[108:111], v[144:147]\n"
  "v_exp_f32 v177, v177\n"
  "v_mfma_f32_16x16x32_bf16 v[148:151], a[92:95], v[108:111], v[148:151]\n"
  "s_waitcnt lgkmcnt(3)\n"
  "ds_read_b128 a[84:87], v219 offset:512\n"
  "v_mfma_f32_16x16x32_bf16 v[0:3], a[96:99], v[152:155], v[0:3]\n"
  "v_exp_f32 v135, v135\n"
  "v_mfma_f32_16x16x32_bf16 a[32:35], a[128:131], v[152:155], a[32:35]\n"
  "ds_read_b128 a[72:75], v218 offset:8192\n"
  "v_cvt_pk_bf16_f32 v130, v132, v133\n"
  "s_nop 2\n"
  "v_mfma_f32_16x16x32_bf16 v[4:7], a[100:103], v[152:155], v[4:7]\n"
  "v_exp_f32 v136, v136\n"
  "v_mfma_f32_16x16x32_bf16 v[8:11], a[104:107], v[152:155], v[8:11]\n"
  "ds_read_b128 a[88:91], v219 offset:8192\n"
  "v_cvt_pk_bf16_f32 v131, v134, v135\n"
  "v_mfma_f32_16x16x32_bf16 v[12:15], a[108:111], v[152:155], v[12:15]\n"
  "v_exp_f32 v137, v137\n"
  "v_mfma_f32_16x16x32_bf16 v[16:19], a[112:115], v[152:155], v[16:19]\n"
  "ds_read_b128 a[76:79], v218 offset:8704\n"
  "v_cvt_pk_bf16_f32 v136, v136, v137\n"
  "v_mfma_f32_16x16x32_bf16 v[20:23], a[116:119], v[152:155], v[20:23]\n"
  "v_exp_f32 v178, v178\n"
  "v_mfma_f32_16x16x32_bf16 v[24:27], a[120:123], v[152:155], v[24:27]\n"
  "v_exp_f32 v138, v138\n"
  "v_mfma_f32_16x16x32_bf16 v[28:31], a[124:127], v[152:155], v[28:31]\n"
  "v_exp_f32 v139, v139\n"
  "v_mfma_f32_16x16x32_bf16 v[32:35], a[96:99], v[160:163], v[32:35]\n"
  "ds_read_b128 a[92:95], v219 offset:8704\n"
  "v_cvt_pk_bf16_f32 v137, v138, v139\n"
  "s_nop 2\n"
  "v_mfma_f32_16x16x32_bf16 a[36:39], a[128:131], v[160:163], a[36:39]\n"
  "v_exp_f32 v140, v140\n"
  "v_mfma_f32_16x16x32_bf16 v[36:39], a[100:103], v[160:163], v[36:39]\n"
  "s_add_u32 m0, s65, 0\n"
  "s_mov_b32 s67, 0x10000\n"
  "v_mfma_f32_16x16x32_bf16 v[40:43], a[104:107], v[160:163], v[40:43]\n"
  "s_waitcnt vmcnt(16)\n"
  "s_barrier\n"
  "v_mfma_f32_16x16x32_bf16 v[44:47], a[108:111], v[160:163], v[44:47]\n"
  "buffer_load_dwordx4 v238, s[48:51], s67 offen nt lds\n"
  "v_mfma_f32_16x16x32_bf16 v[48:51], a[112:115], v[160:163], v[48:51]\n"
  "v_exp_f32 v179, v179\n"
  "v_mfma_f32_16x16x32_bf16 v[52:55], a[116:119], v[160:163], v[52:55]\n"
  "v_exp_f32 v141, v141\n"
  "v_mfma_f32_16x16x32_bf16 v[56:59], a[120:123], v[160:163], v[56:59]\n"
  "v_exp_f32 v142, v142\n"
  "v_mfma_f32_16x16x32_bf16 v[60:63], a[124:127], v[160:163], v[60:63]\n"
  "v_cvt_pk_bf16_f32 v176, v176, v177\n"
  "v_cvt_pk_bf16_f32 v177, v178, v179\n"
  "s_nop 2\n"
  "v_mfma_f32_16x16x32_bf16 v[64:67], a[96:99], v[168:171], v[64:67]\n"
  "v_exp_f32 v143, v143\n"
  "v_mfma_f32_16x16x32_bf16 a[40:43], a[128:131], v[168:171], a[40:43]\n"
  "ds_read_b64_tr_b16 a[96:97], v220 offset:32768\n"
  "ds_read_b64_tr_b16 a[98:99], v221 offset:32768\n"
  "v_mfma_f32_16x16x32_bf16 v[68:71], a[100:103], v[168:171], v[68:71]\n"
  "v_exp_f32 v144, v144\n"
  "v_mfma_f32_16x16x32_bf16 v[72:75], a[104:107], v[168:171], v[72:75]\n"
  "ds_read_b64_tr_b16 a[100:101], v230 offset:32768\n"
  "ds_read_b64_tr_b16 a[102:103], v231 offset:32768\n"
  "v_mfma_f32_16x16x32_bf16 v[76:79], a[108:111], v[168:171], v[76:79]\n"
  "v_exp_f32 v145, v145\n"
  "v_mfma_f32_16x16x32_bf16 v[80:83], a[112:115], v[168:171], v[80:83]\n"
  "ds_read_b64_tr_b16 a[104:105], v220 offset:33280\n"
  "ds_read_b64_tr_b16 a[106:107], v221 offset:33280\n"
  "v_mfma_f32_16x16x32_bf16 v[84:87], a[116:119], v[168:171], v[84:87]\n"
  "ds_read_b64_tr_b16 a[108:109], v230 offset:33280\n"
  "ds_read_b64_tr_b16 a[110:111], v231 offset:33280\n"
  "v_mfma_f32_16x16x32_bf16 v[88:91], a[120:123], v[168:171], v[88:91]\n"
  "v_exp_f32 v146, v146\n"
  "s_nop 2\n"
  "v_mfma_f32_16x16x32_bf16 v[92:95], a[124:127], v[168:171], v[92:95]\n"
  "s_waitcnt lgkmcnt(8)\n"
  "ds_read_b64_tr_b16 a[112:113], v220 offset:40960\n"
  "v_mfma_f32_16x16x32_bf16 v[152:155], a[64:67], v[200:203], v[184:187]\n"
  "L_st3:\n"
  "s_cbranch_execz L_st7\n"
  "v_exp_f32 v147, v147\n"
  "v_mfma_f32_16x16x32_bf16 v[156:159], a[80:83], v[200:203], v[184:187]\n"
  "buffer_load_dwordx4 v239, s[48:51], s67 offen offset:1024 nt lds\n"
  "ds_read_b64_tr_b16 a[114:115], v221 offset:40960\n"
  "v_mfma_f32_16x16x32_bf16 v[152:155], a[68:71], v[204:207], v[152:155]\n"
  "v_exp_f32 v148, v148\n"
  "v_mfma_f32_16x16x32_bf16 v[156:159], a[84:87], v[204:207], v[156:159]\n"
  "ds_read_b64_tr_b16 a[116:117], v230 offset:40960\n"
  "ds_read_b64_tr_b16 a[118:119], v231 offset:40960\n"
  "v_mfma_f32_16x16x32_bf16 v[152:155], a[72:75], v[208:211], v[152:155]\n"
  "ds_read_b64_tr_b16 a[120:121], v220 offset:41472\n"
  "ds_read_b64_tr_b16 a[122:123], v221 offset:41472\n"
  "v_mfma_f32_16x16x32_bf16 v[156:159], a[88:91], v[208:211], v[156:159]\n"
  "v_exp_f32 v149, v149\n"
  "v_mfma_f32_16x16x32_bf16 v[152:155], a[76:79], v[212:215], v[152:155]\n"
  "ds_read_b64_tr_b16 a[124:125], v230 offset:41472\n"
  "ds_read_b64_tr_b16 a[126:127], v231 offset:41472\n"
  "s_nop 2\n"
  "v_mfma_f32_16x16x32_bf16 v[156:159], a[92:95], v[212:215], v[156:159]\n"
  "v_exp_f32 v150, v150\n"
  "v_mfma_f32_16x16x32_bf16 v[160:163], a[64:67], v[112:115], v[188:191]\n"
  "v_exp_f32 v151, v151\n"
  "v_mfma_f32_16x16x32_bf16 v[164:167], a[80:83], v[112:115], v[188:191]\n"
  "v_cvt_pk_bf16_f32 v138, v140, v141\n"
  "v_cvt_pk_bf16_f32 v139, v142, v143\n"
  "v_mfma_f32_16x16x32_bf16 v[160:163], a[68:71], v[116:119], v[160:163]\n"
  "s_waitcnt vmcnt(6)\n"
  "s_barrier\n"
  "v_mfma_f32_16x16x32_bf16 v[164:167], a[84:87], v[116:119], v[164:167]\n"
  "s_lshr_b32 s64, s57, 2\n"
  "s_lshl_b32 s58, s56, 2\n"
  "s_or_b32 s58, s58, s88\n"
  "s_mul_i32 s58, s58, 0x68000\n"
  "s_add_u32 s58, s58, 0x1000000\n"
  "s_mul_i32 s60, s64, 0x1a00\n"
  "s_add_u32 s61, s58, s60\n"
  "s_sub_u32 s60, 0x68000, s60\n"
  "s_add_u32 s36, s30, s61\n"
  "s_addc_u32 s37, s31, 0\n"
  "s_and_b32 s37, s37, 0xffff\n"
  "s_mov_b32 s38, s60\n"
  "s_mov_b32 s39, 0x20000\n"
  "v_mfma_f32_16x16x32_bf16 v[160:163], a[72:75], v[120:123], v[160:163]\n"
  "v_exp_f32 v152, v152\n"
  "v_mfma_f32_16x16x32_bf16 v[164:167], a[88:91], v[120:123], v[164:167]\n"
  "v_exp_f32 v153, v153\n"
  "v_mfma_f32_16x16x32_bf16 v[160:163], a[76:79], v[124:127], v[160:163]\n"
  "s_mul_i32 s60, s64, 0xd000\n"
  "s_add_u32 s61, s58, s60\n"
  "s_add_u32 s40, s30, s61\n"
  "s_addc_u32 s41, s31, 0\n"
  "s_and_b32 s41, s41, 0xffff\n"
  "s_mov_b32 s42, 0xd000\n"
  "s_mov_b32 s43, 0x20000\n"
  "s_nop 2\n"
  "v_mfma_f32_16x16x32_bf16 v[164:167], a[92:95], v[124:127], v[164:167]\n"
  "v_cvt_pk_bf16_f32 v144, v144, v145\n"
  "v_cvt_pk_bf16_f32 v145, v146, v147\n"
  "v_mfma_f32_16x16x32_bf16 v[168:171], a[64:67], v[96:99], v[192:195]\n"
  "buffer_load_dwordx4 v238, s[48:51], s67 offen offset:2048 nt lds\n"
  "v_cvt_pk_bf16_f32 v146, v148, v149\n"
  "v_mfma_f32_16x16x32_bf16 v[172:175], a[80:83], v[96:99], v[192:195]\n"
  "v_exp_f32 v154, v154\n"
  "v_mfma_f32_16x16x32_bf16 v[168:171], a[68:71], v[100:103], v[168:171]\n"
  "ds_read_b128 a[64:67], v216 offset:16384\n"
  "v_cvt_pk_bf16_f32 v147, v150, v151\n"
  "v_mfma_f32_16x16x32_bf16 v[172:175], a[84:87], v[100:103], v[172:175]\n"
  "v_exp_f32 v155, v155\n"
  "v_mfma_f32_16x16x32_bf16 v[168:171], a[72:75], v[104:107], v[168:171]\n"
  "ds_read_b128 a[80:83], v217 offset:16384\n"
  "v_cvt_pk_bf16_f32 v152, v152, v153\n"
  "v_mfma_f32_16x16x32_bf16 v[172:175], a[88:91], v[104:107], v[172:175]\n"
  "v_cvt_pk_bf16_f32 v153, v154, v155\n"
  "s_lshl_b32 s58, 1, s57\n"
  "v_mfma_f32_16x16x32_bf16 v[168:171], a[76:79], v[108:111], v[168:171]\n"
  "v_exp_f32 v156, v156\n"
  "s_nop 2\n"
  "v_mfma_f32_16x16x32_bf16 v[172:175], a[92:95], v[108:111], v[172:175]\n"
  "s_waitcnt lgkmcnt(2)\n"
  "ds_read_b128 a[68:71], v216 offset:16896\n"
  "v_mfma_f32_16x16x32_bf16 v[0:3], a[96:99], v[128:131], v[0:3]\n"
  "v_exp_f32 v157, v157\n"
  "v_mfma_f32_16x16x32_bf16 a[32:35], a[128:131], v[128:131], a[32:35]\n"
  "ds_read_b128 a[84:87], v217 offset:16896\n"
  "v_cvt_pk_bf16_f32 v154, v156, v157\n"
  "v_mfma_f32_16x16x32_bf16 v[4:7], a[100:103], v[128:131], v[4:7]\n"
  "s_and_b32 s59, s57, 28\n"
  "s_lshl_b32 s70, 15, s59\n"
  "v_mfma_f32_16x16x32_bf16 v[8:11], a[104:107], v[128:131], v[8:11]\n"
  "v_exp_f32 v158, v158\n"
  "v_mfma_f32_16x16x32_bf16 v[12:15], a[108:111], v[128:131], v[12:15]\n"
  "ds_read_b128 a[72:75], v216 offset:24576\n"
  "s_xor_b32 s70, s70, s58\n"
  "v_mfma_f32_16x16x32_bf16 v[16:19], a[112:115], v[128:131], v[16:19]\n"
  "v_exp_f32 v159, v159\n"
  "v_mfma_f32_16x16x32_bf16 v[20:23], a[116:119], v[128:131], v[20:23]\n"
  "ds_read_b128 a[88:91], v217 offset:24576\n"
  "v_cvt_pk_bf16_f32 v155, v158, v159\n"
  "s_nop 2\n"
  "v_mfma_f32_16x16x32_bf16 v[24:27], a[120:123], v[128:131], v[24:27]\n"
  "s_lshl_b32 s71, 0x11111111, s88\n"
  "s_xor_b32 s71, s71, s58\n"
  "v_mfma_f32_16x16x32_bf16 v[28:31], a[124:127], v[128:131], v[28:31]\n"
  "v_exp_f32 v160, v160\n"
  "v_mfma_f32_16x16x16_bf16 a[0:3], a[96:97], v[176:177], a[0:3]\n"
  "buffer_load_dwordx4 v239, s[48:51], s67 offen offset:3072 nt lds\n"
  "ds_read_b128 a[76:79], v216 offset:25088\n"
  "v_mfma_f32_16x16x16_bf16 a[4:7], a[100:101], v[176:177], a[4:7]\n"
  "v_exp_f32 v161, v161\n"
  "v_mfma_f32_16x16x16_bf16 a[8:11], a[104:105], v[176:177], a[8:11]\n"
  "ds_read_b128 a[92:95], v217 offset:25088\n"
  "v_cvt_pk_bf16_f32 v160, v160, v161\n"
  "v_mfma_f32_16x16x16_bf16 a[12:15], a[108:109], v[176:177], a[12:15]\n"
  "s_add_u32 s20, s88, 0\n"
  "s_and_b32 s20, s20, 3\n"
  "v_mfma_f32_16x16x16_bf16 a[16:19], a[112:113], v[176:177], a[16:19]\n"
  "v_exp_f32 v162, v162\n"
  "v_mfma_f32_16x16x16_bf16 a[20:23], a[116:117], v[176:177], a[20:23]\n"
  "s_mul_i32 s20, s20, 0xd000\n"
  "s_add_u32 s21, s88, 1\n"
  "s_nop 2\n"
  "v_mfma_f32_16x16x16_bf16 a[24:27], a[120:121], v[176:177], a[24:27]\n"
  "v_exp_f32 v163, v163\n"
  "v_mfma_f32_16x16x16_bf16 a[28:31], a[124:125], v[176:177], a[28:31]\n"
  "v_cvt_pk_bf16_f32 v161, v162, v163\n"
  "s_and_b32 s21, s21, 3\n"
  "v_mfma_f32_16x16x16_bf16 a[44:47], a[128:129], v[176:177], a[44:47]\n"
  "s_mul_i32 s21, s21, 0xd000\n"
  "s_add_u32 s22, s88, 2\n"
  "v_mfma_f32_16x16x32_bf16 v[32:35], a[96:99], v[136:139], v[32:35]\n"
  "v_exp_f32 v164, v164\n"
  "v_mfma_f32_16x16x32_bf16 a[36:39], a[128:131], v[136:139], a[36:39]\n"
  "s_and_b32 s22, s22, 3\n"
  "s_mul_i32 s22, s22, 0xd000\n"
  "v_mfma_f32_16x16x32_bf16 v[36:39], a[100:103], v[136:139], v[36:39]\n"
  "v_exp_f32 v165, v165\n"
  "v_mfma_f32_16x16x32_bf16 v[40:43], a[104:107], v[136:139], v[40:43]\n"
  "v_cvt_pk_bf16_f32 v162, v164, v165\n"
  "s_add_u32 s23, s88, 3\n"
  "v_mfma_f32_16x16x32_bf16 v[44:47], a[108:111], v[136:139], v[44:47]\n"
  "s_and_b32 s23, s23, 3\n"
  "s_mul_i32 s23, s23, 0xd000\n"
  "s_nop 2\n"
  "v_mfma_f32_16x16x32_bf16 v[48:51], a[112:115], v[136:139], v[48:51]\n"
  "v_exp_f32 v166, v166\n"
  "v_mfma_f32_16x16x32_bf16 v[52:55], a[116:119], v[136:139], v[52:55]\n"
  "s_add_u32 m0, s65, 0x24000\n"
  "s_mov_b32 s66, 0x14000\n"
  "v_mfma_f32_16x16x32_bf16 v[56:59], a[120:123], v[136:139], v[56:59]\n"
  "v_exp_f32 v167, v167\n"
  "v_mfma_f32_16x16x32_bf16 v[60:63], a[124:127], v[136:139], v[60:63]\n"
  "buffer_load_dwordx4 v234, s[44:47], s66 offen nt lds\n"
  "v_cvt_pk_bf16_f32 v163, v166, v167\n"
  "v_mfma_f32_16x16x32_bf16 v[64:67], a[96:99], v[144:147], v[64:67]\n"
  "ds_read_b64_tr_b16 a[96:97], v228 offset:32768\n"
  "ds_read_b64_tr_b16 a[98:99], v229 offset:32768\n"
  "v_mfma_f32_16x16x32_bf16 a[40:43], a[128:131], v[144:147], a[40:43]\n"
  "v_exp_f32 v168, v168\n"
  "v_mfma_f32_16x16x32_bf16 v[68:71], a[100:103], v[144:147], v[68:71]\n"
  "ds_read_b64_tr_b16 a[100:101], v232 offset:32768\n"
  "ds_read_b64_tr_b16 a[102:103], v233 offset:32768\n"
  "v_mfma_f32_16x16x32_bf16 v[72:75], a[104:107], v[144:147], v[72:75]\n"
  "v_exp_f32 v169, v169\n"
  "s_nop 2\n"
  "v_mfma_f32_16x16x32_bf16 v[76:79], a[108:111], v[144:147], v[76:79]\n"
  "ds_read_b64_tr_b16 a[104:105], v228 offset:33280\n"
  "ds_read_b64_tr_b16 a[106:107], v229 offset:33280\n"
  "v_mfma_f32_16x16x32_bf16 v[80:83], a[112:115], v[144:147], v[80:83]\n"
  "buffer_load_dwordx4 v235, s[44:47], s66 offen offset:1024 nt lds\n"
  "ds_read_b64_tr_b16 a[108:109], v232 offset:33280\n"
  "v_mfma_f32_16x16x32_bf16 v[84:87], a[116:119], v[144:147], v[84:87]\n"
  "v_exp_f32 v170, v170\n"
  "v_mfma_f32_16x16x32_bf16 v[88:91], a[120:123], v[144:147], v[88:91]\n"
  "ds_read_b64_tr_b16 a[110:111], v233 offset:33280\n"
  "ds_read_b64_tr_b16 a[112:113], v228 offset:40960\n"
  "v_mfma_f32_16x16x32_bf16 v[92:95], a[124:127], v[144:147], v[92:95]\n"
  "s_waitcnt lgkmcnt(9)\n"
  "ds_read_b64_tr_b16 a[114:115], v229 offset:40960\n"
  "v_mfma_f32_16x16x32_bf16 v[128:131], a[64:67], v[200:203], v[184:187]\n"
  "v_exp_f32 v171, v171\n"
  "v_mfma_f32_16x16x32_bf16 v[132:135], a[80:83], v[200:203], v[184:187]\n"
  "ds_read_b64_tr_b16 a[116:117], v232 offset:40960\n"
  "ds_read_b64_tr_b16 a[118:119], v233 offset:40960\n"
  "v_mfma_f32_16x16x32_bf16 v[128:131], a[68:71], v[204:207], v[128:131]\n"
  "v_exp_f32 v172, v172\n"
  "s_nop 2\n"
  "v_mfma_f32_16x16x32_bf16 v[132:135], a[84:87], v[204:207], v[132:135]\n"
  "ds_read_b64_tr_b16 a[120:121], v228 offset:41472\n"
  "ds_read_b64_tr_b16 a[122:123], v229 offset:41472\n"
  "v_mfma_f32_16x16x32_bf16 v[128:131], a[72:75], v[208:211], v[128:131]\n"
  "v_exp_f32 v173, v173\n"
  "v_mfma_f32_16x16x32_bf16 v[132:135], a[88:91], v[208:211], v[132:135]\n"
  "buffer_load_dwordx4 v236, s[44:47], s66 offen offset:2048 nt lds\n"
  "ds_read_b64_tr_b16 a[124:125], v232 offset:41472\n"
  "v_mfma_f32_16x16x32_bf16 v[128:131], a[76:79], v[212:215], v[128:131]\n"
  "ds_read_b64_tr_b16 a[126:127], v233 offset:41472\n"
  "v_cvt_pk_bf16_f32 v168, v168, v169\n"
  "v_mfma_f32_16x16x32_bf16 v[132:135], a[92:95], v[212:215], v[132:135]\n"
  "v_exp_f32 v174, v174\n"
  "v_mfma_f32_16x16x32_bf16 v[176:179], a[64:67], a[48:51], v[196:199]\n"
  "v_cvt_pk_bf16_f32 v169, v170, v171\n"
  "v_cvt_pk_bf16_f32 v170, v172, v173\n"
  "v_mfma_f32_16x16x32_bf16 v[176:179], a[68:71], a[52:55], v[176:179]\n"
  "v_exp_f32 v175, v175\n"
  "v_mfma_f32_16x16x32_bf16 v[176:179], a[72:75], a[56:59], v[176:179]\n"
  "v_cvt_pk_bf16_f32 v171, v174, v175\n"
  "s_lshl_b32 s24, s19, 10\n"
  "s_nop 2\n"
  "v_mfma_f32_16x16x32_bf16 v[176:179], a[76:79], a[60:63], v[176:179]\n"
  "v_exp_f32 v128, v128\n"
  "v_mfma_f32_16x16x32_bf16 v[136:139], a[64:67], v[112:115], v[188:191]\n"
  "buffer_load_dwordx4 v237, s[44:47], s66 offen offset:3072 nt lds\n"
  "s_add_u32 s24, s24, 0xc000\n"
  "v_mfma_f32_16x16x32_bf16 v[140:143], a[80:83], v[112:115], v[188:191]\n"
  "v_exp_f32 v129, v129\n"
  "v_mfma_f32_16x16x32_bf16 v[136:139], a[68:71], v[116:119], v[136:139]\n"
  "v_cvt_pk_bf16_f32 v128, v128, v129\n"
  "v_mfma_f32_16x16x32_bf16 v[140:143], a[84:87], v[116:119], v[140:143]\n"
  "v_exp_f32 v130, v130\n"
  "v_mfma_f32_16x16x32_bf16 v[136:139], a[72:75], v[120:123], v[136:139]\n"
  "v_exp_f32 v176, v176\n"
  "v_mfma_f32_16x16x32_bf16 v[140:143], a[88:91], v[120:123], v[140:143]\n"
  "v_exp_f32 v131, v131\n"
  "v_mfma_f32_16x16x32_bf16 v[136:139], a[76:79], v[124:127], v[136:139]\n"
  "v_cvt_pk_bf16_f32 v129, v130, v131\n"
  "s_nop 2\n"
  "v_mfma_f32_16x16x32_bf16 v[140:143], a[92:95], v[124:127], v[140:143]\n"
  "v_mfma_f32_16x16x32_bf16 v[144:147], a[64:67], v[96:99], v[192:195]\n"
  "s_add_u32 m0, s65, 0x14000\n"
  "s_add_u32 s67, s88, 0\n"
  "s_and_b32 s67, s67, 3\n"
  "v_mfma_f32_16x16x32_bf16 v[148:151], a[80:83], v[96:99], v[192:195]\n"
  "s_lshl_b32 s67, s67, 17\n"
  "s_or_b32 s67, s67, 0x10000\n"
  "v_mfma_f32_16x16x32_bf16 v[144:147], a[68:71], v[100:103], v[144:147]\n"
  "buffer_load_dwordx4 v240, s[72:75], s67 offen lds\n"
  "ds_read_b128 a[64:67], v218 offset:16384\n"
  "v_mfma_f32_16x16x32_bf16 v[148:151], a[84:87], v[100:103], v[148:151]\n"
  "v_exp_f32 v132, v132\n"
  "v_mfma_f32_16x16x32_bf16 v[144:147], a[72:75], v[104:107], v[144:147]\n"
  "v_exp_f32 v133, v133\n"
  "v_mfma_f32_16x16x32_bf16 v[148:151], a[88:91], v[104:107], v[148:151]\n"
  "v_exp_f32 v134, v134\n"
  "v_mfma_f32_16x16x32_bf16 v[144:147], a[76:79], v[108:111], v[144:147]\n"
  "v_exp_f32 v177, v177\n"
  "s_nop 2\n"
  "v_mfma_f32_16x16x32_bf16 v[148:151], a[92:95], v[108:111], v[148:151]\n"
  "s_waitcnt lgkmcnt(1)\n"
  "buffer_load_dwordx4 v241, s[72:75], s67 offen offset:1024 lds\n"
  "v_mfma_f32_16x16x32_bf16 v[0:3], a[96:99], v[152:155], v[0:3]\n"
  "v_exp_f32 v135, v135\n"
  "v_mfma_f32_16x16x32_bf16 a[32:35], a[128:131], v[152:155], a[32:35]\n"
  "ds_read_b128 a[80:83], v219 offset:16384\n"
  "v_cvt_pk_bf16_f32 v130, v132, v133\n"
  "v_mfma_f32_16x16x32_bf16 v[4:7], a[100:103], v[152:155], v[4:7]\n"
  "v_exp_f32 v136, v136\n"
  "v_mfma_f32_16x16x32_bf16 v[8:11], a[104:107], v[152:155], v[8:11]\n"
  "ds_read_b128 a[68:71], v218 offset:16896\n"
  "v_cvt_pk_bf16_f32 v131, v134, v135\n"
  "v_mfma_f32_16x16x32_bf16 v[12:15], a[108:111], v[152:155], v[12:15]\n"
  "v_exp_f32 v137, v137\n"
  "v_mfma_f32_16x16x32_bf16 v[16:19], a[112:115], v[152:155], v[16:19]\n"
  "ds_read_b128 a[84:87], v219 offset:16896\n"
  "v_cvt_pk_bf16_f32 v136, v136, v137\n"
  "v_mfma_f32_16x16x32_bf16 v[20:23], a[116:119], v[152:155], v[20:23]\n"
  "v_exp_f32 v178, v178\n"
  "s_nop 2\n"
  "v_mfma_f32_16x16x32_bf16 v[24:27], a[120:123], v[152:155], v[24:27]\n"
  "v_exp_f32 v138, v138\n"
  "v_mfma_f32_16x16x32_bf16 v[28:31], a[124:127], v[152:155], v[28:31]\n"
  "buffer_load_dwordx4 v242, s[72:75], s67 offen offset:2048 lds\n"
  "ds_read_b128 a[72:75], v218 offset:24576\n"
  "v_mfma_f32_16x16x32_bf16 v[32:35], a[96:99], v[160:163], v[32:35]\n"
  "v_exp_f32 v139, v139\n"
  "v_mfma_f32_16x16x32_bf16 a[36:39], a[128:131], v[160:163], a[36:39]\n"
  "ds_read_b128 a[88:91], v219 offset:24576\n"
  "v_cvt_pk_bf16_f32 v137, v138, v139\n"
  "v_mfma_f32_16x16x32_bf16 v[36:39], a[100:103], v[160:163], v[36:39]\n"
  "v_exp_f32 v140, v140\n"
  "v_mfma_f32_16x16x32_bf16 v[40:43], a[104:107], v[160:163], v[40:43]\n"
  "s_barrier\n"
  "v_mfma_f32_16x16x32_bf16 v[44:47], a[108:111], v[160:163], v[44:47]\n"
  "v_exp_f32 v141, v141\n"
  "v_mfma_f32_16x16x32_bf16 v[48:51], a[112:115], v[160:163], v[48:51]\n"
  "buffer_load_dwordx4 v243, s[72:75], s67 offen offset:3072 lds\n"
  "ds_read_b128 a[76:79], v218 offset:25088\n"
  "s_nop 2\n"
  "v_mfma_f32_16x16x32_bf16 v[52:55], a[116:119], v[160:163], v[52:55]\n"
  "v_exp_f32 v179, v179\n"
  "v_mfma_f32_16x16x32_bf16 v[56:59], a[120:123], v[160:163], v[56:59]\n"
  "v_exp_f32 v142, v142\n"
  "v_mfma_f32_16x16x32_bf16 v[60:63], a[124:127], v[160:163], v[60:63]\n"
  "ds_read_b128 a[92:95], v219 offset:25088\n"
  "v_cvt_pk_bf16_f32 v176, v176, v177\n"
  "v_mfma_f32_16x16x32_bf16 v[64:67], a[96:99], v[168:171], v[64:67]\n"
  "v_exp_f32 v143, v143\n"
  "v_mfma_f32_16x16x32_bf16 a[40:43], a[128:131], v[168:171], a[40:43]\n"
  "ds_read_b64_tr_b16 a[96:97], v220 offset:49152\n"
  "ds_read_b64_tr_b16 a[98:99], v221 offset:49152\n"
  "v_mfma_f32_16x16x32_bf16 v[68:71], a[100:103], v[168:171], v[68:71]\n"
  "v_exp_f32 v144, v144\n"
  "v_mfma_f32_16x16x32_bf16 v[72:75], a[104:107], v[168:171], v[72:75]\n"
  "ds_read_b64_tr_b16 a[100:101], v230 offset:49152\n"
  "ds_read_b64_tr_b16 a[102:103], v231 offset:49152\n"
  "v_mfma_f32_16x16x32_bf16 v[76:79], a[108:111], v[168:171], v[76:79]\n"
  "v_exp_f32 v145, v145\n"
  "s_nop 2\n"
  "v_mfma_f32_16x16x32_bf16 v[80:83], a[112:115], v[168:171], v[80:83]\n"
  "ds_read_b64_tr_b16 a[104:105], v220 offset:49664\n"
  "ds_read_b64_tr_b16 a[106:107], v221 offset:49664\n"
  "v_mfma_f32_16x16x32_bf16 v[84:87], a[116:119], v[168:171], v[84:87]\n"
  "s_add_u32 m0, s65, 0x4000\n"
  "s_mov_b32 s67, 0x14000\n"
  "v_mfma_f32_16x16x32_bf16 v[88:91], a[120:123], v[168:171], v[88:91]\n"
  "v_exp_f32 v146, v146\n"
  "v_mfma_f32_16x16x32_bf16 v[92:95], a[124:127], v[168:171], v[92:95]\n"
  "s_waitcnt lgkmcnt(6)\n"
  "buffer_load_dwordx4 v238, s[48:51], s67 offen nt lds\n"
  "v_mfma_f32_16x16x32_bf16 v[152:155], a[64:67], v[200:203], v[184:187]\n"
  "v_exp_f32 v147, v147\n"
  "v_mfma_f32_16x16x32_bf16 v[156:159], a[80:83], v[200:203], v[184:187]\n"
  "ds_read_b64_tr_b16 a[108:109], v230 offset:49664\n"
  "ds_read_b64_tr_b16 a[110:111], v231 offset:49664\n"
  "v_mfma_f32_16x16x32_bf16 v[152:155], a[68:71], v[204:207], v[152:155]\n"
  "v_exp_f32 v148, v148\n"
  "v_mfma_f32_16x16x32_bf16 v[156:159], a[84:87], v[204:207], v[156:159]\n"
  "ds_read_b64_tr_b16 a[112:113], v220 offset:57344\n"
  "ds_read_b64_tr_b16 a[114:115], v221 offset:57344\n"
  "s_nop 2\n"
  "v_mfma_f32_16x16x32_bf16 v[152:155], a[72:75], v[208:211], v[152:155]\n"
  "ds_read_b64_tr_b16 a[116:117], v230 offset:57344\n"
  "ds_read_b64_tr_b16 a[118:119], v231 offset:57344\n"
  "v_mfma_f32_16x16x32_bf16 v[156:159], a[88:91], v[208:211], v[156:159]\n"
  "v_exp_f32 v149, v149\n"
  "v_mfma_f32_16x16x32_bf16 v[152:155], a[76:79], v[212:215], v[152:155]\n"
  "ds_read_b64_tr_b16 a[120:121], v220 offset:57856\n"
  "ds_read_b64_tr_b16 a[122:123], v221 offset:57856\n"
  "v_mfma_f32_16x16x32_bf16 v[156:159], a[92:95], v[212:215], v[156:159]\n"
  "v_exp_f32 v150, v150\n"
  "v_mfma_f32_16x16x32_bf16 v[160:163], a[64:67], v[112:115], v[188:191]\n"
  "ds_read_b64_tr_b16 a[124:125], v230 offset:57856\n"
  "ds_read_b64_tr_b16 a[126:127], v231 offset:57856\n"
  "v_mfma_f32_16x16x32_bf16 v[164:167], a[80:83], v[112:115], v[188:191]\n"
  "v_exp_f32 v151, v151\n"
  "v_mfma_f32_16x16x32_bf16 v[160:163], a[68:71], v[116:119], v[160:163]\n"
  "s_waitcnt vmcnt(13)\n"
  "s_barrier\n"
  "v_mfma_f32_16x16x32_bf16 v[164:167], a[84:87], v[116:119], v[164:167]\n"
  "v_cvt_pk_bf16_f32 v177, v178, v179\n"
  "v_cvt_pk_bf16_f32 v138, v140, v141\n"
  "s_nop 2\n"
  "v_mfma_f32_16x16x32_bf16 v[160:163], a[72:75], v[120:123], v[160:163]\n"
  "v_exp_f32 v152, v152\n"
  "v_mfma_f32_16x16x32_bf16 v[164:167], a[88:91], v[120:123], v[164:167]\n"
  "buffer_load_dwordx4 v239, s[48:51], s67 offen offset:1024 nt lds\n"
  "v_cvt_pk_bf16_f32 v139, v142, v143\n"
  "v_mfma_f32_16x16x32_bf16 v[160:163], a[76:79], v[124:127], v[160:163]\n"
  "v_exp_f32 v153, v153\n"
  "v_mfma_f32_16x16x32_bf16 v[164:167], a[92:95], v[124:127], v[164:167]\n"
  "v_cvt_pk_bf16_f32 v144, v144, v145\n"
  "v_cvt_pk_bf16_f32 v145, v146, v147\n"
  "v_mfma_f32_16x16x32_bf16 v[168:171], a[64:67], v[96:99], v[192:195]\n"
  "v_cvt_pk_bf16_f32 v146, v148, v149\n"
  "ds_read_b128 a[64:67], v216 offset:32768\n"
  "v_mfma_f32_16x16x32_bf16 v[172:175], a[80:83], v[96:99], v[192:195]\n"
  "v_exp_f32 v154, v154\n"
  "v_mfma_f32_16x16x32_bf16 v[168:171], a[68:71], v[100:103], v[168:171]\n"
  "v_cvt_pk_bf16_f32 v147, v150, v151\n"
  "ds_read_b128 a[80:83], v217 offset:32768\n"
  "v_mfma_f32_16x16x32_bf16 v[172:175], a[84:87], v[100:103], v[172:175]\n"
  "v_exp_f32 v155, v155\n"
  "s_nop 2\n"
  "v_mfma_f32_16x16x32_bf16 v[168:171], a[72:75], v[104:107], v[168:171]\n"
  "ds_read_b128 a[68:71], v216 offset:33280\n"
  "v_cvt_pk_bf16_f32 v152, v152, v153\n"
  "v_mfma_f32_16x16x32_bf16 v[172:175], a[88:91], v[104:107], v[172:175]\n"
  "v_exp_f32 v156, v156\n"
  "v_mfma_f32_16x16x32_bf16 v[168:171], a[76:79], v[108:111], v[168:171]\n"
  "ds_read_b128 a[84:87], v217 offset:33280\n"
  "v_cvt_pk_bf16_f32 v153, v154, v155\n"
  "v_mfma_f32_16x16x32_bf16 v[172:175], a[92:95], v[108:111], v[172:175]\n"
  "s_waitcnt lgkmcnt(4)\n"
  "v_mfma_f32_16x16x32_bf16 v[0:3], a[96:99], v[128:131], v[0:3]\n"
  "v_exp_f32 v157, v157\n"
  "v_mfma_f32_16x16x32_bf16 a[32:35], a[128:131], v[128:131], a[32:35]\n"
  "ds_read_b128 a[72:75], v216 offset:40960\n"
  "v_cvt_pk_bf16_f32 v154, v156, v157\n"
  "v_mfma_f32_16x16x32_bf16 v[4:7], a[100:103], v[128:131], v[4:7]\n"
  "v_exp_f32 v158, v158\n"
  "v_mfma_f32_16x16x32_bf16 v[8:11], a[104:107], v[128:131], v[8:11]\n"
  "ds_read_b128 a[88:91], v217 offset:40960\n"
  "s_nop 2\n"
  "v_mfma_f32_16x16x32_bf16 v[12:15], a[108:111], v[128:131], v[12:15]\n"
  "v_exp_f32 v159, v159\n"
  "v_mfma_f32_16x16x32_bf16 v[16:19], a[112:115], v[128:131], v[16:19]\n"
  "buffer_load_dwordx4 v238, s[48:51], s67 offen offset:2048 nt lds\n"
  "ds_read_b128 a[76:79], v216 offset:41472\n"
  "v_mfma_f32_16x16x32_bf16 v[20:23], a[116:119], v[128:131], v[20:23]\n"
  "v_cvt_pk_bf16_f32 v155, v158, v159\n"
  "v_mfma_f32_16x16x32_bf16 v[24:27], a[120:123], v[128:131], v[24:27]\n"
  "v_exp_f32 v160, v160\n"
  "v_mfma_f32_16x16x32_bf16 v[28:31], a[124:127], v[128:131], v[28:31]\n"
  "ds_read_b128 a[92:95], v217 offset:41472\n"
  "v_mfma_f32_16x16x16_bf16 a[0:3], a[96:97], v[176:177], a[0:3]\n"
  "v_exp_f32 v161, v161\n"
  "v_mfma_f32_16x16x16_bf16 a[4:7], a[100:101], v[176:177], a[4:7]\n"
  "v_cvt_pk_bf16_f32 v160, v160, v161\n"
  "v_mfma_f32_16x16x16_bf16 a[8:11], a[104:105], v[176:177], a[8:11]\n"
  "s_nop 2\n"
  "v_mfma_f32_16x16x16_bf16 a[12:15], a[108:109], v[176:177], a[12:15]\n"
  "v_exp_f32 v162, v162\n"
  "v_mfma_f32_16x16x16_bf16 a[16:19], a[112:113], v[176:177], a[16:19]\n"
  "v_mfma_f32_16x16x16_bf16 a[20:23], a[116:117], v[176:177], a[20:23]\n"
  "v_exp_f32 v163, v163\n"
  "v_mfma_f32_16x16x16_bf16 a[24:27], a[120:121], v[176:177], a[24:27]\n"
  "v_cvt_pk_bf16_f32 v161, v162, v163\n"
  "v_mfma_f32_16x16x16_bf16 a[28:31], a[124:125], v[176:177], a[28:31]\n"
  "v_mfma_f32_16x16x16_bf16 a[44:47], a[128:129], v[176:177], a[44:47]\n"
  "v_exp_f32 v164, v164\n"
  "v_mfma_f32_16x16x32_bf16 v[32:35], a[96:99], v[136:139], v[32:35]\n"
  "v_mfma_f32_16x16x32_bf16 a[36:39], a[128:131], v[136:139], a[36:39]\n"
  "v_exp_f32 v165, v165\n"
  "s_nop 2\n"
  "v_mfma_f32_16x16x32_bf16 v[36:39], a[100:103], v[136:139], v[36:39]\n"
  "v_cvt_pk_bf16_f32 v162, v164, v165\n"
  "v_mfma_f32_16x16x32_bf16 v[40:43], a[104:107], v[136:139], v[40:43]\n"
  "buffer_load_dwordx4 v239, s[48:51], s67 offen offset:3072 nt lds\n"
  "v_mfma_f32_16x16x32_bf16 v[44:47], a[108:111], v[136:139], v[44:47]\n"
  "v_exp_f32 v166, v166\n"
  "v_mfma_f32_16x16x32_bf16 v[48:51], a[112:115], v[136:139], v[48:51]\n"
  "v_mfma_f32_16x16x32_bf16 v[52:55], a[116:119], v[136:139], v[52:55]\n"
  "v_exp_f32 v167, v167\n"
  "v_mfma_f32_16x16x32_bf16 v[56:59], a[120:123], v[136:139], v[56:59]\n"
  "v_cvt_pk_bf16_f32 v163, v166, v167\n"
  "v_mfma_f32_16x16x32_bf16 v[60:63], a[124:127], v[136:139], v[60:63]\n"
  "v_mfma_f32_16x16x32_bf16 v[64:67], a[96:99], v[144:147], v[64:67]\n"
  "ds_read_b64_tr_b16 a[96:97], v228 offset:49152\n"
  "ds_read_b64_tr_b16 a[98:99], v229 offset:49152\n"
  "s_nop 2\n"
  "v_mfma_f32_16x16x32_bf16 a[40:43], a[128:131], v[144:147], a[40:43]\n"
  "v_exp_f32 v168, v168\n"
  "v_mfma_f32_16x16x32_bf16 v[68:71], a[100:103], v[144:147], v[68:71]\n"
  "ds_read_b64_tr_b16 a[100:101], v232 offset:49152\n"
  "ds_read_b64_tr_b16 a[102:103], v233 offset:49152\n"
  "v_mfma_f32_16x16x32_bf16 v[72:75], a[104:107], v[144:147], v[72:75]\n"
  "v_exp_f32 v169, v169\n"
  "v_mfma_f32_16x16x32_bf16 v[76:79], a[108:111], v[144:147], v[76:79]\n"
  "ds_read_b64_tr_b16 a[104:105], v228 offset:49664\n"
  "ds_read_b64_tr_b16 a[106:107], v229 offset:49664\n"
  "v_mfma_f32_16x16x32_bf16 v[80:83], a[112:115], v[144:147], v[80:83]\n"
  "ds_read_b64_tr_b16 a[108:109], v232 offset:49664\n"
  "ds_read_b64_tr_b16 a[110:111], v233 offset:49664\n"
  "v_mfma_f32_16x16x32_bf16 v[84:87], a[116:119], v[144:147], v[84:87]\n"
  "v_exp_f32 v170, v170\n"
  "v_mfma_f32_16x16x32_bf16 v[88:91], a[120:123], v[144:147], v[88:91]\n"
  "ds_read_b64_tr_b16 a[112:113], v228 offset:57344\n"
  "ds_read_b64_tr_b16 a[114:115], v229 offset:57344\n"
  "v_mfma_f32_16x16x32_bf16 v[92:95], a[124:127], v[144:147], v[92:95]\n"
  "s_waitcnt lgkmcnt(10)\n"
  "ds_read_b64_tr_b16 a[116:117], v232 offset:57344\n"
  "s_nop 2\n"
  "v_mfma_f32_16x16x32_bf16 v[128:131], a[64:67], v[200:203], v[184:187]\n"
  "v_exp_f32 v171, v171\n"
  "v_mfma_f32_16x16x32_bf16 v[132:135], a[80:83], v[200:203], v[184:187]\n"
  "s_add_u32 m0, s65, 0x18000\n"
  "s_mov_b32 s66, 0x18000\n"
  "v_mfma_f32_16x16x32_bf16 v[128:131], a[68:71], v[204:207], v[128:131]\n"
  "v_exp_f32 v172, v172\n"
  "v_mfma_f32_16x16x32_bf16 v[132:135], a[84:87], v[204:207], v[132:135]\n"
  "buffer_load_dwordx4 v234, s[44:47], s66 offen nt lds\n"
  "ds_read_b64_tr_b16 a[118:119], v233 offset:57344\n"
  "v_mfma_f32_16x16x32_bf16 v[128:131], a[72:75], v[208:211], v[128:131]\n"
  "v_exp_f32 v173, v173\n"
  "v_mfma_f32_16x16x32_bf16 v[132:135], a[88:91], v[208:211], v[132:135]\n"
  "ds_read_b64_tr_b16 a[120:121], v228 offset:57856\n"
  "ds_read_b64_tr_b16 a[122:123], v229 offset:57856\n"
  "v_mfma_f32_16x16x32_bf16 v[128:131], a[76:79], v[212:215], v[128:131]\n"
  "ds_read_b64_tr_b16 a[124:125], v232 offset:57856\n"
  "ds_read_b64_tr_b16 a[126:127], v233 offset:57856\n"
  "v_mfma_f32_16x16x32_bf16 v[132:135], a[92:95], v[212:215], v[132:135]\n"
  "v_exp_f32 v174, v174\n"
  "s_nop 2\n"
  "v_mfma_f32_16x16x32_bf16 v[176:179], a[64:67], a[48:51], v[196:199]\n"
  "v_exp_f32 v175, v175\n"
  "v_mfma_f32_16x16x32_bf16 v[176:179], a[68:71], a[52:55], v[176:179]\n"
  "v_cvt_pk_bf16_f32 v168, v168, v169\n"
  "v_cvt_pk_bf16_f32 v169, v170, v171\n"
  "v_mfma_f32_16x16x32_bf16 v[176:179], a[72:75], a[56:59], v[176:179]\n"
  "v_cvt_pk_bf16_f32 v170, v172, v173\n"
  "v_cvt_pk_bf16_f32 v171, v174, v175\n"
  "v_mfma_f32_16x16x32_bf16 v[176:179], a[76:79], a[60:63], v[176:179]\n"
  "v_exp_f32 v128, v128\n"
  "v_mfma_f32_16x16x32_bf16 v[136:139], a[64:67], v[112:115], v[188:191]\n"
  "v_mfma_f32_16x16x32_bf16 v[140:143], a[80:83], v[112:115], v[188:191]\n"
  "v_exp_f32 v129, v129\n"
  "v_mfma_f32_16x16x32_bf16 v[136:139], a[68:71], v[116:119], v[136:139]\n"
  "v_cvt_pk_bf16_f32 v128, v128, v129\n"
  "v_mfma_f32_16x16x32_bf16 v[140:143], a[84:87], v[116:119], v[140:143]\n"
  "v_exp_f32 v130, v130\n"
  "s_nop 2\n"
  "v_mfma_f32_16x16x32_bf16 v[136:139], a[72:75], v[120:123], v[136:139]\n"
  "v_exp_f32 v176, v176\n"
  "v_mfma_f32_16x16x32_bf16 v[140:143], a[88:91], v[120:123], v[140:143]\n"
  "v_exp_f32 v131, v131\n"
  "v_mfma_f32_16x16x32_bf16 v[136:139], a[76:79], v[124:127], v[136:139]\n"
  "buffer_load_dwordx4 v235, s[44:47], s66 offen offset:1024 nt lds\n"
  "v_cvt_pk_bf16_f32 v129, v130, v131\n"
  "v_mfma_f32_16x16x32_bf16 v[140:143], a[92:95], v[124:127], v[140:143]\n"
  "v_mfma_f32_16x16x32_bf16 v[144:147], a[64:67], v[96:99], v[192:195]\n"
  "v_exp_f32 v132, v132\n"
  "v_mfma_f32_16x16x32_bf16 v[148:151], a[80:83], v[96:99], v[192:195]\n"
  "ds_read_b128 a[64:67], v218 offset:32768\n"
  "v_mfma_f32_16x16x32_bf16 v[144:147], a[68:71], v[100:103], v[144:147]\n"
  "v_exp_f32 v133, v133\n"
  "v_mfma_f32_16x16x32_bf16 v[148:151], a[84:87], v[100:103], v[148:151]\n"
  "ds_read_b128 a[80:83], v219 offset:32768\n"
  "v_cvt_pk_bf16_f32 v130, v132, v133\n"
  "s_nop 2\n"
  "v_mfma_f32_16x16x32_bf16 v[144:147], a[72:75], v[104:107], v[144:147]\n"
  "v_exp_f32 v134, v134\n"
  "v_mfma_f32_16x16x32_bf16 v[148:151], a[88:91], v[104:107], v[148:151]\n"
  "v_exp_f32 v177, v177\n"
  "v_mfma_f32_16x16x32_bf16 v[144:147], a[76:79], v[108:111], v[144:147]\n"
  "ds_read_b128 a[68:71], v218 offset:33280\n"
  "v_mfma_f32_16x16x32_bf16 v[148:151], a[92:95], v[108:111], v[148:151]\n"
  "s_waitcnt lgkmcnt(3)\n"
  "v_mfma_f32_16x16x32_bf16 v[0:3], a[96:99], v[152:155], v[0:3]\n"
  "v_exp_f32 v135, v135\n"
  "v_mfma_f32_16x16x32_bf16 a[32:35], a[128:131], v[152:155], a[32:35]\n"
  "ds_read_b128 a[84:87], v219 offset:33280\n"
  "v_cvt_pk_bf16_f32 v131, v134, v135\n"
  "v_mfma_f32_16x16x32_bf16 v[4:7], a[100:103], v[152:155], v[4:7]\n"
  "v_exp_f32 v136, v136\n"
  "v_mfma_f32_16x16x32_bf16 v[8:11], a[104:107], v[152:155], v[8:11]\n"
  "v_exp_f32 v137, v137\n"
  "s_nop 2\n"
  "v_mfma_f32_16x16x32_bf16 v[12:15], a[108:111], v[152:155], v[12:15]\n"
  "ds_read_b128 a[72:75], v218 offset:40960\n"
  "v_cvt_pk_bf16_f32 v136, v136, v137\n"
  "v_mfma_f32_16x16x32_bf16 v[16:19], a[112:115], v[152:155], v[16:19]\n"
  "v_exp_f32 v178, v178\n"
  "v_mfma_f32_16x16x32_bf16 v[20:23], a[116:119], v[152:155], v[20:23]\n"
  "v_exp_f32 v138, v138\n"
  "v_mfma_f32_16x16x32_bf16 v[24:27], a[120:123], v[152:155], v[24:27]\n"
  "buffer_load_dwordx4 v236, s[44:47], s66 offen offset:2048 nt lds\n"
  "ds_read_b128 a[88:91], v219 offset:40960\n"
  "v_mfma_f32_16x16x32_bf16 v[28:31], a[124:127], v[152:155], v[28:31]\n"
  "v_exp_f32 v139, v139\n"
  "v_mfma_f32_16x16x32_bf16 v[32:35], a[96:99], v[160:163], v[32:35]\n"
  "ds_read_b128 a[76:79], v218 offset:41472\n"
  "v_cvt_pk_bf16_f32 v137, v138, v139\n"
  "v_mfma_f32_16x16x32_bf16 a[36:39], a[128:131], v[160:163], a[36:39]\n"
  "v_exp_f32 v140, v140\n"
  "v_mfma_f32_16x16x32_bf16 v[36:39], a[100:103], v[160:163], v[36:39]\n"
  "ds_read_b128 a[92:95], v219 offset:41472\n"
  "s_nop 2\n"
  "v_mfma_f32_16x16x32_bf16 v[40:43], a[104:107], v[160:163], v[40:43]\n"
  "s_waitcnt vmcnt(15)\n"
  "s_barrier\n"
  "v_mfma_f32_16x16x32_bf16 v[44:47], a[108:111], v[160:163], v[44:47]\n"
  "v_exp_f32 v141, v141\n"
  "v_mfma_f32_16x16x32_bf16 v[48:51], a[112:115], v[160:163], v[48:51]\n"
  "v_exp_f32 v179, v179\n"
  "v_mfma_f32_16x16x32_bf16 v[52:55], a[116:119], v[160:163], v[52:55]\n"
  "v_cvt_pk_bf16_f32 v176, v176, v177\n"
  "v_cvt_pk_bf16_f32 v177, v178, v179\n"
  "v_mfma_f32_16x16x32_bf16 v[56:59], a[120:123], v[160:163], v[56:59]\n"
  "v_exp_f32 v142, v142\n"
  "v_mfma_f32_16x16x32_bf16 v[60:63], a[124:127], v[160:163], v[60:63]\n"
  "v_exp_f32 v143, v143\n"
  "v_mfma_f32_16x16x32_bf16 v[64:67], a[96:99], v[168:171], v[64:67]\n"
  "ds_read_b64_tr_b16 a[96:97], v220\n"
  "ds_read_b64_tr_b16 a[98:99], v221\n"
  "v_mfma_f32_16x16x32_bf16 a[40:43], a[128:131], v[168:171], a[40:43]\n"
  "v_exp_f32 v144, v144\n"
  "s_nop 2\n"
  "v_mfma_f32_16x16x32_bf16 v[68:71], a[100:103], v[168:171], v[68:71]\n"
  "ds_read_b64_tr_b16 a[100:101], v230\n"
  "ds_read_b64_tr_b16 a[102:103], v231\n"
  "v_mfma_f32_16x16x32_bf16 v[72:75], a[104:107], v[168:171], v[72:75]\n"
  "buffer_load_dwordx4 v237, s[44:47], s66 offen offset:3072 nt lds\n"
  "ds_read_b64_tr_b16 a[104:105], v220 offset:512\n"
  "v_mfma_f32_16x16x32_bf16 v[76:79], a[108:111], v[168:171], v[76:79]\n"
  "v_exp_f32 v145, v145\n"
  "v_mfma_f32_16x16x32_bf16 v[80:83], a[112:115], v[168:171], v[80:83]\n"
  "ds_read_b64_tr_b16 a[106:107], v221 offset:512\n"
  "ds_read_b64_tr_b16 a[108:109], v230 offset:512\n"
  "v_mfma_f32_16x16x32_bf16 v[84:87], a[116:119], v[168:171], v[84:87]\n"
  "ds_read_b64_tr_b16 a[110:111], v231 offset:512\n"
  "ds_read_b64_tr_b16 a[112:113], v220 offset:8192\n"
  "v_mfma_f32_16x16x32_bf16 v[88:91], a[120:123], v[168:171], v[88:91]\n"
  "v_exp_f32 v146, v146\n"
  "v_mfma_f32_16x16x32_bf16 v[92:95], a[124:127], v[168:171], v[92:95]\n"
  "s_waitcnt lgkmcnt(9)\n"
  "ds_read_b64_tr_b16 a[114:115], v221 offset:8192\n"
  "v_mfma_f32_16x16x32_bf16 v[152:155], a[64:67], v[200:203], v[184:187]\n"
  "v_exp_f32 v147, v147\n"
  "s_nop 2\n"
  "v_mfma_f32_16x16x32_bf16 v[156:159], a[80:83], v[200:203], v[184:187]\n"
  "L_st4:\n"
  "s_cbranch_execz L_st8\n"
  "ds_read_b64_tr_b16 a[116:117], v230 offset:8192\n"
  "ds_read_b64_tr_b16 a[118:119], v231 offset:8192\n"
  "v_mfma_f32_16x16x32_bf16 v[152:155], a[68:71], v[204:207], v[152:155]\n"
  "v_exp_f32 v148, v148\n"
  "v_mfma_f32_16x16x32_bf16 v[156:159], a[84:87], v[204:207], v[156:159]\n"
  "ds_read_b64_tr_b16 a[120:121], v220 offset:8704\n"
  "ds_read_b64_tr_b16 a[122:123], v221 offset:8704\n"
  "v_mfma_f32_16x16x32_bf16 v[152:155], a[72:75], v[208:211], v[152:155]\n"
  "ds_read_b64_tr_b16 a[124:125], v230 offset:8704\n"
  "ds_read_b64_tr_b16 a[126:127], v231 offset:8704\n"
  "v_mfma_f32_16x16x32_bf16 v[156:159], a[88:91], v[208:211], v[156:159]\n"
  "v_exp_f32 v149, v149\n"
  "v_mfma_f32_16x16x32_bf16 v[152:155], a[76:79], v[212:215], v[152:155]\n"
  "v_exp_f32 v150, v150\n"
  "v_mfma_f32_16x16x32_bf16 v[156:159], a[92:95], v[212:215], v[156:159]\n"
  "v_cvt_pk_bf16_f32 v138, v140, v141\n"
  "v_cvt_pk_bf16_f32 v139, v142, v143\n"
  "v_mfma_f32_16x16x32_bf16 v[160:163], a[64:67], v[112:115], v[188:191]\n"
  "v_exp_f32 v151, v151\n"
  "s_nop 2\n"
  "v_mfma_f32_16x16x32_bf16 v[164:167], a[80:83], v[112:115], v[188:191]\n"
  "v_cvt_pk_bf16_f32 v144, v144, v145\n"
  "v_cvt_pk_bf16_f32 v145, v146, v147\n"
  "v_mfma_f32_16x16x32_bf16 v[160:163], a[68:71], v[116:119], v[160:163]\n"
  "s_waitcnt vmcnt(12)\n"
  "s_barrier\n"
  "v_mfma_f32_16x16x32_bf16 v[164:167], a[84:87], v[116:119], v[164:167]\n"
  "s_add_u32 m0, s65, 0x8000\n"
  "s_mov_b32 s67, 0x18000\n"
  "v_mfma_f32_16x16x32_bf16 v[160:163], a[72:75], v[120:123], v[160:163]\n"
  "buffer_load_dwordx4 v238, s[48:51], s67 offen nt lds\n"
  "v_cvt_pk_bf16_f32 v146, v148, v149\n"
  "v_mfma_f32_16x16x32_bf16 v[164:167], a[88:91], v[120:123], v[164:167]\n"
  "v_exp_f32 v152, v152\n"
  "v_mfma_f32_16x16x32_bf16 v[160:163], a[76:79], v[124:127], v[160:163]\n"
  "v_exp_f32 v153, v153\n"
  "v_mfma_f32_16x16x32_bf16 v[164:167], a[92:95], v[124:127], v[164:167]\n"
  "v_cvt_pk_bf16_f32 v147, v150, v151\n"
  "v_cvt_pk_bf16_f32 v152, v152, v153\n"
  "v_mfma_f32_16x16x32_bf16 v[168:171], a[64:67], v[96:99], v[192:195]\n"
  "v_exp_f32 v154, v154\n"
  "s_nop 2\n"
  "v_mfma_f32_16x16x32_bf16 v[172:175], a[80:83], v[96:99], v[192:195]\n"
  "ds_read_b128 a[64:67], v216 offset:49152\n"
  "v_mfma_f32_16x16x32_bf16 v[168:171], a[68:71], v[100:103], v[168:171]\n"
  "v_exp_f32 v155, v155\n"
  "v_mfma_f32_16x16x32_bf16 v[172:175], a[84:87], v[100:103], v[172:175]\n"
  "ds_read_b128 a[80:83], v217 offset:49152\n"
  "v_cvt_pk_bf16_f32 v153, v154, v155\n"
  "v_mfma_f32_16x16x32_bf16 v[168:171], a[72:75], v[104:107], v[168:171]\n"
  "v_mfma_f32_16x16x32_bf16 v[172:175], a[88:91], v[104:107], v[172:175]\n"
  "v_exp_f32 v156, v156\n"
  "v_mfma_f32_16x16x32_bf16 v[168:171], a[76:79], v[108:111], v[168:171]\n"
  "ds_read_b128 a[68:71], v216 offset:49664\n"
  "v_mfma_f32_16x16x32_bf16 v[172:175], a[92:95], v[108:111], v[172:175]\n"
  "s_waitcnt lgkmcnt(3)\n"
  "v_mfma_f32_16x16x32_bf16 v[0:3], a[96:99], v[128:131], v[0:3]\n"
  "v_exp_f32 v157, v157\n"
  "s_nop 2\n"
  "v_mfma_f32_16x16x32_bf16 a[32:35], a[128:131], v[128:131], a[32:35]\n"
  "ds_read_b128 a[84:87], v217 offset:49664\n"
  "v_cvt_pk_bf16_f32 v154, v156, v157\n"
  "v_mfma_f32_16x16x32_bf16 v[4:7], a[100:103], v[128:131], v[4:7]\n"
  "v_exp_f32 v158, v158\n"
  "v_mfma_f32_16x16x32_bf16 v[8:11], a[104:107], v[128:131], v[8:11]\n"
  "buffer_load_dwordx4 v239, s[48:51], s67 offen offset:1024 nt lds\n"
  "ds_read_b128 a[72:75], v216 offset:57344\n"
  "v_mfma_f32_16x16x32_bf16 v[12:15], a[108:111], v[128:131], v[12:15]\n"
  "v_exp_f32 v159, v159\n"
  "v_mfma_f32_16x16x32_bf16 v[16:19], a[112:115], v[128:131], v[16:19]\n"
  "ds_read_b128 a[88:91], v217 offset:57344\n"
  "v_cvt_pk_bf16_f32 v155, v158, v159\n"
  "v_mfma_f32_16x16x32_bf16 v[20:23], a[116:119], v[128:131], v[20:23]\n"
  "v_mfma_f32_16x16x32_bf16 v[24:27], a[120:123], v[128:131], v[24:27]\n"
  "v_exp_f32 v160, v160\n"
  "v_mfma_f32_16x16x32_bf16 v[28:31], a[124:127], v[128:131], v[28:31]\n"
  "ds_read_b128 a[76:79], v216 offset:57856\n"
  "s_nop 2\n"
  "v_mfma_f32_16x16x16_bf16 a[0:3], a[96:97], v[176:177], a[0:3]\n"
  "v_exp_f32 v161, v161\n"
  "v_mfma_f32_16x16x16_bf16 a[4:7], a[100:101], v[176:177], a[4:7]\n"
  "ds_read_b128 a[92:95], v217 offset:57856\n"
  "v_cvt_pk_bf16_f32 v160, v160, v161\n"
  "v_mfma_f32_16x16x16_bf16 a[8:11], a[104:105], v[176:177], a[8:11]\n"
  "v_mfma_f32_16x16x16_bf16 a[12:15], a[108:109], v[176:177], a[12:15]\n"
  "v_exp_f32 v162, v162\n"
  "v_mfma_f32_16x16x16_bf16 a[16:19], a[112:113], v[176:177], a[16:19]\n"
  "v_mfma_f32_16x16x16_bf16 a[20:23], a[116:117], v[176:177], a[20:23]\n"
  "v_exp_f32 v163, v163\n"
  "v_mfma_f32_16x16x16_bf16 a[24:27], a[120:121], v[176:177], a[24:27]\n"
  "v_cvt_pk_bf16_f32 v161, v162, v163\n"
  "v_mfma_f32_16x16x16_bf16 a[28:31], a[124:125], v[176:177], a[28:31]\n"
  "s_nop 2\n"
  "v_mfma_f32_16x16x16_bf16 a[44:47], a[128:129], v[176:177], a[44:47]\n"
  "v_exp_f32 v164, v164\n"
  "v_mfma_f32_16x16x32_bf16 v[32:35], a[96:99], v[136:139], v[32:35]\n"
  "buffer_load_dwordx4 v238, s[48:51], s67 offen offset:2048 nt lds\n"
  "v_mfma_f32_16x16x32_bf16 a[36:39], a[128:131], v[136:139], a[36:39]\n"
  "v_exp_f32 v165, v165\n"
  "v_mfma_f32_16x16x32_bf16 v[36:39], a[100:103], v[136:139], v[36:39]\n"
  "v_cvt_pk_bf16_f32 v162, v164, v165\n"
  "v_mfma_f32_16x16x32_bf16 v[40:43], a[104:107], v[136:139], v[40:43]\n"
  "v_mfma_f32_16x16x32_bf16 v[44:47], a[108:111], v[136:139], v[44:47]\n"
  "v_exp_f32 v166, v166\n"
  "v_mfma_f32_16x16x32_bf16 v[48:51], a[112:115], v[136:139], v[48:51]\n"
  "v_mfma_f32_16x16x32_bf16 v[52:55], a[116:119], v[136:139], v[52:55]\n"
  "v_exp_f32 v167, v167\n"
  "s_nop 2\n"
  "v_mfma_f32_16x16x32_bf16 v[56:59], a[120:123], v[136:139], v[56:59]\n"
  "v_cvt_pk_bf16_f32 v163, v166, v167\n"
  "v_mfma_f32_16x16x32_bf16 v[60:63], a[124:127], v[136:139], v[60:63]\n"
  "v_mfma_f32_16x16x32_bf16 v[64:67], a[96:99], v[144:147], v[64:67]\n"
  "ds_read_b64_tr_b16 a[96:97], v228\n"
  "ds_read_b64_tr_b16 a[98:99], v229\n"
  "v_mfma_f32_16x16x32_bf16 a[40:43], a[128:131], v[144:147], a[40:43]\n"
  "v_exp_f32 v168, v168\n"
  "v_mfma_f32_16x16x32_bf16 v[68:71], a[100:103], v[144:147], v[68:71]\n"
  "ds_read_b64_tr_b16 a[100:101], v232\n"
  "ds_read_b64_tr_b16 a[102:103], v233\n"
  "v_mfma_f32_16x16x32_bf16 v[72:75], a[104:107], v[144:147], v[72:75]\n"
  "v_exp_f32 v169, v169\n"
  "v_mfma_f32_16x16x32_bf16 v[76:79], a[108:111], v[144:147], v[76:79]\n"
  "ds_read_b64_tr_b16 a[104:105], v228 offset:512\n"
  "ds_read_b64_tr_b16 a[106:107], v229 offset:512\n"
  "v_mfma_f32_16x16x32_bf16 v[80:83], a[112:115], v[144:147], v[80:83]\n"
  "ds_read_b64_tr_b16 a[108:109], v232 offset:512\n"
  "ds_read_b64_tr_b16 a[110:111], v233 offset:512\n"
  "s_nop 2\n"
  "v_mfma_f32_16x16x32_bf16 v[84:87], a[116:119], v[144:147], v[84:87]\n"
  "v_exp_f32 v170, v170\n"
  "v_mfma_f32_16x16x32_bf16 v[88:91], a[120:123], v[144:147], v[88:91]\n"
  "buffer_load_dwordx4 v239, s[48:51], s67 offen offset:3072 nt lds\n"
  "ds_read_b64_tr_b16 a[112:113], v228 offset:8192\n"
  "v_mfma_f32_16x16x32_bf16 v[92:95], a[124:127], v[144:147], v[92:95]\n"
  "s_waitcnt lgkmcnt(9)\n"
  "ds_read_b64_tr_b16 a[114:115], v229 offset:8192\n"
  "v_mfma_f32_16x16x32_bf16 v[128:131], a[64:67], v[200:203], v[184:187]\n"
  "v_exp_f32 v171, v171\n"
  "v_mfma_f32_16x16x32_bf16 v[132:135], a[80:83], v[200:203], v[184:187]\n"
  "ds_read_b64_tr_b16 a[116:117], v232 offset:8192\n"
  "ds_read_b64_tr_b16 a[118:119], v233 offset:8192\n"
  "v_mfma_f32_16x16x32_bf16 v[128:131], a[68:71], v[204:207], v[128:131]\n"
  "v_exp_f32 v172, v172\n"
  "v_mfma_f32_16x16x32_bf16 v[132:135], a[84:87], v[204:207], v[132:135]\n"
  "ds_read_b64_tr_b16 a[120:121], v228 offset:8704\n"
  "ds_read_b64_tr_b16 a[122:123], v229 offset:8704\n"
  "v_mfma_f32_16x16x32_bf16 v[128:131], a[72:75], v[208:211], v[128:131]\n"
  "v_exp_f32 v173, v173\n"
  "s_nop 2\n"
  "v_mfma_f32_16x16x32_bf16 v[132:135], a[88:91], v[208:211], v[132:135]\n"
  "ds_read_b64_tr_b16 a[124:125], v232 offset:8704\n"
  "ds_read_b64_tr_b16 a[126:127], v233 offset:8704\n"
  "v_mfma_f32_16x16x32_bf16 v[128:131], a[76:79], v[212:215], v[128:131]\n"
  "v_exp_f32 v174, v174\n"
  "v_mfma_f32_16x16x32_bf16 v[132:135], a[92:95], v[212:215], v[132:135]\n"
  "v_cvt_pk_bf16_f32 v168, v168, v169\n"
  "v_cvt_pk_bf16_f32 v169, v170, v171\n"
  "v_mfma_f32_16x16x32_bf16 v[176:179], a[64:67], a[48:51], v[196:199]\n"
  "v_exp_f32 v175, v175\n"
  "v_mfma_f32_16x16x32_bf16 v[176:179], a[68:71], a[52:55], v[176:179]\n"
  "v_cvt_pk_bf16_f32 v170, v172, v173\n"
  "v_cvt_pk_bf16_f32 v171, v174, v175\n"
  "v_mfma_f32_16x16x32_bf16 v[176:179], a[72:75], a[56:59], v[176:179]\n"
  "v_mfma_f32_16x16x32_bf16 v[176:179], a[76:79], a[60:63], v[176:179]\n"
  "v_exp_f32 v128, v128\n"
  "v_mfma_f32_16x16x32_bf16 v[136:139], a[64:67], v[112:115], v[188:191]\n"
  "s_nop 2\n"
  "v_mfma_f32_16x16x32_bf16 v[140:143], a[80:83], v[112:115], v[188:191]\n"
  "v_exp_f32 v129, v129\n"
  "v_mfma_f32_16x16x32_bf16 v[136:139], a[68:71], v[116:119], v[136:139]\n"
  "s_add_u32 m0, s65, 0x1c000\n"
  "s_mov_b32 s66, 0x1c000\n"
  "v_mfma_f32_16x16x32_bf16 v[140:143], a[84:87], v[116:119], v[140:143]\n"
  "buffer_load_dwordx4 v234, s[44:47], s66 offen nt lds\n"
  "v_cvt_pk_bf16_f32 v128, v128, v129\n"
  "v_mfma_f32_16x16x32_bf16 v[136:139], a[72:75], v[120:123], v[136:139]\n"
  "v_exp_f32 v130, v130\n"
  "v_mfma_f32_16x16x32_bf16 v[140:143], a[88:91], v[120:123], v[140:143]\n"
  "v_exp_f32 v131, v131\n"
  "v_mfma_f32_16x16x32_bf16 v[136:139], a[76:79], v[124:127], v[136:139]\n"
  "v_exp_f32 v176, v176\n"
  "v_mfma_f32_16x16x32_bf16 v[140:143], a[92:95], v[124:127], v[140:143]\n"
  "v_cvt_pk_bf16_f32 v129, v130, v131\n"
  "v_mfma_f32_16x16x32_bf16 v[144:147], a[64:67], v[96:99], v[192:195]\n"
  "v_exp_f32 v132, v132\n"
  "s_nop 2\n"
  "v_mfma_f32_16x16x32_bf16 v[148:151], a[80:83], v[96:99], v[192:195]\n"
  "ds_read_b128 a[64:67], v218 offset:49152\n"
  "v_mfma_f32_16x16x32_bf16 v[144:147], a[68:71], v[100:103], v[144:147]\n"
  "v_exp_f32 v133, v133\n"
  "v_mfma_f32_16x16x32_bf16 v[148:151], a[84:87], v[100:103], v[148:151]\n"
  "ds_read_b128 a[80:83], v219 offset:49152\n"
  "v_cvt_pk_bf16_f32 v130, v132, v133\n"
  "v_mfma_f32_16x16x32_bf16 v[144:147], a[72:75], v[104:107], v[144:147]\n"
  "v_exp_f32 v134, v134\n"
  "v_mfma_f32_16x16x32_bf16 v[148:151], a[88:91], v[104:107], v[148:151]\n"
  "v_exp_f32 v177, v177\n"
  "v_mfma_f32_16x16x32_bf16 v[144:147], a[76:79], v[108:111], v[144:147]\n"
  "ds_read_b128 a[68:71], v218 offset:49664\n"
  "v_mfma_f32_16x16x32_bf16 v[148:151], a[92:95], v[108:111], v[148:151]\n"
  "s_waitcnt lgkmcnt(3)\n"
  "v_mfma_f32_16x16x32_bf16 v[0:3], a[96:99], v[152:155], v[0:3]\n"
  "v_exp_f32 v135, v135\n"
  "s_nop 2\n"
  "v_mfma_f32_16x16x32_bf16 a[32:35], a[128:131], v[152:155], a[32:35]\n"
  "ds_read_b128 a[84:87], v219 offset:49664\n"
  "v_cvt_pk_bf16_f32 v131, v134, v135\n"
  "v_mfma_f32_16x16x32_bf16 v[4:7], a[100:103], v[152:155], v[4:7]\n"
  "v_exp_f32 v136, v136\n"
  "v_mfma_f32_16x16x32_bf16 v[8:11], a[104:107], v[152:155], v[8:11]\n"
  "buffer_load_dwordx4 v235, s[44:47], s66 offen offset:1024 nt lds\n"
  "ds_read_b128 a[72:75], v218 offset:57344\n"
  "v_mfma_f32_16x16x32_bf16 v[12:15], a[108:111], v[152:155], v[12:15]\n"
  "v_exp_f32 v137, v137\n"
  "v_mfma_f32_16x16x32_bf16 v[16:19], a[112:115], v[152:155], v[16:19]\n"
  "ds_read_b128 a[88:91], v219 offset:57344\n"
  "v_cvt_pk_bf16_f32 v136, v136, v137\n"
  "v_mfma_f32_16x16x32_bf16 v[20:23], a[116:119], v[152:155], v[20:23]\n"
  "v_exp_f32 v178, v178\n"
  "v_mfma_f32_16x16x32_bf16 v[24:27], a[120:123], v[152:155], v[24:27]\n"
  "v_exp_f32 v138, v138\n"
  "v_mfma_f32_16x16x32_bf16 v[28:31], a[124:127], v[152:155], v[28:31]\n"
  "v_exp_f32 v139, v139\n"
  "s_nop 2\n"
  "v_mfma_f32_16x16x32_bf16 v[32:35], a[96:99], v[160:163], v[32:35]\n"
  "ds_read_b128 a[76:79], v218 offset:57856\n"
  "v_cvt_pk_bf16_f32 v137, v138, v139\n"
  "v_mfma_f32_16x16x32_bf16 a[36:39], a[128:131], v[160:163], a[36:39]\n"
  "v_exp_f32 v140, v140\n"
  "v_mfma_f32_16x16x32_bf16 v[36:39], a[100:103], v[160:163], v[36:39]\n"
  "ds_read_b128 a[92:95], v219 offset:57856\n"
  "v_mfma_f32_16x16x32_bf16 v[40:43], a[104:107], v[160:163], v[40:43]\n"
  "s_waitcnt vmcnt(10)\n"
  "s_barrier\n"
  "v_mfma_f32_16x16x32_bf16 v[44:47], a[108:111], v[160:163], v[44:47]\n"
  "v_exp_f32 v141, v141\n"
  "v_mfma_f32_16x16x32_bf16 v[48:51], a[112:115], v[160:163], v[48:51]\n"
  "v_exp_f32 v179, v179\n"
  "v_mfma_f32_16x16x32_bf16 v[52:55], a[116:119], v[160:163], v[52:55]\n"
  "v_cvt_pk_bf16_f32 v176, v176, v177\n"
  "v_cvt_pk_bf16_f32 v177, v178, v179\n"
  "v_mfma_f32_16x16x32_bf16 v[56:59], a[120:123], v[160:163], v[56:59]\n"
  "v_exp_f32 v142, v142\n"
  "s_nop 2\n"
  "v_mfma_f32_16x16x32_bf16 v[60:63], a[124:127], v[160:163], v[60:63]\n"
  "v_exp_f32 v143, v143\n"
  "v_mfma_f32_16x16x32_bf16 v[64:67], a[96:99], v[168:171], v[64:67]\n"
  "buffer_load_dwordx4 v236, s[44:47], s66 offen offset:2048 nt lds\n"
  "ds_read_b64_tr_b16 a[96:97], v220 offset:16384\n"
  "v_mfma_f32_16x16x32_bf16 a[40:43], a[128:131], v[168:171], a[40:43]\n"
  "ds_read_b64_tr_b16 a[98:99], v221 offset:16384\n"
  "v_cvt_pk_bf16_f32 v138, v140, v141\n"
  "v_mfma_f32_16x16x32_bf16 v[68:71], a[100:103], v[168:171], v[68:71]\n"
  "v_exp_f32 v144, v144\n"
  "v_mfma_f32_16x16x32_bf16 v[72:75], a[104:107], v[168:171], v[72:75]\n"
  "ds_read_b64_tr_b16 a[100:101], v230 offset:16384\n"
  "ds_read_b64_tr_b16 a[102:103], v231 offset:16384\n"
  "v_mfma_f32_16x16x32_bf16 v[76:79], a[108:111], v[168:171], v[76:79]\n"
  "v_exp_f32 v145, v145\n"
  "v_mfma_f32_16x16x32_bf16 v[80:83], a[112:115], v[168:171], v[80:83]\n"
  "ds_read_b64_tr_b16 a[104:105], v220 offset:16896\n"
  "ds_read_b64_tr_b16 a[106:107], v221 offset:16896\n"
  "v_mfma_f32_16x16x32_bf16 v[84:87], a[116:119], v[168:171], v[84:87]\n"
  "ds_read_b64_tr_b16 a[108:109], v230 offset:16896\n"
  "ds_read_b64_tr_b16 a[110:111], v231 offset:16896\n"
  "s_nop 2\n"
  "v_mfma_f32_16x16x32_bf16 v[88:91], a[120:123], v[168:171], v[88:91]\n"
  "v_exp_f32 v146, v146\n"
  "v_mfma_f32_16x16x32_bf16 v[92:95], a[124:127], v[168:171], v[92:95]\n"
  "s_waitcnt lgkmcnt(8)\n"
  "ds_read_b64_tr_b16 a[112:113], v220 offset:24576\n"
  "v_mfma_f32_16x16x32_bf16 v[152:155], a[64:67], v[200:203], v[184:187]\n"
  "v_exp_f32 v147, v147\n"
  "v_mfma_f32_16x16x32_bf16 v[156:159], a[80:83], v[200:203], v[184:187]\n"
  "ds_read_b64_tr_b16 a[114:115], v221 offset:24576\n"
  "ds_read_b64_tr_b16 a[116:117], v230 offset:24576\n"
  "v_mfma_f32_16x16x32_bf16 v[152:155], a[68:71], v[204:207], v[152:155]\n"
  "v_exp_f32 v148, v148\n"
  "v_mfma_f32_16x16x32_bf16 v[156:159], a[84:87], v[204:207], v[156:159]\n"
  "ds_read_b64_tr_b16 a[118:119], v231 offset:24576\n"
  "ds_read_b64_tr_b16 a[120:121], v220 offset:25088\n"
  "v_mfma_f32_16x16x32_bf16 v[152:155], a[72:75], v[208:211], v[152:155]\n"
  "ds_read_b64_tr_b16 a[122:123], v221 offset:25088\n"
  "ds_read_b64_tr_b16 a[124:125], v230 offset:25088\n"
  "v_mfma_f32_16x16x32_bf16 v[156:159], a[88:91], v[208:211], v[156:159]\n"
  "v_exp_f32 v149, v149\n"
  "s_nop 2\n"
  "v_mfma_f32_16x16x32_bf16 v[152:155], a[76:79], v[212:215], v[152:155]\n"
  "ds_read_b64_tr_b16 a[126:127], v231 offset:25088\n"
  "v_cvt_pk_bf16_f32 v139, v142, v143\n"
  "v_mfma_f32_16x16x32_bf16 v[156:159], a[92:95], v[212:215], v[156:159]\n"
  "v_exp_f32 v150, v150\n"
  "v_mfma_f32_16x16x32_bf16 v[160:163], a[64:67], v[112:115], v[188:191]\n"
  "buffer_load_dwordx4 v237, s[44:47], s66 offen offset:3072 nt lds\n"
  "v_cvt_pk_bf16_f32 v144, v144, v145\n"
  "v_mfma_f32_16x16x32_bf16 v[164:167], a[80:83], v[112:115], v[188:191]\n"
  "v_exp_f32 v151, v151\n"
  "v_mfma_f32_16x16x32_bf16 v[160:163], a[68:71], v[116:119], v[160:163]\n"
  "s_waitcnt vmcnt(8)\n"
  "s_barrier\n"
  "s_cmp_eq_u32 s19, 3\n"
  "s_cbranch_scc0 L_o14\n"
  "v_and_b32 v253, 31, v254\n"
  "v_lshlrev_b32 v253, 2, v253\n"
  "buffer_load_dword v252, v253, s[84:87], s98 offen sc1\n"
  "L_o14:\n"
  "v_mfma_f32_16x16x32_bf16 v[164:167], a[84:87], v[116:119], v[164:167]\n"
  "v_exp_f32 v152, v152\n"
  "v_mfma_f32_16x16x32_bf16 v[160:163], a[72:75], v[120:123], v[160:163]\n"
  "s_lshl_b32 s58, s19, 10\n"
  "v_add_u32 v245, s58, v251\n"
  "v_add_u32 v244, 0x4000, v245\n"
  "v_add_u32 v245, 0x18000, v245\n"
  "v_and_b32 v248, 0xf0, v251\n"
  "v_add_u32 v248, 0x8000, v248\n"
  "s_lshl_b32 s58, s19, 2\n"
  "v_add_u32 v246, s58, v248\n"
  "s_cmp_lt_u32 s19, 2\n"
  "s_mov_b32 s59, 0x4000\n"
  "s_cselect_b32 s58, s59, 0x14000\n"
  "s_lshl_b32 s59, s19, 13\n"
  "s_add_u32 s58, s58, s59\n"
  "v_add_u32 v247, s58, v251\n"
  "v_mfma_f32_16x16x32_bf16 v[164:167], a[88:91], v[120:123], v[164:167]\n"
  "v_exp_f32 v153, v153\n"
  "s_nop 2\n"
  "v_mfma_f32_16x16x32_bf16 v[160:163], a[76:79], v[124:127], v[160:163]\n"
  "v_cvt_pk_bf16_f32 v145, v146, v147\n"
  "v_cvt_pk_bf16_f32 v146, v148, v149\n"
  "v_mfma_f32_16x16x32_bf16 v[164:167], a[92:95], v[124:127], v[164:167]\n"
  "v_cvt_pk_bf16_f32 v147, v150, v151\n"
  "v_cvt_pk_bf16_f32 v152, v152, v153\n"
  "v_mfma_f32_16x16x32_bf16 v[168:171], a[64:67], v[96:99], v[192:195]\n"
  "v_exp_f32 v154, v154\n"
  "v_mfma_f32_16x16x32_bf16 v[172:175], a[80:83], v[96:99], v[192:195]\n"
  "ds_read_b128 a[64:67], v216\n"
  "v_mfma_f32_16x16x32_bf16 v[168:171], a[68:71], v[100:103], v[168:171]\n"
  "v_exp_f32 v155, v155\n"
  "v_mfma_f32_16x16x32_bf16 v[172:175], a[84:87], v[100:103], v[172:175]\n"
  "ds_read_b128 a[80:83], v217\n"
  "v_cvt_pk_bf16_f32 v153, v154, v155\n"
  "v_mfma_f32_16x16x32_bf16 v[168:171], a[72:75], v[104:107], v[168:171]\n"
  "v_mfma_f32_16x16x32_bf16 v[172:175], a[88:91], v[104:107], v[172:175]\n"
  "v_exp_f32 v156, v156\n"
  "s_nop 2\n"
  "v_mfma_f32_16x16x32_bf16 v[168:171], a[76:79], v[108:111], v[168:171]\n"
  "ds_read_b128 a[68:71], v216 offset:512\n"
  "v_mfma_f32_16x16x32_bf16 v[172:175], a[92:95], v[108:111], v[172:175]\n"
  "s_waitcnt lgkmcnt(3)\n"
  "s_add_u32 m0, s65, 0xc000\n"
  "v_mfma_f32_16x16x32_bf16 v[0:3], a[96:99], v[128:131], v[0:3]\n"
  "s_mov_b32 s67, 0x1c000\n"
  "buffer_load_dwordx4 v238, s[48:51], s67 offen nt lds\n"
  "v_mfma_f32_16x16x32_bf16 a[32:35], a[128:131], v[128:131], a[32:35]\n"
  "v_exp_f32 v157, v157\n"
  "v_mfma_f32_16x16x32_bf16 v[4:7], a[100:103], v[128:131], v[4:7]\n"
  "ds_read_b128 a[84:87], v217 offset:512\n"
  "v_cvt_pk_bf16_f32 v154, v156, v157\n"
  "v_mfma_f32_16x16x32_bf16 v[8:11], a[104:107], v[128:131], v[8:11]\n"
  "v_exp_f32 v158, v158\n"
  "v_mfma_f32_16x16x32_bf16 v[12:15], a[108:111], v[128:131], v[12:15]\n"
  "v_exp_f32 v159, v159\n"
  "v_mfma_f32_16x16x32_bf16 v[16:19], a[112:115], v[128:131], v[16:19]\n"
  "ds_read_b128 a[72:75], v216 offset:8192\n"
  "v_cvt_pk_bf16_f32 v155, v158, v159\n"
  "s_nop 2\n"
  "v_mfma_f32_16x16x32_bf16 v[20:23], a[116:119], v[128:131], v[20:23]\n"
  "v_mfma_f32_16x16x32_bf16 v[24:27], a[120:123], v[128:131], v[24:27]\n"
  "v_exp_f32 v160, v160\n"
  "v_mfma_f32_16x16x32_bf16 v[28:31], a[124:127], v[128:131], v[28:31]\n"
  "ds_read_b128 a[88:91], v217 offset:8192\n"
  "v_mfma_f32_16x16x16_bf16 a[0:3], a[96:97], v[176:177], a[0:3]\n"
  "v_exp_f32 v161, v161\n"
  "v_mfma_f32_16x16x16_bf16 a[4:7], a[100:101], v[176:177], a[4:7]\n"
  "ds_read_b128 a[76:79], v216 offset:8704\n"
  "v_cvt_pk_bf16_f32 v160, v160, v161\n"
  "v_mfma_f32_16x16x16_bf16 a[8:11], a[104:105], v[176:177], a[8:11]\n"
  "v_mfma_f32_16x16x16_bf16 a[12:15], a[108:109], v[176:177], a[12:15]\n"
  "v_exp_f32 v162, v162\n"
  "v_mfma_f32_16x16x16_bf16 a[16:19], a[112:113], v[176:177], a[16:19]\n"
  "ds_read_b128 a[92:95], v217 offset:8704\n"
  "s_nop 2\n"
  "v_mfma_f32_16x16x16_bf16 a[20:23], a[116:117], v[176:177], a[20:23]\n"
  "v_exp_f32 v163, v163\n"
  "v_mfma_f32_16x16x16_bf16 a[24:27], a[120:121], v[176:177], a[24:27]\n"
  "buffer_load_dwordx4 v239, s[48:51], s67 offen offset:1024 nt lds\n"
  "v_cvt_pk_bf16_f32 v161, v162, v163\n"
  "v_mfma_f32_16x16x16_bf16 a[28:31], a[124:125], v[176:177], a[28:31]\n"
  "v_mfma_f32_16x16x16_bf16 a[44:47], a[128:129], v[176:177], a[44:47]\n"
  "v_exp_f32 v164, v164\n"
  "v_mfma_f32_16x16x32_bf16 v[32:35], a[96:99], v[136:139], v[32:35]\n"
  "v_mfma_f32_16x16x32_bf16 a[36:39], a[128:131], v[136:139], a[36:39]\n"
  "v_exp_f32 v165, v165\n"
  "v_mfma_f32_16x16x32_bf16 v[36:39], a[100:103], v[136:139], v[36:39]\n"
  "v_cvt_pk_bf16_f32 v162, v164, v165\n"
  "v_mfma_f32_16x16x32_bf16 v[40:43], a[104:107], v[136:139], v[40:43]\n"
  "s_nop 2\n"
  "v_mfma_f32_16x16x32_bf16 v[44:47], a[108:111], v[136:139], v[44:47]\n"
  "v_exp_f32 v166, v166\n"
  "v_mfma_f32_16x16x32_bf16 v[48:51], a[112:115], v[136:139], v[48:51]\n"
  "v_mfma_f32_16x16x32_bf16 v[52:55], a[116:119], v[136:139], v[52:55]\n"
  "v_exp_f32 v167, v167\n"
  "v_mfma_f32_16x16x32_bf16 v[56:59], a[120:123], v[136:139], v[56:59]\n"
  "v_cvt_pk_bf16_f32 v163, v166, v167\n"
  "v_mfma_f32_16x16x32_bf16 v[60:63], a[124:127], v[136:139], v[60:63]\n"
  "v_mfma_f32_16x16x32_bf16 v[64:67], a[96:99], v[144:147], v[64:67]\n"
  "ds_read_b64_tr_b16 a[96:97], v228 offset:16384\n"
  "ds_read_b64_tr_b16 a[98:99], v229 offset:16384\n"
  "v_mfma_f32_16x16x32_bf16 a[40:43], a[128:131], v[144:147], a[40:43]\n"
  "v_exp_f32 v168, v168\n"
  "v_mfma_f32_16x16x32_bf16 v[68:71], a[100:103], v[144:147], v[68:71]\n"
  "ds_read_b64_tr_b16 a[100:101], v232 offset:16384\n"
  "ds_read_b64_tr_b16 a[102:103], v233 offset:16384\n"
  "s_nop 2\n"
  "v_mfma_f32_16x16x32_bf16 v[72:75], a[104:107], v[144:147], v[72:75]\n"
  "v_exp_f32 v169, v169\n"
  "v_mfma_f32_16x16x32_bf16 v[76:79], a[108:111], v[144:147], v[76:79]\n"
  "buffer_load_dwordx4 v238, s[48:51], s67 offen offset:2048 nt lds\n"
  "ds_read_b64_tr_b16 a[104:105], v228 offset:16896\n"
  "v_mfma_f32_16x16x32_bf16 v[80:83], a[112:115], v[144:147], v[80:83]\n"
  "ds_read_b64_tr_b16 a[106:107], v229 offset:16896\n"
  "ds_read_b64_tr_b16 a[108:109], v232 offset:16896\n"
  "v_mfma_f32_16x16x32_bf16 v[84:87], a[116:119], v[144:147], v[84:87]\n"
  "v_exp_f32 v170, v170\n"
  "v_mfma_f32_16x16x32_bf16 v[88:91], a[120:123], v[144:147], v[88:91]\n"
  "ds_read_b64_tr_b16 a[110:111], v233 offset:16896\n"
  "ds_read_b64_tr_b16 a[112:113], v228 offset:24576\n"
  "v_mfma_f32_16x16x32_bf16 v[92:95], a[124:127], v[144:147], v[92:95]\n"
  "s_waitcnt lgkmcnt(9)\n"
  "ds_read_b64_tr_b16 a[114:115], v229 offset:24576\n"
  "v_mfma_f32_16x16x32_bf16 v[128:131], a[64:67], v[200:203], v[184:187]\n"
  "v_exp_f32 v171, v171\n"
  "v_mfma_f32_16x16x32_bf16 v[132:135], a[80:83], v[200:203], v[184:187]\n"
  "ds_read_b64_tr_b16 a[116:117], v232 offset:24576\n"
  "ds_read_b64_tr_b16 a[118:119], v233 offset:24576\n"
  "s_nop 2\n"
  "v_mfma_f32_16x16x32_bf16 v[128:131], a[68:71], v[204:207], v[128:131]\n"
  "v_exp_f32 v172, v172\n"
  "v_mfma_f32_16x16x32_bf16 v[132:135], a[84:87], v[204:207], v[132:135]\n"
  "ds_read_b64_tr_b16 a[120:121], v228 offset:25088\n"
  "ds_read_b64_tr_b16 a[122:123], v229 offset:25088\n"
  "v_mfma_f32_16x16x32_bf16 v[128:131], a[72:75], v[208:211], v[128:131]\n"
  "v_exp_f32 v173, v173\n"
  "v_mfma_f32_16x16x32_bf16 v[132:135], a[88:91], v[208:211], v[132:135]\n"
  "ds_read_b64_tr_b16 a[124:125], v232 offset:25088\n"
  "ds_read_b64_tr_b16 a[126:127], v233 offset:25088\n"
  "v_mfma_f32_16x16x32_bf16 v[128:131], a[76:79], v[212:215], v[128:131]\n"
  "v_exp_f32 v174, v174\n"
  "v_mfma_f32_16x16x32_bf16 v[132:135], a[92:95], v[212:215], v[132:135]\n"
  "v_cvt_pk_bf16_f32 v168, v168, v169\n"
  "v_cvt_pk_bf16_f32 v169, v170, v171\n"
  "v_mfma_f32_16x16x32_bf16 v[176:179], a[64:67], a[48:51], v[196:199]\n"
  "v_exp_f32 v175, v175\n"
  "v_mfma_f32_16x16x32_bf16 v[176:179], a[68:71], a[52:55], v[176:179]\n"
  "v_cvt_pk_bf16_f32 v170, v172, v173\n"
  "v_cvt_pk_bf16_f32 v171, v174, v175\n"
  "s_nop 2\n"
  "v_mfma_f32_16x16x32_bf16 v[176:179], a[72:75], a[56:59], v[176:179]\n"
  "v_mfma_f32_16x16x32_bf16 v[176:179], a[76:79], a[60:63], v[176:179]\n"
  "v_exp_f32 v128, v128\n"
  "v_mfma_f32_16x16x32_bf16 v[136:139], a[64:67], v[112:115], v[188:191]\n"
  "buffer_load_dwordx4 v239, s[48:51], s67 offen offset:3072 nt lds\n"
  "v_mfma_f32_16x16x32_bf16 v[140:143], a[80:83], v[112:115], v[188:191]\n"
  "v_exp_f32 v129, v129\n"
  "v_mfma_f32_16x16x32_bf16 v[136:139], a[68:71], v[116:119], v[136:139]\n"
  "v_cvt_pk_bf16_f32 v128, v128, v129\n"
  "v_mfma_f32_16x16x32_bf16 v[140:143], a[84:87], v[116:119], v[140:143]\n"
  "v_exp_f32 v130, v130\n"
  "v_mfma_f32_16x16x32_bf16 v[136:139], a[72:75], v[120:123], v[136:139]\n"
  "v_exp_f32 v176, v176\n"
  "v_mfma_f32_16x16x32_bf16 v[140:143], a[88:91], v[120:123], v[140:143]\n"
  "v_exp_f32 v131, v131\n"
  "s_nop 2\n"
  "v_mfma_f32_16x16x32_bf16 v[136:139], a[76:79], v[124:127], v[136:139]\n"
  "v_cvt_pk_bf16_f32 v129, v130, v131\n"
  "v_mfma_f32_16x16x32_bf16 v[140:143], a[92:95], v[124:127], v[140:143]\n"
  "v_mfma_f32_16x16x32_bf16 v[144:147], a[64:67], v[96:99], v[192:195]\n"
  "v_exp_f32 v132, v132\n"
  "v_mfma_f32_16x16x32_bf16 v[148:151], a[80:83], v[96:99], v[192:195]\n"
  "ds_read_b128 a[64:67], v218\n"
  "v_mfma_f32_16x16x32_bf16 v[144:147], a[68:71], v[100:103], v[144:147]\n"
  "v_exp_f32 v133, v133\n"
  "v_mfma_f32_16x16x32_bf16 v[148:151], a[84:87], v[100:103], v[148:151]\n"
  "ds_read_b128 a[80:83], v219\n"
  "v_cvt_pk_bf16_f32 v130, v132, v133\n"
  "v_mfma_f32_16x16x32_bf16 v[144:147], a[72:75], v[104:107], v[144:147]\n"
  "v_exp_f32 v134, v134\n"
  "v_mfma_f32_16x16x32_bf16 v[148:151], a[88:91], v[104:107], v[148:151]\n"
  "v_exp_f32 v177, v177\n"
  "s_nop 2\n"
  "v_mfma_f32_16x16x32_bf16 v[144:147], a[76:79], v[108:111], v[144:147]\n"
  "ds_read_b128 a[68:71], v218 offset:512\n"
  "v_mfma_f32_16x16x32_bf16 v[148:151], a[92:95], v[108:111], v[148:151]\n"
  "s_waitcnt lgkmcnt(3)\n"
  "v_mfma_f32_16x16x32_bf16 v[0:3], a[96:99], v[152:155], v[0:3]\n"
  "v_exp_f32 v135, v135\n"
  "v_mfma_f32_16x16x32_bf16 a[32:35], a[128:131], v[152:155], a[32:35]\n"
  "ds_read_b128 a[84:87], v219 offset:512\n"
  "v_cvt_pk_bf16_f32 v131, v134, v135\n"
  "v_mfma_f32_16x16x32_bf16 v[4:7], a[100:103], v[152:155], v[4:7]\n"
  "v_exp_f32 v136, v136\n"
  "v_mfma_f32_16x16x32_bf16 v[8:11], a[104:107], v[152:155], v[8:11]\n"
  "v_exp_f32 v137, v137\n"
  "v_mfma_f32_16x16x32_bf16 v[12:15], a[108:111], v[152:155], v[12:15]\n"
  "ds_read_b128 a[72:75], v218 offset:8192\n"
  "v_cvt_pk_bf16_f32 v136, v136, v137\n"
  "v_mfma_f32_16x16x32_bf16 v[16:19], a[112:115], v[152:155], v[16:19]\n"
  "v_exp_f32 v178, v178\n"
  "s_nop 2\n"
  "v_mfma_f32_16x16x32_bf16 v[20:23], a[116:119], v[152:155], v[20:23]\n"
  "v_exp_f32 v138, v138\n"
  "v_mfma_f32_16x16x32_bf16 v[24:27], a[120:123], v[152:155], v[24:27]\n"
  "ds_read_b128 a[88:91], v219 offset:8192\n"
  "v_mfma_f32_16x16x32_bf16 v[28:31], a[124:127], v[152:155], v[28:31]\n"
  "v_exp_f32 v139, v139\n"
  "v_mfma_f32_16x16x32_bf16 v[32:35], a[96:99], v[160:163], v[32:35]\n"
  "ds_read_b128 a[76:79], v218 offset:8704\n"
  "v_cvt_pk_bf16_f32 v137, v138, v139\n"
  "v_mfma_f32_16x16x32_bf16 a[36:39], a[128:131], v[160:163], a[36:39]\n"
  "v_exp_f32 v140, v140\n"
  "v_mfma_f32_16x16x32_bf16 v[36:39], a[100:103], v[160:163], v[36:39]\n"
  "ds_read_b128 a[92:95], v219 offset:8704\n"
  "v_mfma_f32_16x16x32_bf16 v[40:43], a[104:107], v[160:163], v[40:43]\n"
  "s_waitcnt vmcnt(8)\n"
  "s_barrier\n"
  "v_mfma_f32_16x16x32_bf16 v[44:47], a[108:111], v[160:163], v[44:47]\n"
  "v_exp_f32 v141, v141\n"
  "s_nop 2\n"
  "v_mfma_f32_16x16x32_bf16 v[48:51], a[112:115], v[160:163], v[48:51]\n"
  "v_exp_f32 v179, v179\n"
  "v_mfma_f32_16x16x32_bf16 v[52:55], a[116:119], v[160:163], v[52:55]\n"
  "v_cvt_pk_bf16_f32 v176, v176, v177\n"
  "v_cvt_pk_bf16_f32 v177, v178, v179\n"
  "v_mfma_f32_16x16x32_bf16 v[56:59], a[120:123], v[160:163], v[56:59]\n"
  "v_exp_f32 v142, v142\n"
  "v_mfma_f32_16x16x32_bf16 v[60:63], a[124:127], v[160:163], v[60:63]\n"
  "v_exp_f32 v143, v143\n"
  "v_mfma_f32_16x16x32_bf16 v[64:67], a[96:99], v[168:171], v[64:67]\n"
  "ds_read_b64_tr_b16 a[96:97], v220 offset:32768\n"
  "ds_read_b64_tr_b16 a[98:99], v221 offset:32768\n"
  "v_mfma_f32_16x16x32_bf16 a[40:43], a[128:131], v[168:171], a[40:43]\n"
  "v_exp_f32 v144, v144\n"
  "v_mfma_f32_16x16x32_bf16 v[68:71], a[100:103], v[168:171], v[68:71]\n"
  "ds_read_b64_tr_b16 a[100:101], v230 offset:32768\n"
  "ds_read_b64_tr_b16 a[102:103], v231 offset:32768\n"
  "v_mfma_f32_16x16x32_bf16 v[72:75], a[104:107], v[168:171], v[72:75]\n"
  "ds_read_b64_tr_b16 a[104:105], v220 offset:33280\n"
  "ds_read_b64_tr_b16 a[106:107], v221 offset:33280\n"
  "s_nop 2\n"
  "v_mfma_f32_16x16x32_bf16 v[76:79], a[108:111], v[168:171], v[76:79]\n"
  "v_exp_f32 v145, v145\n"
  "v_mfma_f32_16x16x32_bf16 v[80:83], a[112:115], v[168:171], v[80:83]\n"
  "ds_read_b64_tr_b16 a[108:109], v230 offset:33280\n"
  "ds_read_b64_tr_b16 a[110:111], v231 offset:33280\n"
  "v_mfma_f32_16x16x32_bf16 v[84:87], a[116:119], v[168:171], v[84:87]\n"
  "ds_read_b64_tr_b16 a[112:113], v220 offset:40960\n"
  "ds_read_b64_tr_b16 a[114:115], v221 offset:40960\n"
  "v_mfma_f32_16x16x32_bf16 v[88:91], a[120:123], v[168:171], v[88:91]\n"
  "v_exp_f32 v146, v146\n"
  "v_mfma_f32_16x16x32_bf16 v[92:95], a[124:127], v[168:171], v[92:95]\n"
  "s_waitcnt lgkmcnt(10)\n"
  "ds_read_b64_tr_b16 a[116:117], v230 offset:40960\n"
  "v_mfma_f32_16x16x32_bf16 v[152:155], a[64:67], v[200:203], v[184:187]\n"
  "v_exp_f32 v147, v147\n"
  "v_mfma_f32_16x16x32_bf16 v[156:159], a[80:83], v[200:203], v[184:187]\n"
  "ds_read_b64_tr_b16 a[118:119], v231 offset:40960\n"
  "ds_read_b64_tr_b16 a[120:121], v220 offset:41472\n"
  "v_mfma_f32_16x16x32_bf16 v[152:155], a[68:71], v[204:207], v[152:155]\n"
  "v_exp_f32 v148, v148\n"
  "s_nop 2\n"
  "v_mfma_f32_16x16x32_bf16 v[156:159], a[84:87], v[204:207], v[156:159]\n"
  "ds_read_b64_tr_b16 a[122:123], v221 offset:41472\n"
  "ds_read_b64_tr_b16 a[124:125], v230 offset:41472\n"
  "v_mfma_f32_16x16x32_bf16 v[152:155], a[72:75], v[208:211], v[152:155]\n"
  "ds_read_b64_tr_b16 a[126:127], v231 offset:41472\n"
  "v_cvt_pk_bf16_f32 v138, v140, v141\n"
  "v_mfma_f32_16x16x32_bf16 v[156:159], a[88:91], v[208:211], v[156:159]\n"
  "v_exp_f32 v149, v149\n"
  "v_mfma_f32_16x16x32_bf16 v[152:155], a[76:79], v[212:215], v[152:155]\n"
  "v_exp_f32 v150, v150\n"
  "v_mfma_f32_16x16x32_bf16 v[156:159], a[92:95], v[212:215], v[156:159]\n"
  "v_cvt_pk_bf16_f32 v139, v142, v143\n"
  "v_cvt_pk_bf16_f32 v144, v144, v145\n"
  "v_mfma_f32_16x16x32_bf16 v[160:163], a[64:67], v[112:115], v[188:191]\n"
  "v_exp_f32 v151, v151\n"
  "v_mfma_f32_16x16x32_bf16 v[164:167], a[80:83], v[112:115], v[188:191]\n"
  "v_cvt_pk_bf16_f32 v145, v146, v147\n"
  "v_cvt_pk_bf16_f32 v146, v148, v149\n"
  "v_mfma_f32_16x16x32_bf16 v[160:163], a[68:71], v[116:119], v[160:163]\n"
  "s_waitcnt vmcnt(4)\n"
  "s_barrier\n"
  "s_nop 2\n"
  "v_mfma_f32_16x16x32_bf16 v[164:167], a[84:87], v[116:119], v[164:167]\n"
  "v_exp_f32 v152, v152\n"
  "v_mfma_f32_16x16x32_bf16 v[160:163], a[72:75], v[120:123], v[160:163]\n"
  "v_cvt_pk_bf16_f32 v147, v150, v151\n"
  "v_mfma_f32_16x16x32_bf16 v[164:167], a[88:91], v[120:123], v[164:167]\n"
  "L_st5:\n"
  "s_cbranch_execz L_stx\n"
  "v_exp_f32 v153, v153\n"
  "v_mfma_f32_16x16x32_bf16 v[160:163], a[76:79], v[124:127], v[160:163]\n"
  "v_cvt_pk_bf16_f32 v152, v152, v153\n"
  "v_mfma_f32_16x16x32_bf16 v[164:167], a[92:95], v[124:127], v[164:167]\n"
  "v_mfma_f32_16x16x32_bf16 v[168:171], a[64:67], v[96:99], v[192:195]\n"
  "v_exp_f32 v154, v154\n"
  "v_mfma_f32_16x16x32_bf16 v[172:175], a[80:83], v[96:99], v[192:195]\n"
  "ds_read_b128 a[64:67], v216 offset:16384\n"
  "v_mfma_f32_16x16x32_bf16 v[168:171], a[68:71], v[100:103], v[168:171]\n"
  "v_exp_f32 v155, v155\n"
  "v_mfma_f32_16x16x32_bf16 v[172:175], a[84:87], v[100:103], v[172:175]\n"
  "ds_read_b128 a[80:83], v217 offset:16384\n"
  "v_cvt_pk_bf16_f32 v153, v154, v155\n"
  "v_mfma_f32_16x16x32_bf16 v[168:171], a[72:75], v[104:107], v[168:171]\n"
  "v_mfma_f32_16x16x32_bf16 v[172:175], a[88:91], v[104:107], v[172:175]\n"
  "v_exp_f32 v156, v156\n"
  "v_mfma_f32_16x16x32_bf16 v[168:171], a[76:79], v[108:111], v[168:171]\n"
  "ds_read_b128 a[68:71], v216 offset:16896\n"
  "v_mfma_f32_16x16x32_bf16 v[172:175], a[92:95], v[108:111], v[172:175]\n"
  "s_waitcnt lgkmcnt(3)\n"
  "v_mfma_f32_16x16x32_bf16 v[0:3], a[96:99], v[128:131], v[0:3]\n"
  "v_exp_f32 v157, v157\n"
  "v_mfma_f32_16x16x32_bf16 a[32:35], a[128:131], v[128:131], a[32:35]\n"
  "ds_read_b128 a[84:87], v217 offset:16896\n"
  "v_cvt_pk_bf16_f32 v154, v156, v157\n"
  "v_mfma_f32_16x16x32_bf16 v[4:7], a[100:103], v[128:131], v[4:7]\n"
  "v_exp_f32 v158, v158\n"
  "v_mfma_f32_16x16x32_bf16 v[8:11], a[104:107], v[128:131], v[8:11]\n"
  "ds_read_b128 a[72:75], v216 offset:24576\n"
  "v_mfma_f32_16x16x32_bf16 v[12:15], a[108:111], v[128:131], v[12:15]\n"
  "s_add_u32 m0, s65, 0x20000\n"
  "s_add_u32 s66, s88, 1\n"
  "v_mfma_f32_16x16x32_bf16 v[16:19], a[112:115], v[128:131], v[16:19]\n"
  "s_and_b32 s66, s66, 3\n"
  "s_lshl_b32 s66, s66, 17\n"
  "v_mfma_f32_16x16x32_bf16 v[20:23], a[116:119], v[128:131], v[20:23]\n"
  "buffer_load_dwordx4 v240, s[72:75], s66 offen lds\n"
  "ds_read_b128 a[88:91], v217 offset:24576\n"
  "v_mfma_f32_16x16x32_bf16 v[24:27], a[120:123], v[128:131], v[24:27]\n"
  "v_exp_f32 v159, v159\n"
  "v_mfma_f32_16x16x32_bf16 v[28:31], a[124:127], v[128:131], v[28:31]\n"
  "v_exp_f32 v160, v160\n"
  "v_mfma_f32_16x16x16_bf16 a[0:3], a[96:97], v[176:177], a[0:3]\n"
  "ds_read_b128 a[76:79], v216 offset:25088\n"
  "v_cvt_pk_bf16_f32 v155, v158, v159\n"
  "v_mfma_f32_16x16x16_bf16 a[4:7], a[100:101], v[176:177], a[4:7]\n"
  "v_exp_f32 v161, v161\n"
  "v_mfma_f32_16x16x16_bf16 a[8:11], a[104:105], v[176:177], a[8:11]\n"
  "buffer_load_dwordx4 v241, s[72:75], s66 offen offset:1024 lds\n"
  "ds_read_b128 a[92:95], v217 offset:25088\n"
  "v_mfma_f32_16x16x16_bf16 a[12:15], a[108:109], v[176:177], a[12:15]\n"
  "v_exp_f32 v162, v162\n"
  "v_mfma_f32_16x16x16_bf16 a[16:19], a[112:113], v[176:177], a[16:19]\n"
  "v_cvt_pk_bf16_f32 v160, v160, v161\n"
  "v_mfma_f32_16x16x16_bf16 a[20:23], a[116:117], v[176:177], a[20:23]\n"
  "v_exp_f32 v163, v163\n"
  "v_mfma_f32_16x16x16_bf16 a[24:27], a[120:121], v[176:177], a[24:27]\n"
  "v_cvt_pk_bf16_f32 v161, v162, v163\n"
  "v_mfma_f32_16x16x16_bf16 a[28:31], a[124:125], v[176:177], a[28:31]\n"
  "v_mfma_f32_16x16x16_bf16 a[44:47], a[128:129], v[176:177], a[44:47]\n"
  "v_exp_f32 v164, v164\n"
  "v_mfma_f32_16x16x32_bf16 v[32:35], a[96:99], v[136:139], v[32:35]\n"
  "buffer_load_dwordx4 v242, s[72:75], s66 offen offset:2048 lds\n"
  "v_mfma_f32_16x16x32_bf16 a[36:39], a[128:131], v[136:139], a[36:39]\n"
  "v_exp_f32 v165, v165\n"
  "v_mfma_f32_16x16x32_bf16 v[36:39], a[100:103], v[136:139], v[36:39]\n"
  "v_cvt_pk_bf16_f32 v162, v164, v165\n"
  "v_mfma_f32_16x16x32_bf16 v[40:43], a[104:107], v[136:139], v[40:43]\n"
  "v_mfma_f32_16x16x32_bf16 v[44:47], a[108:111], v[136:139], v[44:47]\n"
  "v_exp_f32 v166, v166\n"
  "v_mfma_f32_16x16x32_bf16 v[48:51], a[112:115], v[136:139], v[48:51]\n"
  "v_mfma_f32_16x16x32_bf16 v[52:55], a[116:119], v[136:139], v[52:55]\n"
  "v_exp_f32 v167, v167\n"
  "v_mfma_f32_16x16x32_bf16 v[56:59], a[120:123], v[136:139], v[56:59]\n"
  "buffer_load_dwordx4 v243, s[72:75], s66 offen offset:3072 lds\n"
  "v_cvt_pk_bf16_f32 v163, v166, v167\n"
  "v_mfma_f32_16x16x32_bf16 v[60:63], a[124:127], v[136:139], v[60:63]\n"
  "v_mfma_f32_16x16x32_bf16 v[64:67], a[96:99], v[144:147], v[64:67]\n"
  "ds_read_b64_tr_b16 a[96:97], v228 offset:32768\n"
  "ds_read_b64_tr_b16 a[98:99], v229 offset:32768\n"
  "v_mfma_f32_16x16x32_bf16 a[40:43], a[128:131], v[144:147], a[40:43]\n"
  "v_exp_f32 v168, v168\n"
  "v_mfma_f32_16x16x32_bf16 v[68:71], a[100:103], v[144:147], v[68:71]\n"
  "ds_read_b64_tr_b16 a[100:101], v232 offset:32768\n"
  "ds_read_b64_tr_b16 a[102:103], v233 offset:32768\n"
  "v_mfma_f32_16x16x32_bf16 v[72:75], a[104:107], v[144:147], v[72:75]\n"
  "v_exp_f32 v169, v169\n"
  "v_mfma_f32_16x16x32_bf16 v[76:79], a[108:111], v[144:147], v[76:79]\n"
  "ds_read_b64_tr_b16 a[104:105], v228 offset:33280\n"
  "ds_read_b64_tr_b16 a[106:107], v229 offset:33280\n"
  "v_mfma_f32_16x16x32_bf16 v[80:83], a[112:115], v[144:147], v[80:83]\n"
  "ds_read_b64_tr_b16 a[108:109], v232 offset:33280\n"
  "ds_read_b64_tr_b16 a[110:111], v233 offset:33280\n"
  "v_mfma_f32_16x16x32_bf16 v[84:87], a[116:119], v[144:147], v[84:87]\n"
  "v_exp_f32 v170, v170\n"
  "v_mfma_f32_16x16x32_bf16 v[88:91], a[120:123], v[144:147], v[88:91]\n"
  "ds_read_b64_tr_b16 a[112:113], v228 offset:40960\n"
  "ds_read_b64_tr_b16 a[114:115], v229 offset:40960\n"
  "v_mfma_f32_16x16x32_bf16 v[92:95], a[124:127], v[144:147], v[92:95]\n"
  "s_waitcnt lgkmcnt(10)\n"
  "ds_read_b64_tr_b16 a[116:117], v232 offset:40960\n"
  "v_mfma_f32_16x16x32_bf16 v[128:131], a[64:67], v[200:203], v[184:187]\n"
  "v_exp_f32 v171, v171\n"
  "v_mfma_f32_16x16x32_bf16 v[132:135], a[80:83], v[200:203], v[184:187]\n"
  "ds_read_b64_tr_b16 a[118:119], v233 offset:40960\n"
  "ds_read_b64_tr_b16 a[120:121], v228 offset:41472\n"
  "v_mfma_f32_16x16x32_bf16 v[128:131], a[68:71], v[204:207], v[128:131]\n"
  "s_add_u32 m0, s65, 0\n"
  "s_add_u32 s67, s88, 2\n"
  "v_mfma_f32_16x16x32_bf16 v[132:135], a[84:87], v[204:207], v[132:135]\n"
  "s_and_b32 s67, s67, 3\n"
  "s_lshl_b32 s67, s67, 17\n"
  "v_mfma_f32_16x16x32_bf16 v[128:131], a[72:75], v[208:211], v[128:131]\n"
  "buffer_load_dwordx4 v240, s[72:75], s67 offen lds\n"
  "ds_read_b64_tr_b16 a[122:123], v229 offset:41472\n"
  "v_mfma_f32_16x16x32_bf16 v[132:135], a[88:91], v[208:211], v[132:135]\n"
  "v_exp_f32 v172, v172\n"
  "v_mfma_f32_16x16x32_bf16 v[128:131], a[76:79], v[212:215], v[128:131]\n"
  "v_exp_f32 v173, v173\n"
  "v_mfma_f32_16x16x32_bf16 v[132:135], a[92:95], v[212:215], v[132:135]\n"
  "v_exp_f32 v174, v174\n"
  "v_mfma_f32_16x16x32_bf16 v[176:179], a[64:67], a[48:51], v[196:199]\n"
  "ds_read_b64_tr_b16 a[124:125], v232 offset:41472\n"
  "ds_read_b64_tr_b16 a[126:127], v233 offset:41472\n"
  "v_mfma_f32_16x16x32_bf16 v[176:179], a[68:71], a[52:55], v[176:179]\n"
  "v_exp_f32 v175, v175\n"
  "v_mfma_f32_16x16x32_bf16 v[176:179], a[72:75], a[56:59], v[176:179]\n"
  "buffer_load_dwordx4 v241, s[72:75], s67 offen offset:1024 lds\n"
  "ds_read_b128 a[196:199], v218 offset:16384\n"
  "v_mfma_f32_16x16x32_bf16 v[176:179], a[76:79], a[60:63], v[176:179]\n"
  "v_exp_f32 v128, v128\n"
  "v_mfma_f32_16x16x32_bf16 v[136:139], a[64:67], v[112:115], v[188:191]\n"
  "v_exp_f32 v129, v129\n"
  "v_mfma_f32_16x16x32_bf16 v[140:143], a[80:83], v[112:115], v[188:191]\n"
  "v_exp_f32 v130, v130\n"
  "v_mfma_f32_16x16x32_bf16 v[136:139], a[68:71], v[116:119], v[136:139]\n"
  "v_exp_f32 v131, v131\n"
  "v_mfma_f32_16x16x32_bf16 v[140:143], a[84:87], v[116:119], v[140:143]\n"
  "v_exp_f32 v132, v132\n"
  "v_mfma_f32_16x16x32_bf16 v[136:139], a[72:75], v[120:123], v[136:139]\n"
  "ds_read_b128 a[212:215], v219 offset:16384\n"
  "ds_read_b128 a[200:203], v218 offset:16896\n"
  "v_mfma_f32_16x16x32_bf16 v[140:143], a[88:91], v[120:123], v[140:143]\n"
  "ds_read_b128 a[216:219], v219 offset:16896\n"
  "ds_read_b128 a[204:207], v218 offset:24576\n"
  "v_mfma_f32_16x16x32_bf16 v[136:139], a[76:79], v[124:127], v[136:139]\n"
  "buffer_load_dwordx4 v242, s[72:75], s67 offen offset:2048 lds\n"
  "ds_read_b128 a[220:223], v219 offset:24576\n"
  "ds_read_b128 a[208:211], v218 offset:25088\n"
  "v_mfma_f32_16x16x32_bf16 v[140:143], a[92:95], v[124:127], v[140:143]\n"
  "s_waitcnt lgkmcnt(7)\n"
  "ds_read_b128 a[224:227], v219 offset:25088\n"
  "v_mfma_f32_16x16x32_bf16 v[0:3], a[96:99], v[152:155], v[0:3]\n"
  "v_exp_f32 v133, v133\n"
  "v_mfma_f32_16x16x32_bf16 a[32:35], a[128:131], v[152:155], a[32:35]\n"
  "s_waitcnt vmcnt(7)\n"
  "s_barrier\n"
  "v_mfma_f32_16x16x32_bf16 v[4:7], a[100:103], v[152:155], v[4:7]\n"
  "v_exp_f32 v134, v134\n"
  "v_mfma_f32_16x16x32_bf16 v[8:11], a[104:107], v[152:155], v[8:11]\n"
  "v_exp_f32 v135, v135\n"
  "v_mfma_f32_16x16x32_bf16 v[12:15], a[108:111], v[152:155], v[12:15]\n"
  "v_exp_f32 v176, v176\n"
  "v_mfma_f32_16x16x32_bf16 v[16:19], a[112:115], v[152:155], v[16:19]\n"
  "buffer_load_dwordx4 v243, s[72:75], s67 offen offset:3072 lds\n"
  "ds_read_b64_tr_b16 a[132:133], v220 offset:49152\n"
  "v_mfma_f32_16x16x32_bf16 v[20:23], a[116:119], v[152:155], v[20:23]\n"
  "v_exp_f32 v177, v177\n"
  "v_mfma_f32_16x16x32_bf16 v[24:27], a[120:123], v[152:155], v[24:27]\n"
  "ds_read_b64_tr_b16 a[134:135], v221 offset:49152\n"
  "ds_read_b64_tr_b16 a[136:137], v230 offset:49152\n"
  "v_mfma_f32_16x16x32_bf16 v[28:31], a[124:127], v[152:155], v[28:31]\n"
  "s_waitcnt lgkmcnt(3)\n"
  "ds_read_b64_tr_b16 a[138:139], v231 offset:49152\n"
  "v_mfma_f32_16x16x32_bf16 v[152:155], a[196:199], v[200:203], v[184:187]\n"
  "ds_read_b64_tr_b16 a[140:141], v220 offset:49664\n"
  "ds_read_b64_tr_b16 a[142:143], v221 offset:49664\n"
  "v_mfma_f32_16x16x32_bf16 v[156:159], a[212:215], v[200:203], v[184:187]\n"
  "ds_read_b64_tr_b16 a[144:145], v230 offset:49664\n"
  "ds_read_b64_tr_b16 a[146:147], v231 offset:49664\n"
  "v_mfma_f32_16x16x32_bf16 v[152:155], a[200:203], v[204:207], v[152:155]\n"
  "ds_read_b64_tr_b16 a[148:149], v220 offset:57344\n"
  "ds_read_b64_tr_b16 a[150:151], v221 offset:57344\n"
  "v_mfma_f32_16x16x32_bf16 v[156:159], a[216:219], v[204:207], v[156:159]\n"
  "ds_read_b64_tr_b16 a[152:153], v230 offset:57344\n"
  "ds_read_b64_tr_b16 a[154:155], v231 offset:57344\n"
  "v_mfma_f32_16x16x32_bf16 v[152:155], a[204:207], v[208:211], v[152:155]\n"
  "ds_read_b64_tr_b16 a[156:157], v220 offset:57856\n"
  "ds_read_b64_tr_b16 a[158:159], v221 offset:57856\n"
  "v_mfma_f32_16x16x32_bf16 v[156:159], a[220:223], v[208:211], v[156:159]\n"
  "v_exp_f32 v178, v178\n"
  "v_mfma_f32_16x16x32_bf16 v[152:155], a[208:211], v[212:215], v[152:155]\n"
  "ds_read_b64_tr_b16 a[160:161], v230 offset:57856\n"
  "ds_read_b64_tr_b16 a[162:163], v231 offset:57856\n"
  "v_mfma_f32_16x16x32_bf16 v[156:159], a[224:227], v[212:215], v[156:159]\n"
  "v_cvt_pk_bf16_f32 v168, v168, v169\n"
  "v_cvt_pk_bf16_f32 v169, v170, v171\n"
  "v_mfma_f32_16x16x32_bf16 v[32:35], a[96:99], v[160:163], v[32:35]\n"
  "s_add_u32 m0, s65, 0x24000\n"
  "s_add_u32 s66, s88, 3\n"
  "v_mfma_f32_16x16x32_bf16 a[36:39], a[128:131], v[160:163], a[36:39]\n"
  "s_and_b32 s66, s66, 3\n"
  "s_lshl_b32 s66, s66, 17\n"
  "v_mfma_f32_16x16x32_bf16 v[36:39], a[100:103], v[160:163], v[36:39]\n"
  "buffer_load_dwordx4 v240, s[72:75], s66 offen lds\n"
  "v_cvt_pk_bf16_f32 v170, v172, v173\n"
  "v_mfma_f32_16x16x32_bf16 v[40:43], a[104:107], v[160:163], v[40:43]\n"
  "v_cvt_pk_bf16_f32 v171, v174, v175\n"
  "v_cvt_pk_bf16_f32 v128, v128, v129\n"
  "v_mfma_f32_16x16x32_bf16 v[44:47], a[108:111], v[160:163], v[44:47]\n"
  "v_cvt_pk_bf16_f32 v129, v130, v131\n"
  "v_cvt_pk_bf16_f32 v130, v132, v133\n"
  "v_mfma_f32_16x16x32_bf16 v[48:51], a[112:115], v[160:163], v[48:51]\n"
  "v_cvt_pk_bf16_f32 v131, v134, v135\n"
  "v_mfma_f32_16x16x32_bf16 v[52:55], a[116:119], v[160:163], v[52:55]\n"
  "v_exp_f32 v179, v179\n"
  "v_mfma_f32_16x16x32_bf16 v[56:59], a[120:123], v[160:163], v[56:59]\n"
  "v_exp_f32 v152, v152\n"
  "v_mfma_f32_16x16x32_bf16 v[60:63], a[124:127], v[160:163], v[60:63]\n"
  "s_waitcnt lgkmcnt(0)\n"
  "buffer_load_dwordx4 v241, s[72:75], s66 offen offset:1024 lds\n"
  "v_mfma_f32_16x16x32_bf16 v[0:3], a[132:135], v[128:131], v[0:3]\n"
  "v_exp_f32 v153, v153\n"
  "v_mfma_f32_16x16x32_bf16 a[32:35], a[128:131], v[128:131], a[32:35]\n"
  "v_exp_f32 v154, v154\n"
  "v_mfma_f32_16x16x32_bf16 v[4:7], a[136:139], v[128:131], v[4:7]\n"
  "v_exp_f32 v155, v155\n"
  "v_mfma_f32_16x16x32_bf16 v[8:11], a[140:143], v[128:131], v[8:11]\n"
  "v_exp_f32 v156, v156\n"
  "v_mfma_f32_16x16x32_bf16 v[12:15], a[144:147], v[128:131], v[12:15]\n"
  "v_cvt_pk_bf16_f32 v176, v176, v177\n"
  "v_cvt_pk_bf16_f32 v177, v178, v179\n"
  "v_mfma_f32_16x16x32_bf16 v[16:19], a[148:151], v[128:131], v[16:19]\n"
  "v_exp_f32 v157, v157\n"
  "v_mfma_f32_16x16x32_bf16 v[20:23], a[152:155], v[128:131], v[20:23]\n"
  "v_exp_f32 v158, v158\n"
  "v_mfma_f32_16x16x32_bf16 v[24:27], a[156:159], v[128:131], v[24:27]\n"
  "buffer_load_dwordx4 v242, s[72:75], s66 offen offset:2048 lds\n"
  "ds_read_b64_tr_b16 a[164:165], v228 offset:49152\n"
  "v_mfma_f32_16x16x32_bf16 v[28:31], a[160:163], v[128:131], v[28:31]\n"
  "v_exp_f32 v159, v159\n"
  "v_mfma_f32_16x16x16_bf16 a[0:3], a[132:133], v[176:177], a[0:3]\n"
  "ds_read_b64_tr_b16 a[166:167], v229 offset:49152\n"
  "ds_read_b64_tr_b16 a[168:169], v232 offset:49152\n"
  "v_mfma_f32_16x16x16_bf16 a[4:7], a[136:137], v[176:177], a[4:7]\n"
  "ds_read_b64_tr_b16 a[170:171], v233 offset:49152\n"
  "ds_read_b64_tr_b16 a[172:173], v228 offset:49664\n"
  "v_mfma_f32_16x16x16_bf16 a[8:11], a[140:141], v[176:177], a[8:11]\n"
  "ds_read_b64_tr_b16 a[174:175], v229 offset:49664\n"
  "ds_read_b64_tr_b16 a[176:177], v232 offset:49664\n"
  "v_mfma_f32_16x16x16_bf16 a[12:15], a[144:145], v[176:177], a[12:15]\n"
  "ds_read_b64_tr_b16 a[178:179], v233 offset:49664\n"
  "ds_read_b64_tr_b16 a[180:181], v228 offset:57344\n"
  "v_mfma_f32_16x16x16_bf16 a[16:19], a[148:149], v[176:177], a[16:19]\n"
  "ds_read_b64_tr_b16 a[182:183], v229 offset:57344\n"
  "ds_read_b64_tr_b16 a[184:185], v232 offset:57344\n"
  "v_mfma_f32_16x16x16_bf16 a[20:23], a[152:153], v[176:177], a[20:23]\n"
  "buffer_load_dwordx4 v243, s[72:75], s66 offen offset:3072 lds\n"
  "ds_read_b64_tr_b16 a[186:187], v233 offset:57344\n"
  "ds_read_b64_tr_b16 a[188:189], v228 offset:57856\n"
  "v_mfma_f32_16x16x16_bf16 a[24:27], a[156:157], v[176:177], a[24:27]\n"
  "ds_read_b64_tr_b16 a[190:191], v229 offset:57856\n"
  "ds_read_b64_tr_b16 a[192:193], v232 offset:57856\n"
  "v_mfma_f32_16x16x16_bf16 a[28:31], a[160:161], v[176:177], a[28:31]\n"
  "ds_read_b64_tr_b16 a[194:195], v233 offset:57856\n"
  "v_cvt_pk_bf16_f32 v152, v152, v153\n"
  "v_mfma_f32_16x16x16_bf16 a[44:47], a[128:129], v[176:177], a[44:47]\n"
  "v_exp_f32 v136, v136\n"
  "v_mfma_f32_16x16x32_bf16 v[160:163], a[196:199], v[112:115], v[188:191]\n"
  "v_exp_f32 v137, v137\n"
  "v_mfma_f32_16x16x32_bf16 v[164:167], a[212:215], v[112:115], v[188:191]\n"
  "v_exp_f32 v138, v138\n"
  "v_mfma_f32_16x16x32_bf16 v[160:163], a[200:203], v[116:119], v[160:163]\n"
  "v_exp_f32 v139, v139\n"
  "v_mfma_f32_16x16x32_bf16 v[164:167], a[216:219], v[116:119], v[164:167]\n"
  "v_cvt_pk_bf16_f32 v153, v154, v155\n"
  "v_cvt_pk_bf16_f32 v154, v156, v157\n"
  "v_mfma_f32_16x16x32_bf16 v[160:163], a[204:207], v[120:123], v[160:163]\n"
  "v_cvt_pk_bf16_f32 v155, v158, v159\n"
  "ds_write_b128 v244, a[0:3]\n"
  "v_mfma_f32_16x16x32_bf16 v[164:167], a[220:223], v[120:123], v[164:167]\n"
  "v_exp_f32 v140, v140\n"
  "v_mfma_f32_16x16x32_bf16 v[160:163], a[208:211], v[124:127], v[160:163]\n"
  "v_exp_f32 v141, v141\n"
  "v_mfma_f32_16x16x32_bf16 v[164:167], a[224:227], v[124:127], v[164:167]\n"
  "s_waitcnt lgkmcnt(1)\n"
  "ds_write_b128 v244, a[4:7] offset:4096\n"
  "v_mfma_f32_16x16x32_bf16 v[0:3], a[164:167], v[152:155], v[0:3]\n"
  "v_exp_f32 v142, v142\n"
  "v_mfma_f32_16x16x32_bf16 a[32:35], a[128:131], v[152:155], a[32:35]\n"
  "v_exp_f32 v143, v143\n"
  "v_mfma_f32_16x16x32_bf16 v[4:7], a[168:171], v[152:155], v[4:7]\n"
  "ds_write_b128 v244, a[8:11] offset:8192\n"
  "v_cvt_pk_bf16_f32 v136, v136, v137\n"
  "v_mfma_f32_16x16x32_bf16 v[8:11], a[172:175], v[152:155], v[8:11]\n"
  "v_exp_f32 v160, v160\n"
  "v_mfma_f32_16x16x32_bf16 v[12:15], a[176:179], v[152:155], v[12:15]\n"
  "ds_write_b128 v244, a[12:15] offset:12288\n"
  "v_cvt_pk_bf16_f32 v137, v138, v139\n"
  "v_mfma_f32_16x16x32_bf16 v[16:19], a[180:183], v[152:155], v[16:19]\n"
  "ds_write_b128 v245, a[16:19]\n"
  "ds_write_b128 v245, a[20:23] offset:4096\n"
  "v_mfma_f32_16x16x32_bf16 v[20:23], a[184:187], v[152:155], v[20:23]\n"
  "ds_write_b128 v245, a[24:27] offset:8192\n"
  "ds_write_b128 v245, a[28:31] offset:12288\n"
  "v_mfma_f32_16x16x32_bf16 v[24:27], a[188:191], v[152:155], v[24:27]\n"
  "ds_write_b32 v246, a44\n"
  "v_accvgpr_read_b32 v199, a32\n"
  "v_mfma_f32_16x16x32_bf16 v[28:31], a[192:195], v[152:155], v[28:31]\n"
  "v_exp_f32 v161, v161\n"
  "v_mfma_f32_16x16x32_bf16 v[144:147], a[64:67], v[96:99], v[192:195]\n"
  "v_cmp_nge_f32 s[26:27], 1.0, v199\n"
  "v_cvt_pk_bf16_f32 v138, v140, v141\n"
  "v_mfma_f32_16x16x32_bf16 v[148:151], a[80:83], v[96:99], v[192:195]\n"
  "v_rcp_f32 v198, v199\n"
  "v_mfma_f32_16x16x32_bf16 v[144:147], a[68:71], v[100:103], v[144:147]\n"
  "v_mov_b32 v199, v198\n"
  "v_mul_f32 v0, v198, v0\n"
  "v_mfma_f32_16x16x32_bf16 v[148:151], a[84:87], v[100:103], v[148:151]\n"
  "v_mul_f32 v1, v198, v1\n"
  "v_mul_f32 v2, v198, v2\n"
  "v_mfma_f32_16x16x32_bf16 v[144:147], a[72:75], v[104:107], v[144:147]\n"
  "s_waitcnt lgkmcnt(0)\n"
  "s_barrier\n"
  "ds_read_b128 v[180:183], v248\n"
  "v_mfma_f32_16x16x32_bf16 v[148:151], a[88:91], v[104:107], v[148:151]\n"
  "v_mul_f32 v3, v198, v3\n"
  "v_mul_f32 v4, v198, v4\n"
  "v_mul_f32 v5, v198, v5\n"
  "v_mfma_f32_16x16x32_bf16 v[144:147], a[76:79], v[108:111], v[144:147]\n"
  "v_mul_f32 v6, v198, v6\n"
  "v_mul_f32 v7, v198, v7\n"
  "v_cvt_pk_bf16_f32 v128, v0, v1\n"
  "v_mfma_f32_16x16x32_bf16 v[148:151], a[92:95], v[108:111], v[148:151]\n"
  "v_cvt_pk_bf16_f32 v129, v2, v3\n"
  "v_cvt_pk_bf16_f32 v130, v4, v5\n"
  "v_mfma_f32_16x16x32_bf16 v[64:67], a[96:99], v[168:171], v[64:67]\n"
  "v_cvt_pk_bf16_f32 v131, v6, v7\n"
  "buffer_store_dwordx4 v[128:131], v250, s[52:55], 0 offen sc1\n"
  "v_mfma_f32_16x16x32_bf16 a[40:43], a[128:131], v[168:171], a[40:43]\n"
  "v_mul_f32 v8, v198, v8\n"
  "v_mul_f32 v9, v198, v9\n"
  "v_mfma_f32_16x16x32_bf16 v[68:71], a[100:103], v[168:171], v[68:71]\n"
  "v_mul_f32 v10, v198, v10\n"
  "v_mul_f32 v11, v198, v11\n"
  "v_mfma_f32_16x16x32_bf16 v[72:75], a[104:107], v[168:171], v[72:75]\n"
  "v_mul_f32 v12, v198, v12\n"
  "v_mul_f32 v13, v198, v13\n"
  "v_mfma_f32_16x16x32_bf16 v[76:79], a[108:111], v[168:171], v[76:79]\n"
  "v_mul_f32 v14, v198, v14\n"
  "v_mul_f32 v15, v198, v15\n"
  "v_mfma_f32_16x16x32_bf16 v[80:83], a[112:115], v[168:171], v[80:83]\n"
  "v_cvt_pk_bf16_f32 v139, v142, v143\n"
  "v_cvt_pk_bf16_f32 v132, v8, v9\n"
  "v_cvt_pk_bf16_f32 v133, v10, v11\n"
  "v_mfma_f32_16x16x32_bf16 v[84:87], a[116:119], v[168:171], v[84:87]\n"
  "v_cvt_pk_bf16_f32 v134, v12, v13\n"
  "v_cvt_pk_bf16_f32 v135, v14, v15\n"
  "v_mfma_f32_16x16x32_bf16 v[88:91], a[120:123], v[168:171], v[88:91]\n"
  "buffer_store_dwordx4 v[132:135], v250, s[52:55], 0 offen offset:1024 sc1\n"
  "v_mul_f32 v16, v198, v16\n"
  "v_mfma_f32_16x16x32_bf16 v[92:95], a[124:127], v[168:171], v[92:95]\n"
  "v_mul_f32 v17, v198, v17\n"
  "v_mul_f32 v18, v198, v18\n"
  "v_mfma_f32_16x16x32_bf16 v[32:35], a[132:135], v[136:139], v[32:35]\n"
  "v_mul_f32 v19, v198, v19\n"
  "v_mul_f32 v20, v198, v20\n"
  "v_mfma_f32_16x16x32_bf16 a[36:39], a[128:131], v[136:139], a[36:39]\n"
  "v_mul_f32 v21, v198, v21\n"
  "v_mul_f32 v22, v198, v22\n"
  "v_mfma_f32_16x16x32_bf16 v[36:39], a[136:139], v[136:139], v[36:39]\n"
  "v_mul_f32 v23, v198, v23\n"
  "v_cvt_pk_bf16_f32 v152, v16, v17\n"
  "v_mfma_f32_16x16x32_bf16 v[40:43], a[140:143], v[136:139], v[40:43]\n"
  "v_cvt_pk_bf16_f32 v153, v18, v19\n"
  "v_cvt_pk_bf16_f32 v154, v20, v21\n"
  "v_mfma_f32_16x16x32_bf16 v[44:47], a[144:147], v[136:139], v[44:47]\n"
  "v_cvt_pk_bf16_f32 v155, v22, v23\n"
  "buffer_store_dwordx4 v[152:155], v250, s[52:55], 0 offen offset:2048 sc1\n"
  "v_mfma_f32_16x16x32_bf16 v[48:51], a[148:151], v[136:139], v[48:51]\n"
  "v_mul_f32 v24, v198, v24\n"
  "s_waitcnt lgkmcnt(0)\n"
  "v_add_f32 v180, v180, v181\n"
  "v_mul_f32 v25, v198, v25\n"
  "v_mfma_f32_16x16x32_bf16 v[52:55], a[152:155], v[136:139], v[52:55]\n"
  "v_add_f32 v182, v182, v183\n"
  "v_mul_f32 v26, v198, v26\n"
  "v_add_f32 v180, v180, v182\n"
  "v_mfma_f32_16x16x32_bf16 v[56:59], a[156:159], v[136:139], v[56:59]\n"
  "v_mul_f32 v27, v198, v27\n"
  "v_cmp_nge_f32 s[96:97], 1.0, v180\n"
  "v_mul_f32 v28, v198, v28\n"
  "v_mfma_f32_16x16x32_bf16 v[60:63], a[160:163], v[136:139], v[60:63]\n"
  "v_mul_f32 v29, v198, v29\n"
  "v_rcp_f32 v249, v180\n"
  "v_mfma_f32_16x16x32_bf16 v[168:171], a[196:199], v[96:99], v[192:195]\n"
  "v_mul_f32 v30, v198, v30\n"
  "v_exp_f32 v162, v162\n"
  "v_mfma_f32_16x16x32_bf16 v[172:175], a[212:215], v[96:99], v[192:195]\n"
  "v_mul_f32 v31, v198, v31\n"
  "v_exp_f32 v163, v163\n"
  "v_mfma_f32_16x16x32_bf16 v[168:171], a[200:203], v[100:103], v[168:171]\n"
  "v_cvt_pk_bf16_f32 v156, v24, v25\n"
  "v_cvt_pk_bf16_f32 v157, v26, v27\n"
  "v_cvt_pk_bf16_f32 v158, v28, v29\n"
  "v_mfma_f32_16x16x32_bf16 v[172:175], a[216:219], v[100:103], v[172:175]\n"
  "v_cvt_pk_bf16_f32 v159, v30, v31\n"
  "v_exp_f32 v164, v164\n"
  "v_mfma_f32_16x16x32_bf16 v[168:171], a[204:207], v[104:107], v[168:171]\n"
  "buffer_store_dwordx4 v[156:159], v250, s[52:55], 0 offen offset:3072 sc1\n"
  "v_exp_f32 v165, v165\n"
  "v_mfma_f32_16x16x32_bf16 v[172:175], a[220:223], v[104:107], v[172:175]\n"
  "v_cvt_pk_bf16_f32 v160, v160, v161\n"
  "v_exp_f32 v166, v166\n"
  "v_mfma_f32_16x16x32_bf16 v[168:171], a[208:211], v[108:111], v[168:171]\n"
  "v_cvt_pk_bf16_f32 v161, v162, v163\n"
  "v_cvt_pk_bf16_f32 v162, v164, v165\n"
  "v_exp_f32 v167, v167\n"
  "s_nop 0\n"
  "v_cvt_pk_bf16_f32 v163, v166, v167\n"
  "v_mfma_f32_16x16x32_bf16 v[172:175], a[224:227], v[108:111], v[172:175]\n"
  "v_exp_f32 v144, v144\n"
  "v_mfma_f32_16x16x32_bf16 v[32:35], a[164:167], v[160:163], v[32:35]\n"
  "v_exp_f32 v145, v145\n"
  "v_mfma_f32_16x16x32_bf16 a[36:39], a[128:131], v[160:163], a[36:39]\n"
  "v_exp_f32 v146, v146\n"
  "v_mfma_f32_16x16x32_bf16 v[36:39], a[168:171], v[160:163], v[36:39]\n"
  "v_exp_f32 v147, v147\n"
  "v_mfma_f32_16x16x32_bf16 v[40:43], a[172:175], v[160:163], v[40:43]\n"
  "v_cvt_pk_bf16_f32 v144, v144, v145\n"
  "v_exp_f32 v148, v148\n"
  "v_mfma_f32_16x16x32_bf16 v[44:47], a[176:179], v[160:163], v[44:47]\n"
  "v_cvt_pk_bf16_f32 v145, v146, v147\n"
  "v_exp_f32 v149, v149\n"
  "v_mfma_f32_16x16x32_bf16 v[48:51], a[180:183], v[160:163], v[48:51]\n"
  "v_accvgpr_read_b32 v199, a36\n"
  "v_exp_f32 v150, v150\n"
  "v_mfma_f32_16x16x32_bf16 v[52:55], a[184:187], v[160:163], v[52:55]\n"
  "v_cmp_nge_f32 vcc, 1.0, v199\n"
  "s_nop 1\n"
  "s_or_b64 s[26:27], s[26:27], vcc\n"
  "v_mfma_f32_16x16x32_bf16 v[56:59], a[188:191], v[160:163], v[56:59]\n"
  "v_cvt_pk_bf16_f32 v146, v148, v149\n"
  "v_exp_f32 v151, v151\n"
  "s_nop 0\n"
  "v_cvt_pk_bf16_f32 v147, v150, v151\n"
  "v_mfma_f32_16x16x32_bf16 v[60:63], a[192:195], v[160:163], v[60:63]\n"
  "v_rcp_f32 v198, v199\n"
  "v_mfma_f32_16x16x32_bf16 v[64:67], a[132:135], v[144:147], v[64:67]\n"
  "v_exp_f32 v168, v168\n"
  "v_mfma_f32_16x16x32_bf16 a[40:43], a[128:131], v[144:147], a[40:43]\n"
  "v_exp_f32 v169, v169\n"
  "v_mfma_f32_16x16x32_bf16 v[68:71], a[136:139], v[144:147], v[68:71]\n"
  "v_mov_b32 v199, v198\n"
  "v_mul_f32 v32, v198, v32\n"
  "v_mul_f32 v33, v198, v33\n"
  "v_mfma_f32_16x16x32_bf16 v[72:75], a[140:143], v[144:147], v[72:75]\n"
  "v_mul_f32 v34, v198, v34\n"
  "v_mul_f32 v35, v198, v35\n"
  "v_mul_f32 v36, v198, v36\n"
  "v_mfma_f32_16x16x32_bf16 v[76:79], a[144:147], v[144:147], v[76:79]\n"
  "v_mul_f32 v37, v198, v37\n"
  "v_mul_f32 v38, v198, v38\n"
  "v_mul_f32 v39, v198, v39\n"
  "v_mfma_f32_16x16x32_bf16 v[80:83], a[148:151], v[144:147], v[80:83]\n"
  "v_cvt_pk_bf16_f32 v136, v32, v33\n"
  "v_cvt_pk_bf16_f32 v137, v34, v35\n"
  "v_cvt_pk_bf16_f32 v138, v36, v37\n"
  "v_mfma_f32_16x16x32_bf16 v[84:87], a[152:155], v[144:147], v[84:87]\n"
  "v_cvt_pk_bf16_f32 v139, v38, v39\n"
  "buffer_store_dwordx4 v[136:139], v250, s[52:55], s68 offen sc1\n"
  "v_mul_f32 v40, v198, v40\n"
  "v_mfma_f32_16x16x32_bf16 v[88:91], a[156:159], v[144:147], v[88:91]\n"
  "v_exp_f32 v170, v170\n"
  "v_exp_f32 v171, v171\n"
  "v_exp_f32 v172, v172\n"
  "v_exp_f32 v173, v173\n"
  "v_cvt_pk_bf16_f32 v168, v168, v169\n"
  "v_exp_f32 v174, v174\n"
  "v_cvt_pk_bf16_f32 v169, v170, v171\n"
  "v_cvt_pk_bf16_f32 v170, v172, v173\n"
  "v_exp_f32 v175, v175\n"
  "s_nop 0\n"
  "v_cvt_pk_bf16_f32 v171, v174, v175\n"
  "v_mfma_f32_16x16x32_bf16 v[92:95], a[160:163], v[144:147], v[92:95]\n"
  "v_mul_f32 v41, v198, v41\n"
  "v_mul_f32 v42, v198, v42\n"
  "v_mul_f32 v43, v198, v43\n"
  "v_mfma_f32_16x16x32_bf16 v[64:67], a[164:167], v[168:171], v[64:67]\n"
  "v_mul_f32 v44, v198, v44\n"
  "v_mul_f32 v45, v198, v45\n"
  "v_mul_f32 v46, v198, v46\n"
  "v_mfma_f32_16x16x32_bf16 a[40:43], a[128:131], v[168:171], a[40:43]\n"
  "v_mul_f32 v47, v198, v47\n"
  "v_cvt_pk_bf16_f32 v140, v40, v41\n"
  "v_cvt_pk_bf16_f32 v141, v42, v43\n"
  "v_mfma_f32_16x16x32_bf16 v[68:71], a[168:171], v[168:171], v[68:71]\n"
  "v_cvt_pk_bf16_f32 v142, v44, v45\n"
  "v_cvt_pk_bf16_f32 v143, v46, v47\n"
  "buffer_store_dwordx4 v[140:143], v250, s[52:55], s68 offen offset:1024 sc1\n"
  "v_mfma_f32_16x16x32_bf16 v[72:75], a[172:175], v[168:171], v[72:75]\n"
  "v_mul_f32 v48, v198, v48\n"
  "v_mul_f32 v49, v198, v49\n"
  "v_mul_f32 v50, v198, v50\n"
  "v_mfma_f32_16x16x32_bf16 v[76:79], a[176:179], v[168:171], v[76:79]\n"
  "v_mul_f32 v51, v198, v51\n"
  "v_mul_f32 v52, v198, v52\n"
  "v_mul_f32 v53, v198, v53\n"
  "v_mfma_f32_16x16x32_bf16 v[80:83], a[180:183], v[168:171], v[80:83]\n"
  "v_mul_f32 v54, v198, v54\n"
  "v_mul_f32 v55, v198, v55\n"
  "v_cvt_pk_bf16_f32 v160, v48, v49\n"
  "v_mfma_f32_16x16x32_bf16 v[84:87], a[184:187], v[168:171], v[84:87]\n"
  "v_cvt_pk_bf16_f32 v161, v50, v51\n"
  "v_cvt_pk_bf16_f32 v162, v52, v53\n"
  "v_cvt_pk_bf16_f32 v163, v54, v55\n"
  "v_mfma_f32_16x16x32_bf16 v[88:91], a[188:191], v[168:171], v[88:91]\n"
  "buffer_store_dwordx4 v[160:163], v250, s[52:55], s68 offen offset:2048 sc1\n"
  "v_mul_f32 v56, v198, v56\n"
  "v_mul_f32 v57, v198, v57\n"
  "v_mfma_f32_16x16x32_bf16 v[92:95], a[192:195], v[168:171], v[92:95]\n"
  "v_mul_f32 v58, v198, v58\n"
  "v_mul_f32 v59, v198, v59\n"
  "v_mul_f32 v60, v198, v60\n"
  "v_mul_f32 v61, v198, v61\n"
  "v_mul_f32 v62, v198, v62\n"
  "v_mul_f32 v63, v198, v63\n"
  "v_cvt_pk_bf16_f32 v164, v56, v57\n"
  "v_cvt_pk_bf16_f32 v165, v58, v59\n"
  "v_cvt_pk_bf16_f32 v166, v60, v61\n"
  "v_cvt_pk_bf16_f32 v167, v62, v63\n"
  "buffer_store_dwordx4 v[164:167], v250, s[52:55], s68 offen offset:3072 sc1\n"
  "v_accvgpr_read_b32 v199, a40\n"
  "v_cmp_nge_f32 vcc, 1.0, v199\n"
  "s_nop 1\n"
  "s_or_b64 s[26:27], s[26:27], vcc\n"
  "v_rcp_f32 v198, v199\n"
  "s_nop 0\n"
  "v_mov_b32 v199, v198\n"
  "v_pk_mul_f32 v[64:65], v[64:65], v[198:199]\n"
  "v_pk_mul_f32 v[66:67], v[66:67], v[198:199]\n"
  "v_pk_mul_f32 v[68:69], v[68:69], v[198:199]\n"
  "v_pk_mul_f32 v[70:71], v[70:71], v[198:199]\n"
  "v_cvt_pk_bf16_f32 v144, v64, v65\n"
  "v_cvt_pk_bf16_f32 v145, v66, v67\n"
  "v_cvt_pk_bf16_f32 v146, v68, v69\n"
  "v_cvt_pk_bf16_f32 v147, v70, v71\n"
  "buffer_store_dwordx4 v[144:147], v250, s[52:55], s69 offen sc1\n"
  "v_pk_mul_f32 v[72:73], v[72:73], v[198:199]\n"
  "v_pk_mul_f32 v[74:75], v[74:75], v[198:199]\n"
  "v_pk_mul_f32 v[76:77], v[76:77], v[198:199]\n"
  "v_pk_mul_f32 v[78:79], v[78:79], v[198:199]\n"
  "v_cvt_pk_bf16_f32 v148, v72, v73\n"
  "v_cvt_pk_bf16_f32 v149, v74, v75\n"
  "v_cvt_pk_bf16_f32 v150, v76, v77\n"
  "v_cvt_pk_bf16_f32 v151, v78, v79\n"
  "buffer_store_dwordx4 v[148:151], v250, s[52:55], s69 offen offset:1024 sc1\n"
  "v_pk_mul_f32 v[80:81], v[80:81], v[198:199]\n"
  "v_pk_mul_f32 v[82:83], v[82:83], v[198:199]\n"
  "v_pk_mul_f32 v[84:85], v[84:85], v[198:199]\n"
  "v_pk_mul_f32 v[86:87], v[86:87], v[198:199]\n"
  "v_cvt_pk_bf16_f32 v168, v80, v81\n"
  "v_cvt_pk_bf16_f32 v169, v82, v83\n"
  "v_cvt_pk_bf16_f32 v170, v84, v85\n"
  "v_cvt_pk_bf16_f32 v171, v86, v87\n"
  "buffer_store_dwordx4 v[168:171], v250, s[52:55], s69 offen offset:2048 sc1\n"
  "v_pk_mul_f32 v[88:89], v[88:89], v[198:199]\n"
  "v_pk_mul_f32 v[90:91], v[90:91], v[198:199]\n"
  "v_pk_mul_f32 v[92:93], v[92:93], v[198:199]\n"
  "v_pk_mul_f32 v[94:95], v[94:95], v[198:199]\n"
  "v_cvt_pk_bf16_f32 v172, v88, v89\n"
  "v_cvt_pk_bf16_f32 v173, v90, v91\n"
  "v_cvt_pk_bf16_f32 v174, v92, v93\n"
  "v_cvt_pk_bf16_f32 v175, v94, v95\n"
  "buffer_store_dwordx4 v[172:175], v250, s[52:55], s69 offen offset:3072 sc1\n"
  "s_or_b64 s[26:27], s[26:27], s[96:97]\n"
  "s_cbranch_scc1 L_slow\n"
  "L_c:\n"
  "ds_read_b128 v[96:99], v247\n"
  "ds_read_b128 v[100:103], v247 offset:1024\n"
  "ds_read_b128 v[104:107], v247 offset:2048\n"
  "ds_read_b128 v[108:111], v247 offset:3072\n"
  "ds_read_b128 v[112:115], v247 offset:4096\n"
  "ds_read_b128 v[116:119], v247 offset:5120\n"
  "ds_read_b128 v[120:123], v247 offset:6144\n"
  "ds_read_b128 v[124:127], v247 offset:7168\n"
  "ds_read_b64_tr_b16 a[208:209], v223\n"
  "ds_read_b64_tr_b16 a[210:211], v223 offset:256\n"
  "ds_read_b64_tr_b16 a[212:213], v226\n"
  "ds_read_b64_tr_b16 a[214:215], v226 offset:256\n"
  "ds_read_b64_tr_b16 a[216:217], v223 offset:512\n"
  "ds_read_b64_tr_b16 a[218:219], v223 offset:768\n"
  "s_waitcnt lgkmcnt(4)\n"
  "v_mfma_f32_16x16x32_bf16 v[0:3], a[208:211], v[128:131], 0\n"
  "v_add_f32 v96, v96, v100\n"
  "v_add_f32 v97, v97, v101\n"
  "v_add_f32 v98, v98, v102\n"
  "v_mfma_f32_16x16x32_bf16 v[32:35], a[208:211], v[136:139], 0\n"
  "v_add_f32 v99, v99, v103\n"
  "v_add_f32 v96, v96, v104\n"
  "v_add_f32 v97, v97, v105\n"
  "v_mfma_f32_16x16x32_bf16 v[64:67], a[208:211], v[144:147], 0\n"
  "ds_read_b64_tr_b16 a[220:221], v226 offset:512\n"
  "ds_read_b64_tr_b16 a[222:223], v226 offset:768\n"
  "v_add_f32 v98, v98, v106\n"
  "s_waitcnt lgkmcnt(2)\n"
  "v_mfma_f32_16x16x32_bf16 v[4:7], a[212:215], v[128:131], 0\n"
  "v_add_f32 v99, v99, v107\n"
  "v_add_f32 v96, v96, v108\n"
  "L_st6:\n"
  "s_cbranch_execz L_stx\n"
  "v_add_f32 v97, v97, v109\n"
  "v_mfma_f32_16x16x32_bf16 v[36:39], a[212:215], v[136:139], 0\n"
  "v_add_f32 v98, v98, v110\n"
  "v_add_f32 v99, v99, v111\n"
  "v_mul_f32 v96, v249, v96\n"
  "v_mfma_f32_16x16x32_bf16 v[68:71], a[212:215], v[144:147], 0\n"
  "ds_read_b64_tr_b16 a[224:225], v223 offset:8192\n"
  "ds_read_b64_tr_b16 a[226:227], v223 offset:8448\n"
  "v_mul_f32 v97, v249, v97\n"
  "v_mfma_f32_16x16x32_bf16 v[8:11], a[216:219], v[128:131], 0\n"
  "v_mul_f32 v98, v249, v98\n"
  "v_mul_f32 v99, v249, v99\n"
  "v_cvt_pk_bf16_f32 v176, v96, v97\n"
  "v_mfma_f32_16x16x32_bf16 v[40:43], a[216:219], v[136:139], 0\n"
  "v_cvt_pk_bf16_f32 v177, v98, v99\n"
  "v_add_f32 v112, v112, v116\n"
  "v_add_f32 v113, v113, v117\n"
  "v_mfma_f32_16x16x32_bf16 v[72:75], a[216:219], v[144:147], 0\n"
  "ds_read_b64_tr_b16 a[228:229], v226 offset:8192\n"
  "ds_read_b64_tr_b16 a[230:231], v226 offset:8448\n"
  "v_add_f32 v114, v114, v118\n"
  "s_waitcnt lgkmcnt(2)\n"
  "v_mfma_f32_16x16x32_bf16 v[12:15], a[220:223], v[128:131], 0\n"
  "v_add_f32 v115, v115, v119\n"
  "v_add_f32 v112, v112, v120\n"
  "v_add_f32 v113, v113, v121\n"
  "v_mfma_f32_16x16x32_bf16 v[44:47], a[220:223], v[136:139], 0\n"
  "v_add_f32 v114, v114, v122\n"
  "v_add_f32 v115, v115, v123\n"
  "v_add_f32 v112, v112, v124\n"
  "v_mfma_f32_16x16x32_bf16 v[76:79], a[220:223], v[144:147], 0\n"
  "ds_read_b64_tr_b16 a[232:233], v223 offset:8704\n"
  "ds_read_b64_tr_b16 a[234:235], v223 offset:8960\n"
  "v_add_f32 v113, v113, v125\n"
  "v_mfma_f32_16x16x32_bf16 v[16:19], a[224:227], v[128:131], 0\n"
  "v_add_f32 v114, v114, v126\n"
  "v_add_f32 v115, v115, v127\n"
  "v_mul_f32 v112, v249, v112\n"
  "v_mfma_f32_16x16x32_bf16 v[48:51], a[224:227], v[136:139], 0\n"
  "v_mul_f32 v113, v249, v113\n"
  "v_mul_f32 v114, v249, v114\n"
  "v_mul_f32 v115, v249, v115\n"
  "v_mfma_f32_16x16x32_bf16 v[80:83], a[224:227], v[144:147], 0\n"
  "ds_read_b64_tr_b16 a[236:237], v226 offset:8704\n"
  "ds_read_b64_tr_b16 a[238:239], v226 offset:8960\n"
  "v_cvt_pk_bf16_f32 v178, v112, v113\n"
  "s_waitcnt lgkmcnt(2)\n"
  "v_mfma_f32_16x16x32_bf16 v[20:23], a[228:231], v[128:131], 0\n"
  "v_cvt_pk_bf16_f32 v179, v114, v115\n"
  "buffer_store_dwordx4 v[176:179], v251, s[52:55], s24 offen sc1\n"
  "ds_read_b64_tr_b16 a[240:241], v223 offset:4096\n"
  "v_mfma_f32_16x16x32_bf16 v[52:55], a[228:231], v[136:139], 0\n"
  "ds_read_b64_tr_b16 a[242:243], v223 offset:4352\n"
  "ds_read_b64_tr_b16 a[244:245], v226 offset:4096\n"
  "v_mfma_f32_16x16x32_bf16 v[84:87], a[228:231], v[144:147], 0\n"
  "ds_read_b64_tr_b16 a[246:247], v226 offset:4352\n"
  "ds_read_b64_tr_b16 a[248:249], v223 offset:4608\n"
  "v_mfma_f32_16x16x32_bf16 v[24:27], a[232:235], v[128:131], 0\n"
  "ds_read_b64_tr_b16 a[250:251], v223 offset:4864\n"
  "ds_read_b64_tr_b16 a[252:253], v226 offset:4608\n"
  "v_mfma_f32_16x16x32_bf16 v[56:59], a[232:235], v[136:139], 0\n"
  "ds_read_b64_tr_b16 a[254:255], v226 offset:4864\n"
  "ds_read_b64_tr_b16 a[168:169], v223 offset:12288\n"
  "v_mfma_f32_16x16x32_bf16 v[88:91], a[232:235], v[144:147], 0\n"
  "ds_read_b64_tr_b16 a[170:171], v223 offset:12544\n"
  "ds_read_b64_tr_b16 a[172:173], v226 offset:12288\n"
  "s_waitcnt lgkmcnt(9)\n"
  "v_mfma_f32_16x16x32_bf16 v[28:31], a[236:239], v[128:131], 0\n"
  "ds_read_b64_tr_b16 a[174:175], v226 offset:12544\n"
  "v_mfma_f32_16x16x32_bf16 v[60:63], a[236:239], v[136:139], 0\n"
  "ds_read_b64_tr_b16 a[176:177], v223 offset:12800\n"
  "ds_read_b64_tr_b16 a[178:179], v223 offset:13056\n"
  "v_mfma_f32_16x16x32_bf16 v[92:95], a[236:239], v[144:147], 0\n"
  "v_lshlrev_b32 v185, 2, v254\n"
  "v_cmp_lt_u32 vcc, 31, v254\n"
  "v_mov_b32 v189, 0x40000000\n"
  "s_nop 0\n"
  "v_cndmask_b32 v185, v185, v189, vcc\n"
  "v_mfma_f32_16x16x32_bf16 v[0:3], a[240:243], v[132:135], v[0:3]\n"
  "v_lshlrev_b32 v188, 7, v254\n"
  "s_lshl_b32 s63, s57, 2\n"
  "v_mfma_f32_16x16x32_bf16 v[32:35], a[240:243], v[140:143], v[32:35]\n"
  "ds_read_b64_tr_b16 a[180:181], v226 offset:12800\n"
  "ds_read_b64_tr_b16 a[182:183], v226 offset:13056\n"
  "v_mfma_f32_16x16x32_bf16 v[64:67], a[240:243], v[148:151], v[64:67]\n"
  "v_add_u32 v188, s63, v188\n"
  "s_waitcnt lgkmcnt(2)\n"
  "v_mfma_f32_16x16x32_bf16 v[4:7], a[244:247], v[132:135], v[4:7]\n"
  "s_mov_b32 s93, 0\n"
  "s_mov_b32 s92, s70\n"
  "v_cndmask_b32 v186, v189, v188, s[92:93]\n"
  "v_mfma_f32_16x16x32_bf16 v[36:39], a[244:247], v[140:143], v[36:39]\n"
  "ds_read_b64_tr_b16 a[184:185], v223 offset:16384\n"
  "ds_read_b64_tr_b16 a[186:187], v223 offset:16640\n"
  "v_mfma_f32_16x16x32_bf16 v[68:71], a[244:247], v[148:151], v[68:71]\n"
  "s_mov_b32 s93, 0\n"
  "s_mov_b32 s92, s71\n"
  "v_cndmask_b32 v187, v189, v188, s[92:93]\n"
  "v_mfma_f32_16x16x32_bf16 v[8:11], a[248:251], v[132:135], v[8:11]\n"
  "s_mul_i32 s63, s19, 0x600\n"
  "v_add_u32 v190, s63, v251\n"
  "v_mfma_f32_16x16x32_bf16 v[40:43], a[248:251], v[140:143], v[40:43]\n"
  "ds_read_b64_tr_b16 a[188:189], v226 offset:16384\n"
  "ds_read_b64_tr_b16 a[190:191], v226 offset:16640\n"
  "v_mfma_f32_16x16x32_bf16 v[72:75], a[248:251], v[148:151], v[72:75]\n"
  "v_lshlrev_b32 v192, 3, v254\n"
  "v_add_u32 v191, s63, v192\n"
  "v_mfma_f32_16x16x32_bf16 v[12:15], a[252:255], v[132:135], v[12:15]\n"
  "v_add_u32 v192, 0x1800, v192\n"
  "v_and_b32 v193, 15, v254\n"
  "v_mfma_f32_16x16x32_bf16 v[44:47], a[252:255], v[140:143], v[44:47]\n"
  "ds_read_b64_tr_b16 a[208:209], v223 offset:16896\n"
  "ds_read_b64_tr_b16 a[210:211], v223 offset:17152\n"
  "v_mfma_f32_16x16x32_bf16 v[76:79], a[252:255], v[148:151], v[76:79]\n"
  "v_lshlrev_b32 v193, 10, v193\n"
  "v_lshrrev_b32 v194, 4, v254\n"
  "v_mfma_f32_16x16x32_bf16 v[16:19], a[168:171], v[132:135], v[16:19]\n"
  "v_lshl_add_u32 v193, v194, 3, v193\n"
  "s_mul_i32 s63, s19, 0xc000\n"
  "v_mfma_f32_16x16x32_bf16 v[48:51], a[168:171], v[140:143], v[48:51]\n"
  "ds_read_b64_tr_b16 a[212:213], v226 offset:16896\n"
  "ds_read_b64_tr_b16 a[214:215], v226 offset:17152\n"
  "v_mfma_f32_16x16x32_bf16 v[80:83], a[168:171], v[148:151], v[80:83]\n"
  "v_add_u32 v193, s63, v193\n"
  "v_add_u32 v194, 0x8000, v244\n"
  "v_mfma_f32_16x16x32_bf16 v[20:23], a[172:175], v[132:135], v[20:23]\n"
  "v_add_u32 v195, 0x1000, v250\n"
  "v_add_u32 v196, 0x2000, v250\n"
  "v_mfma_f32_16x16x32_bf16 v[52:55], a[172:175], v[140:143], v[52:55]\n"
  "ds_read_b64_tr_b16 a[216:217], v223 offset:24576\n"
  "ds_read_b64_tr_b16 a[218:219], v223 offset:24832\n"
  "v_mfma_f32_16x16x32_bf16 v[84:87], a[172:175], v[148:151], v[84:87]\n"
  "s_lshr_b32 s58, s19, 1\n"
  "s_lshl_b32 s58, s58, 13\n"
  "v_mfma_f32_16x16x32_bf16 v[24:27], a[176:179], v[132:135], v[24:27]\n"
  "s_and_b32 s59, s19, 1\n"
  "s_lshl_b32 s59, s59, 9\n"
  "v_mfma_f32_16x16x32_bf16 v[56:59], a[176:179], v[140:143], v[56:59]\n"
  "ds_read_b64_tr_b16 a[220:221], v226 offset:24576\n"
  "ds_read_b64_tr_b16 a[222:223], v226 offset:24832\n"
  "v_mfma_f32_16x16x32_bf16 v[88:91], a[176:179], v[148:151], v[88:91]\n"
  "s_or_b32 s58, s58, s59\n"
  "v_add_u32 v228, s58, v222\n"
  "s_waitcnt lgkmcnt(3)\n"
  "v_mfma_f32_16x16x32_bf16 v[28:31], a[180:183], v[132:135], v[28:31]\n"
  "v_add_u32 v229, s58, v223\n"
  "v_add_u32 v230, s58, v224\n"
  "v_mfma_f32_16x16x32_bf16 v[60:63], a[180:183], v[140:143], v[60:63]\n"
  "v_add_u32 v231, s58, v225\n"
  "ds_read_b64_tr_b16 a[224:225], v223 offset:25088\n"
  "v_mfma_f32_16x16x32_bf16 v[92:95], a[180:183], v[148:151], v[92:95]\n"
  "v_add_u32 v232, s58, v226\n"
  "ds_read_b64_tr_b16 a[226:227], v223 offset:25344\n"
  "v_mfma_f32_16x16x32_bf16 v[0:3], a[184:187], v[152:155], v[0:3]\n"
  "v_add_u32 v233, s58, v227\n"
  "v_mfma_f32_16x16x32_bf16 v[32:35], a[184:187], v[160:163], v[32:35]\n"
  "ds_read_b64_tr_b16 a[228:229], v226 offset:25088\n"
  "ds_read_b64_tr_b16 a[230:231], v226 offset:25344\n"
  "v_mfma_f32_16x16x32_bf16 v[64:67], a[184:187], v[168:171], v[64:67]\n"
  "v_mfma_f32_16x16x32_bf16 v[4:7], a[188:191], v[152:155], v[4:7]\n"
  "v_mfma_f32_16x16x32_bf16 v[36:39], a[188:191], v[160:163], v[36:39]\n"
  "ds_read_b64_tr_b16 a[232:233], v223 offset:20480\n"
  "ds_read_b64_tr_b16 a[234:235], v223 offset:20736\n"
  "v_mfma_f32_16x16x32_bf16 v[68:71], a[188:191], v[168:171], v[68:71]\n"
  "v_mfma_f32_16x16x32_bf16 v[8:11], a[208:211], v[152:155], v[8:11]\n"
  "v_mfma_f32_16x16x32_bf16 v[40:43], a[208:211], v[160:163], v[40:43]\n"
  "ds_read_b64_tr_b16 a[236:237], v226 offset:20480\n"
  "ds_read_b64_tr_b16 a[238:239], v226 offset:20736\n"
  "v_mfma_f32_16x16x32_bf16 v[72:75], a[208:211], v[168:171], v[72:75]\n"
  "v_mfma_f32_16x16x32_bf16 v[12:15], a[212:215], v[152:155], v[12:15]\n"
  "v_mfma_f32_16x16x32_bf16 v[44:47], a[212:215], v[160:163], v[44:47]\n"
  "ds_read_b64_tr_b16 a[240:241], v223 offset:20992\n"
  "ds_read_b64_tr_b16 a[242:243], v223 offset:21248\n"
  "v_mfma_f32_16x16x32_bf16 v[76:79], a[212:215], v[168:171], v[76:79]\n"
  "s_waitcnt lgkmcnt(6)\n"
  "v_mfma_f32_16x16x32_bf16 v[16:19], a[216:219], v[152:155], v[16:19]\n"
  "s_waitcnt vmcnt(0)\n"
  "s_barrier\n"
  "s_cmp_eq_u32 s19, 3\n"
  "s_cbranch_scc0 L_o15\n"
  "buffer_store_dword v252, v186, s[84:87], s99 offen sc1\n"
  "L_o15:\n"
  "v_mfma_f32_16x16x32_bf16 v[48:51], a[216:219], v[160:163], v[48:51]\n"
  "s_add_u32 m0, s65, 0x1c000\n"
  "s_add_u32 s67, s88, 1\n"
  "v_mfma_f32_16x16x32_bf16 v[80:83], a[216:219], v[168:171], v[80:83]\n"
  "s_and_b32 s67, s67, 3\n"
  "s_lshl_b32 s67, s67, 17\n"
  "v_mfma_f32_16x16x32_bf16 v[20:23], a[220:223], v[152:155], v[20:23]\n"
  "s_or_b32 s67, s67, 0x10000\n"
  "buffer_load_dwordx4 v240, s[72:75], s67 offen lds\n"
  "v_mfma_f32_16x16x32_bf16 v[52:55], a[220:223], v[160:163], v[52:55]\n"
  "buffer_load_dwordx4 v241, s[72:75], s67 offen offset:1024 lds\n"
  "ds_read_b64_tr_b16 a[244:245], v226 offset:20992\n"
  "v_mfma_f32_16x16x32_bf16 v[84:87], a[220:223], v[168:171], v[84:87]\n"
  "buffer_load_dwordx4 v242, s[72:75], s67 offen offset:2048 lds\n"
  "ds_read_b64_tr_b16 a[246:247], v226 offset:21248\n"
  "v_mfma_f32_16x16x32_bf16 v[24:27], a[224:227], v[152:155], v[24:27]\n"
  "buffer_load_dwordx4 v243, s[72:75], s67 offen offset:3072 lds\n"
  "s_add_u32 m0, s65, 0xc000\n"
  "v_mfma_f32_16x16x32_bf16 v[56:59], a[224:227], v[160:163], v[56:59]\n"
  "s_add_u32 s66, s88, 2\n"
  "s_and_b32 s66, s66, 3\n"
  "v_mfma_f32_16x16x32_bf16 v[88:91], a[224:227], v[168:171], v[88:91]\n"
  "s_lshl_b32 s66, s66, 17\n"
  "s_or_b32 s66, s66, 0x10000\n"
  "v_mfma_f32_16x16x32_bf16 v[28:31], a[228:231], v[152:155], v[28:31]\n"
  "ds_read_b64_tr_b16 a[248:249], v223 offset:28672\n"
  "buffer_load_dwordx4 v240, s[72:75], s66 offen lds\n"
  "v_mfma_f32_16x16x32_bf16 v[60:63], a[228:231], v[160:163], v[60:63]\n"
  "ds_read_b64_tr_b16 a[250:251], v223 offset:28928\n"
  "buffer_load_dwordx4 v241, s[72:75], s66 offen offset:1024 lds\n"
  "v_mfma_f32_16x16x32_bf16 v[92:95], a[228:231], v[168:171], v[92:95]\n"
  "buffer_load_dwordx4 v242, s[72:75], s66 offen offset:2048 lds\n"
  "ds_read_b64_tr_b16 a[252:253], v226 offset:28672\n"
  "s_waitcnt lgkmcnt(3)\n"
  "v_mfma_f32_16x16x32_bf16 v[0:3], a[232:235], v[156:159], v[0:3]\n"
  "buffer_load_dwordx4 v243, s[72:75], s66 offen offset:3072 lds\n"
  "s_add_u32 m0, s65, 0x8000\n"
  "v_mfma_f32_16x16x32_bf16 v[32:35], a[232:235], v[164:167], v[32:35]\n"
  "s_add_u32 s67, s88, 3\n"
  "s_and_b32 s67, s67, 3\n"
  "v_mfma_f32_16x16x32_bf16 v[64:67], a[232:235], v[172:175], v[64:67]\n"
  "ds_read_b64_tr_b16 a[254:255], v226 offset:28928\n"
  "s_lshl_b32 s67, s67, 17\n"
  "v_mfma_f32_16x16x32_bf16 v[4:7], a[236:239], v[156:159], v[4:7]\n"
  "s_or_b32 s67, s67, 0x10000\n"
  "buffer_load_dwordx4 v240, s[72:75], s67 offen lds\n"
  "v_mfma_f32_16x16x32_bf16 v[36:39], a[236:239], v[164:167], v[36:39]\n"
  "buffer_load_dwordx4 v241, s[72:75], s67 offen offset:1024 lds\n"
  "ds_read_b64_tr_b16 a[168:169], v223 offset:29184\n"
  "v_mfma_f32_16x16x32_bf16 v[68:71], a[236:239], v[172:175], v[68:71]\n"
  "buffer_load_dwordx4 v242, s[72:75], s67 offen offset:2048 lds\n"
  "ds_read_b64_tr_b16 a[170:171], v223 offset:29440\n"
  "v_mfma_f32_16x16x32_bf16 v[8:11], a[240:243], v[156:159], v[8:11]\n"
  "buffer_load_dwordx4 v243, s[72:75], s67 offen offset:3072 lds\n"
  "ds_read_b64_tr_b16 a[172:173], v226 offset:29184\n"
  "v_mfma_f32_16x16x32_bf16 v[40:43], a[240:243], v[164:167], v[40:43]\n"
  "ds_read_b64_tr_b16 a[174:175], v226 offset:29440\n"
  "ds_read_b64_tr_b16 a[176:177], v224 offset:8192\n"
  "v_mfma_f32_16x16x32_bf16 v[72:75], a[240:243], v[172:175], v[72:75]\n"
  "ds_read_b64_tr_b16 a[178:179], v224 offset:8448\n"
  "ds_read_b64_tr_b16 a[180:181], v227 offset:8192\n"
  "v_mfma_f32_16x16x32_bf16 v[12:15], a[244:247], v[156:159], v[12:15]\n"
  "ds_read_b64_tr_b16 a[182:183], v227 offset:8448\n"
  "v_mfma_f32_16x16x32_bf16 v[44:47], a[244:247], v[164:167], v[44:47]\n"
  "ds_read_b64_tr_b16 a[184:185], v224 offset:8704\n"
  "ds_read_b64_tr_b16 a[186:187], v224 offset:8960\n"
  "v_mfma_f32_16x16x32_bf16 v[76:79], a[244:247], v[172:175], v[76:79]\n"
  "s_waitcnt lgkmcnt(8)\n"
  "v_mfma_f32_16x16x32_bf16 v[16:19], a[248:251], v[156:159], v[16:19]\n"
  "s_cmp_eq_u32 s19, 3\n"
  "s_cbranch_scc0 L_o16\n"
  "s_lshl_b32 s90, s57, 7\n"
  "s_add_u32 s90, s90, s99\n"
  "s_mov_b32 s91, 0\n"
  "L_poll00:\n"
  "buffer_load_dword v184, v185, s[84:87], s90 offen sc1\n"
  "s_waitcnt vmcnt(0)\n"
  "v_subrev_u32 v184, s89, v184\n"
  "v_cmp_lt_u32 s[92:93], 1, v184\n"
  "s_add_u32 s91, s91, 1\n"
  "s_nop 3\n"
  "s_and_b32 s92, s92, s70\n"
  "s_cbranch_scc0 L_polled00\n"
  "s_and_b32 s92, s91, 7\n"
  "s_cbranch_scc1 L_poll00\n"
  "buffer_load_dword v188, v185, s[84:87], s98 offen sc1\n"
  "s_waitcnt vmcnt(0)\n"
  "v_cmp_ne_u32 s[92:93], v252, v188\n"
  "s_nop 4\n"
  "s_and_b32 s92, s92, s70\n"
  "s_cbranch_scc0 L_same00\n"
  "v_mov_b32 v252, v188\n"
  "buffer_store_dword v252, v186, s[84:87], s99 offen sc1\n"
  "L_same00:\n"
  "s_sleep 2\n"
  "s_cmp_lt_u32 s91, 0x9c40\n"
  "s_cbranch_scc1 L_poll00\n"
  "L_polled00:\n"
  "L_o16:\n"
  "s_waitcnt vmcnt(0)\n"
  "s_barrier\n"
  "buffer_load_dwordx4 a[0:3], v250, s[76:79], s21 offen sc1\n"
  "buffer_load_dwordx4 a[4:7], v195, s[76:79], s21 offen sc1\n"
  "buffer_load_dwordx4 a[8:11], v196, s[76:79], s21 offen sc1\n"
  "buffer_load_dwordx4 a[12:15], v250, s[76:79], s21 offen offset:1024 sc1\n"
  "buffer_load_dwordx4 a[16:19], v195, s[76:79], s21 offen offset:1024 sc1\n"
  "buffer_load_dwordx4 a[20:23], v196, s[76:79], s21 offen offset:1024 sc1\n"
  "v_mfma_f32_16x16x32_bf16 v[48:51], a[248:251], v[164:167], v[48:51]\n"
  "ds_read_b64_tr_b16 a[188:189], v227 offset:8704\n"
  "ds_read_b64_tr_b16 a[190:191], v227 offset:8960\n"
  "v_mfma_f32_16x16x32_bf16 v[80:83], a[248:251], v[172:175], v[80:83]\n"
  "v_mfma_f32_16x16x32_bf16 v[20:23], a[252:255], v[156:159], v[20:23]\n"
  "v_mfma_f32_16x16x32_bf16 v[52:55], a[252:255], v[164:167], v[52:55]\n"
  "ds_read_b64_tr_b16 a[208:209], v227\n"
  "ds_read_b64_tr_b16 a[210:211], v227 offset:256\n"
  "v_mfma_f32_16x16x32_bf16 v[84:87], a[252:255], v[172:175], v[84:87]\n"
  "v_mfma_f32_16x16x32_bf16 v[24:27], a[168:171], v[156:159], v[24:27]\n"
  "v_mfma_f32_16x16x32_bf16 v[56:59], a[168:171], v[164:167], v[56:59]\n"
  "ds_read_b64_tr_b16 a[212:213], v224 offset:512\n"
  "ds_read_b64_tr_b16 a[214:215], v224 offset:768\n"
  "v_mfma_f32_16x16x32_bf16 v[88:91], a[168:171], v[172:175], v[88:91]\n"
  "s_waitcnt lgkmcnt(6)\n"
  "v_mfma_f32_16x16x32_bf16 v[28:31], a[172:175], v[156:159], v[28:31]\n"
  "v_mfma_f32_16x16x32_bf16 v[60:63], a[172:175], v[164:167], v[60:63]\n"
  "ds_read_b64_tr_b16 a[216:217], v227 offset:512\n"
  "ds_read_b64_tr_b16 a[218:219], v227 offset:768\n"
  "v_mfma_f32_16x16x32_bf16 v[92:95], a[172:175], v[172:175], v[92:95]\n"
  "s_waitcnt vmcnt(5)\n"
  "v_mfma_f32_16x16x32_bf16 v[16:19], a[176:179], a[0:3], v[16:19]\n"
  "buffer_load_dwordx4 a[24:27], v250, s[76:79], s22 offen sc1\n"
  "s_waitcnt vmcnt(5)\n"
  "v_mfma_f32_16x16x32_bf16 v[48:51], a[176:179], a[4:7], v[48:51]\n"
  "ds_read_b64_tr_b16 a[220:221], v224 offset:12288\n"
  "ds_read_b64_tr_b16 a[222:223], v224 offset:12544\n"
  "s_waitcnt vmcnt(4)\n"
  "v_mfma_f32_16x16x32_bf16 v[80:83], a[176:179], a[8:11], v[80:83]\n"
  "v_mfma_f32_16x16x32_bf16 v[20:23], a[180:183], a[0:3], v[20:23]\n"
  "v_mfma_f32_16x16x32_bf16 v[52:55], a[180:183], a[4:7], v[52:55]\n"
  "buffer_load_dwordx4 a[28:31], v195, s[76:79], s22 offen sc1\n"
  "ds_read_b64_tr_b16 a[224:225], v227 offset:12288\n"
  "v_mfma_f32_16x16x32_bf16 v[84:87], a[180:183], a[8:11], v[84:87]\n"
  "ds_read_b64_tr_b16 a[226:227], v227 offset:12544\n"
  "v_mfma_f32_16x16x32_bf16 v[24:27], a[184:187], a[0:3], v[24:27]\n"
  "v_mfma_f32_16x16x32_bf16 v[56:59], a[184:187], a[4:7], v[56:59]\n"
  "ds_read_b64_tr_b16 a[228:229], v224 offset:12800\n"
  "ds_read_b64_tr_b16 a[230:231], v224 offset:13056\n"
  "v_mfma_f32_16x16x32_bf16 v[88:91], a[184:187], a[8:11], v[88:91]\n"
  "buffer_load_dwordx4 a[32:35], v196, s[76:79], s22 offen sc1\n"
  "s_waitcnt lgkmcnt(4)\n"
  "v_mfma_f32_16x16x32_bf16 v[28:31], a[188:191], a[0:3], v[28:31]\n"
  "v_mfma_f32_16x16x32_bf16 v[60:63], a[188:191], a[4:7], v[60:63]\n"
  "ds_read_b64_tr_b16 a[232:233], v227 offset:12800\n"
  "ds_read_b64_tr_b16 a[234:235], v227 offset:13056\n"
  "v_mfma_f32_16x16x32_bf16 v[92:95], a[188:191], a[8:11], v[92:95]\n"
  "v_mfma_f32_16x16x32_bf16 v[4:7], a[208:211], a[0:3], v[4:7]\n"
  "buffer_load_dwordx4 a[36:39], v250, s[76:79], s22 offen offset:1024 sc1\n"
  "v_mfma_f32_16x16x32_bf16 v[36:39], a[208:211], a[4:7], v[36:39]\n"
  "ds_read_b64_tr_b16 a[236:237], v227 offset:4096\n"
  "ds_read_b64_tr_b16 a[238:239], v227 offset:4352\n"
  "v_mfma_f32_16x16x32_bf16 v[68:71], a[208:211], a[8:11], v[68:71]\n"
  "v_mfma_f32_16x16x32_bf16 v[8:11], a[212:215], a[0:3], v[8:11]\n"
  "v_mfma_f32_16x16x32_bf16 v[40:43], a[212:215], a[4:7], v[40:43]\n"
  "buffer_load_dwordx4 a[40:43], v195, s[76:79], s22 offen offset:1024 sc1\n"
  "ds_read_b64_tr_b16 a[240:241], v224 offset:4608\n"
  "v_mfma_f32_16x16x32_bf16 v[72:75], a[212:215], a[8:11], v[72:75]\n"
  "ds_read_b64_tr_b16 a[242:243], v224 offset:4864\n"
  "v_mfma_f32_16x16x32_bf16 v[12:15], a[216:219], a[0:3], v[12:15]\n"
  "v_mfma_f32_16x16x32_bf16 v[44:47], a[216:219], a[4:7], v[44:47]\n"
  "ds_read_b64_tr_b16 a[244:245], v227 offset:4608\n"
  "ds_read_b64_tr_b16 a[246:247], v227 offset:4864\n"
  "v_mfma_f32_16x16x32_bf16 v[76:79], a[216:219], a[8:11], v[76:79]\n"
  "buffer_load_dwordx4 a[44:47], v196, s[76:79], s22 offen offset:1024 sc1\n"
  "s_waitcnt vmcnt(8)\n"
  "v_mfma_f32_16x16x32_bf16 v[16:19], a[220:223], a[12:15], v[16:19]\n"
  "s_waitcnt vmcnt(7)\n"
  "v_mfma_f32_16x16x32_bf16 v[48:51], a[220:223], a[16:19], v[48:51]\n"
  "ds_read_b64_tr_b16 a[248:249], v222 offset:8192\n"
  "ds_read_b64_tr_b16 a[250:251], v222 offset:8448\n"
  "s_waitcnt vmcnt(6)\n"
  "v_mfma_f32_16x16x32_bf16 v[80:83], a[220:223], a[20:23], v[80:83]\n"
  "s_waitcnt lgkmcnt(4)\n"
  "v_mfma_f32_16x16x32_bf16 v[20:23], a[224:227], a[12:15], v[20:23]\n"
  "buffer_load_dwordx4 a[48:51], v250, s[76:79], s23 offen sc1\n"
  "v_mfma_f32_16x16x32_bf16 v[52:55], a[224:227], a[16:19], v[52:55]\n"
  "ds_read_b64_tr_b16 a[252:253], v225 offset:8192\n"
  "ds_read_b64_tr_b16 a[254:255], v225 offset:8448\n"
  "v_mfma_f32_16x16x32_bf16 v[84:87], a[224:227], a[20:23], v[84:87]\n"
  "v_mfma_f32_16x16x32_bf16 v[24:27], a[228:231], a[12:15], v[24:27]\n"
  "v_mfma_f32_16x16x32_bf16 v[56:59], a[228:231], a[16:19], v[56:59]\n"
  "buffer_load_dwordx4 a[52:55], v195, s[76:79], s23 offen sc1\n"
  "ds_read_b64_tr_b16 a[168:169], v222 offset:8704\n"
  "v_mfma_f32_16x16x32_bf16 v[88:91], a[228:231], a[20:23], v[88:91]\n"
  "ds_read_b64_tr_b16 a[170:171], v222 offset:8960\n"
  "v_mfma_f32_16x16x32_bf16 v[28:31], a[232:235], a[12:15], v[28:31]\n"
  "buffer_load_dwordx4 a[192:195], v194, s[76:79], s20 offen sc1\n"
  "v_mfma_f32_16x16x32_bf16 v[60:63], a[232:235], a[16:19], v[60:63]\n"
  "ds_read_b64_tr_b16 a[172:173], v225 offset:8704\n"
  "ds_read_b64_tr_b16 a[174:175], v225 offset:8960\n"
  "v_mfma_f32_16x16x32_bf16 v[92:95], a[232:235], a[20:23], v[92:95]\n"
  "buffer_load_dwordx4 a[56:59], v196, s[76:79], s23 offen sc1\n"
  "v_mfma_f32_16x16x32_bf16 v[4:7], a[236:239], a[12:15], v[4:7]\n"
  "v_mfma_f32_16x16x32_bf16 v[36:39], a[236:239], a[16:19], v[36:39]\n"
  "ds_read_b64_tr_b16 a[176:177], v225\n"
  "ds_read_b64_tr_b16 a[178:179], v225 offset:256\n"
  "v_mfma_f32_16x16x32_bf16 v[68:71], a[236:239], a[20:23], v[68:71]\n"
  "v_mfma_f32_16x16x32_bf16 v[8:11], a[240:243], a[12:15], v[8:11]\n"
  "buffer_load_dwordx4 a[60:63], v250, s[76:79], s23 offen offset:1024 sc1\n"
  "v_mfma_f32_16x16x32_bf16 v[40:43], a[240:243], a[16:19], v[40:43]\n"
  "buffer_load_dwordx4 a[196:199], v194, s[76:79], s21 offen sc1\n"
  "ds_read_b64_tr_b16 a[180:181], v222 offset:512\n"
  "v_mfma_f32_16x16x32_bf16 v[72:75], a[240:243], a[20:23], v[72:75]\n"
  "ds_read_b64_tr_b16 a[182:183], v222 offset:768\n"
  "s_waitcnt lgkmcnt(4)\n"
  "v_mfma_f32_16x16x32_bf16 v[12:15], a[244:247], a[12:15], v[12:15]\n"
  "v_mfma_f32_16x16x32_bf16 v[44:47], a[244:247], a[16:19], v[44:47]\n"
  "buffer_load_dword v188, v185, s[84:87], s98 offen sc1\n"
  "buffer_load_dwordx4 a[64:67], v195, s[76:79], s23 offen offset:1024 sc1\n"
  "v_mfma_f32_16x16x32_bf16 v[76:79], a[244:247], a[20:23], v[76:79]\n"
  "ds_read_b64_tr_b16 a[184:185], v225 offset:512\n"
  "ds_read_b64_tr_b16 a[186:187], v225 offset:768\n"
  "s_waitcnt vmcnt(13)\n"
  "v_mfma_f32_16x16x32_bf16 v[16:19], a[248:251], a[24:27], v[16:19]\n"
  "buffer_load_dwordx4 a[200:203], v194, s[76:79], s22 offen sc1\n"
  "s_waitcnt vmcnt(13)\n"
  "v_mfma_f32_16x16x32_bf16 v[48:51], a[248:251], a[28:31], v[48:51]\n"
  "ds_read_b64_tr_b16 a[188:189], v222 offset:12288\n"
  "ds_read_b64_tr_b16 a[190:191], v222 offset:12544\n"
  "s_waitcnt vmcnt(12)\n"
  "v_mfma_f32_16x16x32_bf16 v[80:83], a[248:251], a[32:35], v[80:83]\n"
  "buffer_load_dwordx4 a[68:71], v196, s[76:79], s23 offen offset:1024 sc1\n"
  "v_mfma_f32_16x16x32_bf16 v[20:23], a[252:255], a[24:27], v[20:23]\n"
  "v_mfma_f32_16x16x32_bf16 v[52:55], a[252:255], a[28:31], v[52:55]\n"
  "ds_read_b64_tr_b16 a[208:209], v225 offset:12288\n"
  "ds_read_b64_tr_b16 a[210:211], v225 offset:12544\n"
  "v_mfma_f32_16x16x32_bf16 v[84:87], a[252:255], a[32:35], v[84:87]\n"
  "v_mfma_f32_16x16x32_bf16 v[24:27], a[168:171], a[24:27], v[24:27]\n"
  "buffer_load_dwordx4 a[72:75], v250, s[76:79], s21 offen offset:2048 sc1\n"
  "v_mfma_f32_16x16x32_bf16 v[56:59], a[168:171], a[28:31], v[56:59]\n"
  "buffer_load_dwordx4 a[204:207], v194, s[76:79], s23 offen sc1\n"
  "ds_read_b64_tr_b16 a[212:213], v222 offset:12800\n"
  "v_mfma_f32_16x16x32_bf16 v[88:91], a[168:171], a[32:35], v[88:91]\n"
  "ds_read_b64_tr_b16 a[214:215], v222 offset:13056\n"
  "v_mfma_f32_16x16x32_bf16 v[28:31], a[172:175], a[24:27], v[28:31]\n"
  "v_mfma_f32_16x16x32_bf16 v[60:63], a[172:175], a[28:31], v[60:63]\n"
  "buffer_load_dwordx4 a[76:79], v195, s[76:79], s21 offen offset:2048 sc1\n"
  "ds_read_b64_tr_b16 a[216:217], v225 offset:12800\n"
  "v_mfma_f32_16x16x32_bf16 v[92:95], a[172:175], a[32:35], v[92:95]\n"
  "ds_read_b64_tr_b16 a[218:219], v225 offset:13056\n"
  "s_waitcnt lgkmcnt(4)\n"
  "v_mfma_f32_16x16x32_bf16 v[4:7], a[176:179], a[24:27], v[4:7]\n"
  "v_mfma_f32_16x16x32_bf16 v[36:39], a[176:179], a[28:31], v[36:39]\n"
  "ds_read_b64_tr_b16 a[220:221], v224 offset:24576\n"
  "ds_read_b64_tr_b16 a[222:223], v224 offset:24832\n"
  "v_mfma_f32_16x16x32_bf16 v[68:71], a[176:179], a[32:35], v[68:71]\n"
  "buffer_load_dwordx4 a[80:83], v196, s[76:79], s21 offen offset:2048 sc1\n"
  "v_mfma_f32_16x16x32_bf16 v[8:11], a[180:183], a[24:27], v[8:11]\n"
  "v_mfma_f32_16x16x32_bf16 v[40:43], a[180:183], a[28:31], v[40:43]\n"
  "ds_read_b64_tr_b16 a[224:225], v227 offset:24576\n"
  "ds_read_b64_tr_b16 a[226:227], v227 offset:24832\n"
  "v_mfma_f32_16x16x32_bf16 v[72:75], a[180:183], a[32:35], v[72:75]\n"
  "v_mfma_f32_16x16x32_bf16 v[12:15], a[184:187], a[24:27], v[12:15]\n"
  "buffer_load_dwordx4 a[84:87], v250, s[76:79], s21 offen offset:3072 sc1\n"
  "v_mfma_f32_16x16x32_bf16 v[44:47], a[184:187], a[28:31], v[44:47]\n"
  "ds_read_b64_tr_b16 a[228:229], v224 offset:25088\n"
  "ds_read_b64_tr_b16 a[230:231], v224 offset:25344\n"
  "v_mfma_f32_16x16x32_bf16 v[76:79], a[184:187], a[32:35], v[76:79]\n"
  "s_waitcnt vmcnt(17)\n"
  "v_mfma_f32_16x16x32_bf16 v[16:19], a[188:191], a[36:39], v[16:19]\n"
  "s_waitcnt vmcnt(16)\n"
  "v_mfma_f32_16x16x32_bf16 v[48:51], a[188:191], a[40:43], v[48:51]\n"
  "buffer_load_dwordx4 a[88:91], v195, s[76:79], s21 offen offset:3072 sc1\n"
  "ds_read_b64_tr_b16 a[232:233], v227 offset:25088\n"
  "s_waitcnt vmcnt(16)\n"
  "v_mfma_f32_16x16x32_bf16 v[80:83], a[188:191], a[44:47], v[80:83]\n"
  "ds_read_b64_tr_b16 a[234:235], v227 offset:25344\n"
  "v_mfma_f32_16x16x32_bf16 v[20:23], a[208:211], a[36:39], v[20:23]\n"
  "v_mfma_f32_16x16x32_bf16 v[52:55], a[208:211], a[40:43], v[52:55]\n"
  "ds_read_b64_tr_b16 a[236:237], v224 offset:28672\n"
  "ds_read_b64_tr_b16 a[238:239], v224 offset:28928\n"
  "v_mfma_f32_16x16x32_bf16 v[84:87], a[208:211], a[44:47], v[84:87]\n"
  "buffer_load_dwordx4 a[92:95], v196, s[76:79], s21 offen offset:3072 sc1\n"
  "s_waitcnt lgkmcnt(4)\n"
  "v_mfma_f32_16x16x32_bf16 v[24:27], a[212:215], a[36:39], v[24:27]\n"
  "v_mfma_f32_16x16x32_bf16 v[56:59], a[212:215], a[40:43], v[56:59]\n"
  "ds_read_b64_tr_b16 a[240:241], v227 offset:28672\n"
  "ds_read_b64_tr_b16 a[242:243], v227 offset:28928\n"
  "v_mfma_f32_16x16x32_bf16 v[88:91], a[212:215], a[44:47], v[88:91]\n"
  "v_mfma_f32_16x16x32_bf16 v[28:31], a[216:219], a[36:39], v[28:31]\n"
  "buffer_load_dwordx4 a[96:99], v250, s[76:79], s22 offen offset:2048 sc1\n"
  "v_mfma_f32_16x16x32_bf16 v[60:63], a[216:219], a[40:43], v[60:63]\n"
  "ds_read_b64_tr_b16 a[244:245], v224 offset:29184\n"
  "ds_read_b64_tr_b16 a[246:247], v224 offset:29440\n"
  "v_mfma_f32_16x16x32_bf16 v[92:95], a[216:219], a[44:47], v[92:95]\n"
  "s_waitcnt vmcnt(17)\n"
  "v_mfma_f32_16x16x32_bf16 v[16:19], a[220:223], a[48:51], v[16:19]\n"
  "s_waitcnt vmcnt(16)\n"
  "v_mfma_f32_16x16x32_bf16 v[48:51], a[220:223], a[52:55], v[48:51]\n"
  "buffer_load_dwordx4 a[100:103], v195, s[76:79], s22 offen offset:2048 sc1\n"
  "ds_read_b64_tr_b16 a[248:249], v227 offset:29184\n"
  "s_waitcnt vmcnt(15)\n"
  "v_mfma_f32_16x16x32_bf16 v[80:83], a[220:223], a[56:59], v[80:83]\n"
  "ds_read_b64_tr_b16 a[250:251], v227 offset:29440\n"
  "v_mfma_f32_16x16x32_bf16 v[20:23], a[224:227], a[48:51], v[20:23]\n"
  "v_mfma_f32_16x16x32_bf16 v[52:55], a[224:227], a[52:55], v[52:55]\n"
  "ds_read_b64_tr_b16 a[252:253], v223 offset:57344\n"
  "ds_read_b64_tr_b16 a[254:255], v223 offset:57600\n"
  "v_mfma_f32_16x16x32_bf16 v[84:87], a[224:227], a[56:59], v[84:87]\n"
  "buffer_load_dwordx4 a[104:107], v196, s[76:79], s22 offen offset:2048 sc1\n"
  "v_mfma_f32_16x16x32_bf16 v[24:27], a[228:231], a[48:51], v[24:27]\n"
  "v_mfma_f32_16x16x32_bf16 v[56:59], a[228:231], a[52:55], v[56:59]\n"
  "ds_read_b64_tr_b16 a[168:169], v226 offset:57344\n"
  "ds_read_b64_tr_b16 a[170:171], v226 offset:57600\n"
  "v_mfma_f32_16x16x32_bf16 v[88:91], a[228:231], a[56:59], v[88:91]\n"
  "s_waitcnt lgkmcnt(5)\n"
  "v_mfma_f32_16x16x32_bf16 v[28:31], a[232:235], a[48:51], v[28:31]\n"
  "buffer_load_dwordx4 a[108:111], v250, s[76:79], s22 offen offset:3072 sc1\n"
  "v_mfma_f32_16x16x32_bf16 v[60:63], a[232:235], a[52:55], v[60:63]\n"
  "ds_read_b64_tr_b16 a[172:173], v223 offset:57856\n"
  "ds_read_b64_tr_b16 a[174:175], v223 offset:58112\n"
  "v_mfma_f32_16x16x32_bf16 v[92:95], a[232:235], a[56:59], v[92:95]\n"
  "s_waitcnt vmcnt(16)\n"
  "v_mfma_f32_16x16x32_bf16 v[16:19], a[236:239], a[60:63], v[16:19]\n"
  "s_waitcnt vmcnt(13)\n"
  "v_mfma_f32_16x16x32_bf16 v[48:51], a[236:239], a[64:67], v[48:51]\n"
  "buffer_load_dwordx4 a[112:115], v195, s[76:79], s22 offen offset:3072 sc1\n"
  "ds_read_b64_tr_b16 a[176:177], v226 offset:57856\n"
  "s_waitcnt vmcnt(12)\n"
  "v_mfma_f32_16x16x32_bf16 v[80:83], a[236:239], a[68:71], v[80:83]\n"
  "ds_read_b64_tr_b16 a[178:179], v226 offset:58112\n"
  "v_mfma_f32_16x16x32_bf16 v[20:23], a[240:243], a[60:63], v[20:23]\n"
  "v_mfma_f32_16x16x32_bf16 v[52:55], a[240:243], a[64:67], v[52:55]\n"
  "ds_read_b64_tr_b16 a[180:181], v223 offset:61440\n"
  "ds_read_b64_tr_b16 a[182:183], v223 offset:61696\n"
  "v_mfma_f32_16x16x32_bf16 v[84:87], a[240:243], a[68:71], v[84:87]\n"
  "buffer_load_dwordx4 a[116:119], v196, s[76:79], s22 offen offset:3072 sc1\n"
  "v_mfma_f32_16x16x32_bf16 v[24:27], a[244:247], a[60:63], v[24:27]\n"
  "v_mfma_f32_16x16x32_bf16 v[56:59], a[244:247], a[64:67], v[56:59]\n"
  "ds_read_b64_tr_b16 a[184:185], v226 offset:61440\n"
  "ds_read_b64_tr_b16 a[186:187], v226 offset:61696\n"
  "v_mfma_f32_16x16x32_bf16 v[88:91], a[244:247], a[68:71], v[88:91]\n"
  "s_waitcnt lgkmcnt(5)\n"
  "v_mfma_f32_16x16x32_bf16 v[28:31], a[248:251], a[60:63], v[28:31]\n"
  "buffer_load_dwordx4 a[120:123], v250, s[76:79], s23 offen offset:2048 sc1\n"
  "v_mfma_f32_16x16x32_bf16 v[60:63], a[248:251], a[64:67], v[60:63]\n"
  "ds_read_b64_tr_b16 a[188:189], v223 offset:61952\n"
  "ds_read_b64_tr_b16 a[190:191], v223 offset:62208\n"
  "v_mfma_f32_16x16x32_bf16 v[92:95], a[248:251], a[68:71], v[92:95]\n"
  "s_waitcnt vmcnt(13)\n"
  "v_mfma_f32_16x16x32_bf16 v[16:19], a[252:255], a[72:75], v[16:19]\n"
  "s_waitcnt vmcnt(11)\n"
  "v_mfma_f32_16x16x32_bf16 v[48:51], a[252:255], a[76:79], v[48:51]\n"
  "buffer_load_dwordx4 a[124:127], v195, s[76:79], s23 offen offset:2048 sc1\n"
  "ds_read_b64_tr_b16 a[208:209], v226 offset:61952\n"
  "s_waitcnt vmcnt(11)\n"
  "v_mfma_f32_16x16x32_bf16 v[80:83], a[252:255], a[80:83], v[80:83]\n"
  "ds_read_b64_tr_b16 a[210:211], v226 offset:62208\n"
  "v_mfma_f32_16x16x32_bf16 v[20:23], a[168:171], a[72:75], v[20:23]\n"
  "v_mfma_f32_16x16x32_bf16 v[52:55], a[168:171], a[76:79], v[52:55]\n"
  "ds_read_b64_tr_b16 a[212:213], v222 offset:57344\n"
  "ds_read_b64_tr_b16 a[214:215], v222 offset:57600\n"
  "v_mfma_f32_16x16x32_bf16 v[84:87], a[168:171], a[80:83], v[84:87]\n"
  "buffer_load_dwordx4 a[128:131], v196, s[76:79], s23 offen offset:2048 sc1\n"
  "v_mfma_f32_16x16x32_bf16 v[24:27], a[172:175], a[72:75], v[24:27]\n"
  "v_mfma_f32_16x16x32_bf16 v[56:59], a[172:175], a[76:79], v[56:59]\n"
  "ds_read_b64_tr_b16 a[216:217], v225 offset:57344\n"
  "ds_read_b64_tr_b16 a[218:219], v225 offset:57600\n"
  "v_mfma_f32_16x16x32_bf16 v[88:91], a[172:175], a[80:83], v[88:91]\n"
  "s_waitcnt lgkmcnt(5)\n"
  "v_mfma_f32_16x16x32_bf16 v[28:31], a[176:179], a[72:75], v[28:31]\n"
  "buffer_load_dwordx4 a[132:135], v250, s[76:79], s23 offen offset:3072 sc1\n"
  "v_mfma_f32_16x16x32_bf16 v[60:63], a[176:179], a[76:79], v[60:63]\n"
  "ds_read_b64_tr_b16 a[220:221], v222 offset:57856\n"
  "ds_read_b64_tr_b16 a[222:223], v222 offset:58112\n"
  "v_mfma_f32_16x16x32_bf16 v[92:95], a[176:179], a[80:83], v[92:95]\n"
  "s_waitcnt vmcnt(12)\n"
  "v_mfma_f32_16x16x32_bf16 v[16:19], a[180:183], a[84:87], v[16:19]\n"
  "s_waitcnt vmcnt(11)\n"
  "v_mfma_f32_16x16x32_bf16 v[48:51], a[180:183], a[88:91], v[48:51]\n"
  "buffer_load_dwordx4 a[136:139], v195, s[76:79], s23 offen offset:3072 sc1\n"
  "ds_read_b64_tr_b16 a[224:225], v225 offset:57856\n"
  "s_waitcnt vmcnt(11)\n"
  "v_mfma_f32_16x16x32_bf16 v[80:83], a[180:183], a[92:95], v[80:83]\n"
  "ds_read_b64_tr_b16 a[226:227], v225 offset:58112\n"
  "v_mfma_f32_16x16x32_bf16 v[20:23], a[184:187], a[84:87], v[20:23]\n"
  "v_mfma_f32_16x16x32_bf16 v[52:55], a[184:187], a[88:91], v[52:55]\n"
  "ds_read_b64_tr_b16 a[228:229], v222 offset:61440\n"
  "ds_read_b64_tr_b16 a[230:231], v222 offset:61696\n"
  "v_mfma_f32_16x16x32_bf16 v[84:87], a[184:187], a[92:95], v[84:87]\n"
  "buffer_load_dwordx4 a[140:143], v196, s[76:79], s23 offen offset:3072 sc1\n"
  "v_mfma_f32_16x16x32_bf16 v[24:27], a[188:191], a[84:87], v[24:27]\n"
  "v_mfma_f32_16x16x32_bf16 v[56:59], a[188:191], a[88:91], v[56:59]\n"
  "ds_read_b64_tr_b16 a[232:233], v225 offset:61440\n"
  "ds_read_b64_tr_b16 a[234:235], v225 offset:61696\n"
  "v_mfma_f32_16x16x32_bf16 v[88:91], a[188:191], a[92:95], v[88:91]\n"
  "s_waitcnt lgkmcnt(5)\n"
  "v_mfma_f32_16x16x32_bf16 v[28:31], a[208:211], a[84:87], v[28:31]\n"
  "v_mfma_f32_16x16x32_bf16 v[60:63], a[208:211], a[88:91], v[60:63]\n"
  "ds_read_b64_tr_b16 a[236:237], v222 offset:61952\n"
  "ds_read_b64_tr_b16 a[238:239], v222 offset:62208\n"
  "v_mfma_f32_16x16x32_bf16 v[92:95], a[208:211], a[92:95], v[92:95]\n"
  "L_st7:\n"
  "s_cbranch_execz L_stx\n"
  "s_waitcnt vmcnt(11)\n"
  "v_mfma_f32_16x16x32_bf16 v[16:19], a[212:215], a[96:99], v[16:19]\n"
  "s_waitcnt vmcnt(10)\n"
  "v_mfma_f32_16x16x32_bf16 v[48:51], a[212:215], a[100:103], v[48:51]\n"
  "ds_read_b64_tr_b16 a[240:241], v225 offset:61952\n"
  "ds_read_b64_tr_b16 a[242:243], v225 offset:62208\n"
  "s_waitcnt vmcnt(9)\n"
  "v_mfma_f32_16x16x32_bf16 v[80:83], a[212:215], a[104:107], v[80:83]\n"
  "v_mfma_f32_16x16x32_bf16 v[20:23], a[216:219], a[96:99], v[20:23]\n"
  "v_mfma_f32_16x16x32_bf16 v[52:55], a[216:219], a[100:103], v[52:55]\n"
  "ds_read_b64_tr_b16 a[244:245], v222 offset:40960\n"
  "ds_read_b64_tr_b16 a[246:247], v222 offset:41216\n"
  "v_mfma_f32_16x16x32_bf16 v[84:87], a[216:219], a[104:107], v[84:87]\n"
  "v_mfma_f32_16x16x32_bf16 v[24:27], a[220:223], a[96:99], v[24:27]\n"
  "v_mfma_f32_16x16x32_bf16 v[56:59], a[220:223], a[100:103], v[56:59]\n"
  "ds_read_b64_tr_b16 a[248:249], v225 offset:40960\n"
  "ds_read_b64_tr_b16 a[250:251], v225 offset:41216\n"
  "v_mfma_f32_16x16x32_bf16 v[88:91], a[220:223], a[104:107], v[88:91]\n"
  "s_waitcnt lgkmcnt(5)\n"
  "v_mfma_f32_16x16x32_bf16 v[28:31], a[224:227], a[96:99], v[28:31]\n"
  "v_mfma_f32_16x16x32_bf16 v[60:63], a[224:227], a[100:103], v[60:63]\n"
  "ds_read_b64_tr_b16 a[252:253], v222 offset:41472\n"
  "ds_read_b64_tr_b16 a[254:255], v222 offset:41728\n"
  "v_mfma_f32_16x16x32_bf16 v[92:95], a[224:227], a[104:107], v[92:95]\n"
  "s_waitcnt vmcnt(8)\n"
  "v_mfma_f32_16x16x32_bf16 v[16:19], a[228:231], a[108:111], v[16:19]\n"
  "s_waitcnt vmcnt(7)\n"
  "v_mfma_f32_16x16x32_bf16 v[48:51], a[228:231], a[112:115], v[48:51]\n"
  "ds_read_b64_tr_b16 a[168:169], v225 offset:41472\n"
  "ds_read_b64_tr_b16 a[170:171], v225 offset:41728\n"
  "s_waitcnt vmcnt(6)\n"
  "v_mfma_f32_16x16x32_bf16 v[80:83], a[228:231], a[116:119], v[80:83]\n"
  "ds_write_b128 v244, a[192:195]\n"
  "v_mfma_f32_16x16x32_bf16 v[20:23], a[232:235], a[108:111], v[20:23]\n"
  "v_mfma_f32_16x16x32_bf16 v[52:55], a[232:235], a[112:115], v[52:55]\n"
  "ds_write_b128 v244, a[196:199] offset:4096\n"
  "ds_read_b64_tr_b16 a[172:173], v222 offset:45056\n"
  "v_mfma_f32_16x16x32_bf16 v[84:87], a[232:235], a[116:119], v[84:87]\n"
  "ds_read_b64_tr_b16 a[174:175], v222 offset:45312\n"
  "v_mfma_f32_16x16x32_bf16 v[24:27], a[236:239], a[108:111], v[24:27]\n"
  "ds_write_b128 v244, a[200:203] offset:8192\n"
  "v_mfma_f32_16x16x32_bf16 v[56:59], a[236:239], a[112:115], v[56:59]\n"
  "ds_read_b64_tr_b16 a[176:177], v225 offset:45056\n"
  "ds_read_b64_tr_b16 a[178:179], v225 offset:45312\n"
  "v_mfma_f32_16x16x32_bf16 v[88:91], a[236:239], a[116:119], v[88:91]\n"
  "ds_write_b128 v244, a[204:207] offset:12288\n"
  "s_waitcnt lgkmcnt(7)\n"
  "v_mfma_f32_16x16x32_bf16 v[28:31], a[240:243], a[108:111], v[28:31]\n"
  "v_mfma_f32_16x16x32_bf16 v[60:63], a[240:243], a[112:115], v[60:63]\n"
  "ds_read_b64_tr_b16 a[180:181], v222 offset:45568\n"
  "ds_read_b64_tr_b16 a[182:183], v222 offset:45824\n"
  "v_mfma_f32_16x16x32_bf16 v[92:95], a[240:243], a[116:119], v[92:95]\n"
  "s_waitcnt vmcnt(5)\n"
  "v_mfma_f32_16x16x32_bf16 v[16:19], a[244:247], a[120:123], v[16:19]\n"
  "s_waitcnt vmcnt(4)\n"
  "v_mfma_f32_16x16x32_bf16 v[48:51], a[244:247], a[124:127], v[48:51]\n"
  "ds_read_b64_tr_b16 a[184:185], v225 offset:45568\n"
  "ds_read_b64_tr_b16 a[186:187], v225 offset:45824\n"
  "s_waitcnt vmcnt(3)\n"
  "v_mfma_f32_16x16x32_bf16 v[80:83], a[244:247], a[128:131], v[80:83]\n"
  "v_mfma_f32_16x16x32_bf16 v[20:23], a[248:251], a[120:123], v[20:23]\n"
  "v_mfma_f32_16x16x32_bf16 v[52:55], a[248:251], a[124:127], v[52:55]\n"
  "ds_read_b64_tr_b16 a[188:189], v229\n"
  "ds_read_b64_tr_b16 a[190:191], v229 offset:256\n"
  "v_mfma_f32_16x16x32_bf16 v[84:87], a[248:251], a[128:131], v[84:87]\n"
  "ds_read_b64_tr_b16 a[208:209], v232\n"
  "ds_read_b64_tr_b16 a[210:211], v232 offset:256\n"
  "v_mfma_f32_16x16x32_bf16 v[24:27], a[252:255], a[120:123], v[24:27]\n"
  "ds_read_b64_tr_b16 a[212:213], v229 offset:4096\n"
  "ds_read_b64_tr_b16 a[214:215], v229 offset:4352\n"
  "v_mfma_f32_16x16x32_bf16 v[56:59], a[252:255], a[124:127], v[56:59]\n"
  "ds_read_b64_tr_b16 a[216:217], v232 offset:4096\n"
  "ds_read_b64_tr_b16 a[218:219], v232 offset:4352\n"
  "v_mfma_f32_16x16x32_bf16 v[88:91], a[252:255], a[128:131], v[88:91]\n"
  "ds_read_b64_tr_b16 a[220:221], v225 offset:4096\n"
  "ds_read_b64_tr_b16 a[222:223], v225 offset:4352\n"
  "v_mfma_f32_16x16x32_bf16 v[28:31], a[168:171], a[120:123], v[28:31]\n"
  "v_mfma_f32_16x16x32_bf16 v[60:63], a[168:171], a[124:127], v[60:63]\n"
  "s_waitcnt lgkmcnt(14)\n"
  "s_barrier\n"
  "v_mfma_f32_16x16x32_bf16 v[92:95], a[168:171], a[128:131], v[92:95]\n"
  "ds_read_b128 a[144:147], v251 offset:16384\n"
  "ds_read_b64_tr_b16 a[224:225], v222 offset:4608\n"
  "s_waitcnt vmcnt(2)\n"
  "v_mfma_f32_16x16x32_bf16 v[16:19], a[172:175], a[132:135], v[16:19]\n"
  "ds_read_b64_tr_b16 a[226:227], v222 offset:4864\n"
  "s_waitcnt vmcnt(1)\n"
  "v_mfma_f32_16x16x32_bf16 v[48:51], a[172:175], a[136:139], v[48:51]\n"
  "ds_read_b128 a[148:151], v251 offset:17408\n"
  "s_waitcnt vmcnt(0)\n"
  "v_mfma_f32_16x16x32_bf16 v[80:83], a[172:175], a[140:143], v[80:83]\n"
  "ds_read_b64_tr_b16 a[228:229], v225 offset:4608\n"
  "ds_read_b64_tr_b16 a[230:231], v225 offset:4864\n"
  "v_mfma_f32_16x16x32_bf16 v[20:23], a[176:179], a[132:135], v[20:23]\n"
  "ds_read_b128 a[152:155], v251 offset:18432\n"
  "v_mfma_f32_16x16x32_bf16 v[52:55], a[176:179], a[136:139], v[52:55]\n"
  "v_mfma_f32_16x16x32_bf16 v[84:87], a[176:179], a[140:143], v[84:87]\n"
  "s_xor_b32 s61, s64, 4\n"
  "s_mul_i32 s61, s61, 0xd000\n"
  "s_waitcnt lgkmcnt(4)\n"
  "v_mfma_f32_16x16x32_bf16 v[24:27], a[180:183], a[132:135], v[24:27]\n"
  "v_cvt_pk_bf16_f32 v16, v16, v17\n"
  "v_cvt_pk_bf16_f32 v17, v18, v19\n"
  "v_mfma_f32_16x16x32_bf16 v[56:59], a[180:183], a[136:139], v[56:59]\n"
  "v_cvt_pk_bf16_f32 v18, v48, v49\n"
  "v_cvt_pk_bf16_f32 v19, v50, v51\n"
  "v_mfma_f32_16x16x32_bf16 v[88:91], a[180:183], a[140:143], v[88:91]\n"
  "buffer_store_dwordx4 v[16:19], v190, s[36:39], s61 offen sc1\n"
  "v_cvt_pk_bf16_f32 v80, v80, v81\n"
  "v_mfma_f32_16x16x32_bf16 v[28:31], a[184:187], a[132:135], v[28:31]\n"
  "v_cvt_pk_bf16_f32 v81, v82, v83\n"
  "buffer_store_dwordx2 v[80:81], v191, s[36:39], s61 offen offset:1024 sc1\n"
  "v_mfma_f32_16x16x32_bf16 v[60:63], a[184:187], a[136:139], v[60:63]\n"
  "s_xor_b32 s62, s64, 5\n"
  "s_mul_i32 s62, s62, 0xd000\n"
  "v_mfma_f32_16x16x32_bf16 v[92:95], a[184:187], a[140:143], v[92:95]\n"
  "v_cvt_pk_bf16_f32 v20, v20, v21\n"
  "v_cvt_pk_bf16_f32 v21, v22, v23\n"
  "v_mfma_f32_16x16x32_bf16 v[96:99], a[188:191], a[144:147], 0\n"
  "v_cvt_pk_bf16_f32 v22, v52, v53\n"
  "v_cvt_pk_bf16_f32 v23, v54, v55\n"
  "v_mfma_f32_16x16x32_bf16 v[100:103], a[208:211], a[144:147], 0\n"
  "buffer_store_dwordx4 v[20:23], v190, s[36:39], s62 offen sc1\n"
  "v_cvt_pk_bf16_f32 v84, v84, v85\n"
  "s_waitcnt lgkmcnt(0)\n"
  "v_mfma_f32_16x16x32_bf16 v[96:99], a[212:215], a[148:151], v[96:99]\n"
  "v_cvt_pk_bf16_f32 v85, v86, v87\n"
  "buffer_store_dwordx2 v[84:85], v191, s[36:39], s62 offen offset:1024 sc1\n"
  "v_mfma_f32_16x16x32_bf16 v[100:103], a[216:219], a[148:151], v[100:103]\n"
  "s_xor_b32 s61, s64, 6\n"
  "ds_read_b64_tr_b16 a[232:233], v229 offset:16384\n"
  "v_mfma_f32_16x16x32_bf16 v[4:7], a[220:223], a[36:39], v[4:7]\n"
  "s_mul_i32 s61, s61, 0xd000\n"
  "ds_read_b64_tr_b16 a[234:235], v229 offset:16640\n"
  "v_cvt_pk_bf16_f32 v24, v24, v25\n"
  "v_mfma_f32_16x16x32_bf16 v[36:39], a[220:223], a[40:43], v[36:39]\n"
  "ds_read_b64_tr_b16 a[236:237], v232 offset:16384\n"
  "v_cvt_pk_bf16_f32 v25, v26, v27\n"
  "ds_read_b64_tr_b16 a[238:239], v232 offset:16640\n"
  "v_mfma_f32_16x16x32_bf16 v[68:71], a[220:223], a[44:47], v[68:71]\n"
  "v_cvt_pk_bf16_f32 v26, v56, v57\n"
  "ds_read_b64_tr_b16 a[240:241], v229 offset:20480\n"
  "v_cvt_pk_bf16_f32 v27, v58, v59\n"
  "v_mfma_f32_16x16x32_bf16 v[8:11], a[224:227], a[36:39], v[8:11]\n"
  "ds_read_b128 a[156:159], v251 offset:19456\n"
  "ds_read_b64_tr_b16 a[242:243], v229 offset:20736\n"
  "buffer_store_dwordx4 v[24:27], v190, s[36:39], s61 offen sc1\n"
  "v_mfma_f32_16x16x32_bf16 v[40:43], a[224:227], a[40:43], v[40:43]\n"
  "ds_read_b64_tr_b16 a[244:245], v232 offset:20480\n"
  "v_cvt_pk_bf16_f32 v88, v88, v89\n"
  "ds_read_b64_tr_b16 a[246:247], v232 offset:20736\n"
  "v_mfma_f32_16x16x32_bf16 v[72:75], a[224:227], a[44:47], v[72:75]\n"
  "v_cvt_pk_bf16_f32 v89, v90, v91\n"
  "ds_read_b64_tr_b16 a[248:249], v227 offset:16384\n"
  "buffer_store_dwordx2 v[88:89], v191, s[36:39], s61 offen offset:1024 sc1\n"
  "v_mfma_f32_16x16x32_bf16 v[12:15], a[228:231], a[36:39], v[12:15]\n"
  "ds_read_b64_tr_b16 a[250:251], v227 offset:16640\n"
  "s_xor_b32 s62, s64, 7\n"
  "s_mul_i32 s62, s62, 0xd000\n"
  "v_mfma_f32_16x16x32_bf16 v[44:47], a[228:231], a[40:43], v[44:47]\n"
  "v_cvt_pk_bf16_f32 v28, v28, v29\n"
  "v_cvt_pk_bf16_f32 v29, v30, v31\n"
  "v_cvt_pk_bf16_f32 v30, v60, v61\n"
  "v_mfma_f32_16x16x32_bf16 v[76:79], a[228:231], a[44:47], v[76:79]\n"
  "ds_read_b64_tr_b16 a[252:253], v224 offset:16896\n"
  "v_cvt_pk_bf16_f32 v31, v62, v63\n"
  "ds_read_b64_tr_b16 a[254:255], v224 offset:17152\n"
  "s_waitcnt lgkmcnt(3)\n"
  "v_mfma_f32_16x16x32_bf16 v[96:99], a[232:235], a[152:155], v[96:99]\n"
  "buffer_store_dwordx4 v[28:31], v190, s[36:39], s62 offen sc1\n"
  "v_cvt_pk_bf16_f32 v92, v92, v93\n"
  "v_cvt_pk_bf16_f32 v93, v94, v95\n"
  "v_mfma_f32_16x16x32_bf16 v[100:103], a[236:239], a[152:155], v[100:103]\n"
  "buffer_store_dwordx2 v[92:93], v191, s[36:39], s62 offen offset:1024 sc1\n"
  "ds_read_b64_tr_b16 a[168:169], v227 offset:16896\n"
  "ds_read_b64_tr_b16 a[170:171], v227 offset:17152\n"
  "v_mfma_f32_16x16x32_bf16 v[96:99], a[240:243], a[156:159], v[96:99]\n"
  "ds_read_b64_tr_b16 a[172:173], v230\n"
  "ds_read_b128 a[160:163], v251 offset:20480\n"
  "v_mfma_f32_16x16x32_bf16 v[100:103], a[244:247], a[156:159], v[100:103]\n"
  "ds_read_b64_tr_b16 a[174:175], v230 offset:256\n"
  "ds_read_b64_tr_b16 a[176:177], v233\n"
  "s_waitcnt lgkmcnt(6)\n"
  "v_mfma_f32_16x16x32_bf16 v[4:7], a[248:251], a[48:51], v[4:7]\n"
  "ds_read_b64_tr_b16 a[178:179], v233 offset:256\n"
  "ds_read_b64_tr_b16 a[180:181], v230 offset:4096\n"
  "v_mfma_f32_16x16x32_bf16 v[36:39], a[248:251], a[52:55], v[36:39]\n"
  "ds_read_b128 a[164:167], v251 offset:21504\n"
  "ds_read_b64_tr_b16 a[182:183], v230 offset:4352\n"
  "v_mfma_f32_16x16x32_bf16 v[68:71], a[248:251], a[56:59], v[68:71]\n"
  "ds_read_b64_tr_b16 a[184:185], v233 offset:4096\n"
  "ds_read_b64_tr_b16 a[186:187], v233 offset:4352\n"
  "v_mfma_f32_16x16x32_bf16 v[8:11], a[252:255], a[48:51], v[8:11]\n"
  "ds_read_b64_tr_b16 a[188:189], v227 offset:20480\n"
  "ds_read_b64_tr_b16 a[190:191], v227 offset:20736\n"
  "v_mfma_f32_16x16x32_bf16 v[40:43], a[252:255], a[52:55], v[40:43]\n"
  "ds_read_b64_tr_b16 a[208:209], v224 offset:20992\n"
  "ds_read_b64_tr_b16 a[210:211], v224 offset:21248\n"
  "v_mfma_f32_16x16x32_bf16 v[72:75], a[252:255], a[56:59], v[72:75]\n"
  "ds_read_b64_tr_b16 a[212:213], v227 offset:20992\n"
  "ds_read_b64_tr_b16 a[214:215], v227 offset:21248\n"
  "s_waitcnt lgkmcnt(9)\n"
  "v_mfma_f32_16x16x32_bf16 v[12:15], a[168:171], a[48:51], v[12:15]\n"
  "ds_read_b64_tr_b16 a[216:217], v228\n"
  "ds_read_b128 a[144:147], v251 offset:24576\n"
  "v_mfma_f32_16x16x32_bf16 v[44:47], a[168:171], a[52:55], v[44:47]\n"
  "ds_read_b64_tr_b16 a[218:219], v228 offset:256\n"
  "ds_read_b64_tr_b16 a[220:221], v231\n"
  "v_mfma_f32_16x16x32_bf16 v[76:79], a[168:171], a[56:59], v[76:79]\n"
  "ds_read_b64_tr_b16 a[222:223], v231 offset:256\n"
  "ds_read_b64_tr_b16 a[224:225], v228 offset:4096\n"
  "v_mfma_f32_16x16x32_bf16 v[96:99], a[172:175], a[160:163], v[96:99]\n"
  "ds_read_b128 a[148:151], v251 offset:25600\n"
  "ds_read_b64_tr_b16 a[226:227], v228 offset:4352\n"
  "v_mfma_f32_16x16x32_bf16 v[100:103], a[176:179], a[160:163], v[100:103]\n"
  "ds_read_b64_tr_b16 a[228:229], v231 offset:4096\n"
  "ds_read_b64_tr_b16 a[230:231], v231 offset:4352\n"
  "s_waitcnt lgkmcnt(9)\n"
  "v_mfma_f32_16x16x32_bf16 v[96:99], a[180:183], a[164:167], v[96:99]\n"
  "ds_read_b64_tr_b16 a[232:233], v226 offset:49152\n"
  "ds_read_b64_tr_b16 a[234:235], v226 offset:49408\n"
  "v_mfma_f32_16x16x32_bf16 v[100:103], a[184:187], a[164:167], v[100:103]\n"
  "ds_read_b64_tr_b16 a[236:237], v223 offset:49664\n"
  "ds_read_b64_tr_b16 a[238:239], v223 offset:49920\n"
  "v_mfma_f32_16x16x32_bf16 v[4:7], a[188:191], a[60:63], v[4:7]\n"
  "ds_read_b64_tr_b16 a[240:241], v226 offset:49664\n"
  "ds_read_b64_tr_b16 a[242:243], v226 offset:49920\n"
  "v_mfma_f32_16x16x32_bf16 v[36:39], a[188:191], a[64:67], v[36:39]\n"
  "ds_read_b128 a[152:155], v251 offset:28672\n"
  "v_mfma_f32_16x16x32_bf16 v[68:71], a[188:191], a[68:71], v[68:71]\n"
  "ds_read_b64_tr_b16 a[244:245], v230 offset:16384\n"
  "ds_read_b64_tr_b16 a[246:247], v230 offset:16640\n"
  "v_mfma_f32_16x16x32_bf16 v[8:11], a[208:211], a[60:63], v[8:11]\n"
  "ds_read_b64_tr_b16 a[248:249], v233 offset:16384\n"
  "ds_read_b64_tr_b16 a[250:251], v233 offset:16640\n"
  "v_mfma_f32_16x16x32_bf16 v[40:43], a[208:211], a[64:67], v[40:43]\n"
  "ds_read_b64_tr_b16 a[252:253], v230 offset:20480\n"
  "ds_read_b128 a[156:159], v251 offset:29696\n"
  "v_mfma_f32_16x16x32_bf16 v[72:75], a[208:211], a[68:71], v[72:75]\n"
  "ds_read_b64_tr_b16 a[254:255], v230 offset:20736\n"
  "ds_read_b64_tr_b16 a[168:169], v233 offset:20480\n"
  "v_mfma_f32_16x16x32_bf16 v[12:15], a[212:215], a[60:63], v[12:15]\n"
  "ds_read_b64_tr_b16 a[170:171], v233 offset:20736\n"
  "ds_read_b64_tr_b16 a[172:173], v226 offset:53248\n"
  "v_mfma_f32_16x16x32_bf16 v[44:47], a[212:215], a[64:67], v[44:47]\n"
  "ds_read_b64_tr_b16 a[174:175], v226 offset:53504\n"
  "v_mfma_f32_16x16x32_bf16 v[76:79], a[212:215], a[68:71], v[76:79]\n"
  "s_waitcnt lgkmcnt(8)\n"
  "v_mfma_f32_16x16x32_bf16 v[96:99], a[216:219], a[144:147], v[96:99]\n"
  "ds_read_b64_tr_b16 a[176:177], v223 offset:53760\n"
  "ds_read_b64_tr_b16 a[178:179], v223 offset:54016\n"
  "v_mfma_f32_16x16x32_bf16 v[100:103], a[220:223], a[144:147], v[100:103]\n"
  "v_mfma_f32_16x16x32_bf16 v[96:99], a[224:227], a[148:151], v[96:99]\n"
  "ds_read_b128 a[160:163], v251 offset:22528\n"
  "v_mfma_f32_16x16x32_bf16 v[100:103], a[228:231], a[148:151], v[100:103]\n"
  "ds_read_b64_tr_b16 a[180:181], v226 offset:53760\n"
  "ds_read_b64_tr_b16 a[182:183], v226 offset:54016\n"
  "v_mfma_f32_16x16x32_bf16 v[4:7], a[232:235], a[72:75], v[4:7]\n"
  "ds_read_b128 a[164:167], v251 offset:23552\n"
  "v_mfma_f32_16x16x32_bf16 v[36:39], a[232:235], a[76:79], v[36:39]\n"
  "v_mfma_f32_16x16x32_bf16 v[68:71], a[232:235], a[80:83], v[68:71]\n"
  "ds_read_b64_tr_b16 a[184:185], v229 offset:49152\n"
  "ds_read_b64_tr_b16 a[186:187], v229 offset:49408\n"
  "v_mfma_f32_16x16x32_bf16 v[8:11], a[236:239], a[72:75], v[8:11]\n"
  "ds_read_b64_tr_b16 a[188:189], v232 offset:49152\n"
  "ds_read_b64_tr_b16 a[190:191], v232 offset:49408\n"
  "v_mfma_f32_16x16x32_bf16 v[40:43], a[236:239], a[76:79], v[40:43]\n"
  "ds_read_b64_tr_b16 a[208:209], v229 offset:53248\n"
  "ds_read_b64_tr_b16 a[210:211], v229 offset:53504\n"
  "v_mfma_f32_16x16x32_bf16 v[72:75], a[236:239], a[80:83], v[72:75]\n"
  "ds_read_b64_tr_b16 a[212:213], v232 offset:53248\n"
  "ds_read_b64_tr_b16 a[214:215], v232 offset:53504\n"
  "v_mfma_f32_16x16x32_bf16 v[12:15], a[240:243], a[72:75], v[12:15]\n"
  "ds_read_b64_tr_b16 a[216:217], v225 offset:49152\n"
  "ds_read_b64_tr_b16 a[218:219], v225 offset:49408\n"
  "v_mfma_f32_16x16x32_bf16 v[44:47], a[240:243], a[76:79], v[44:47]\n"
  "v_mfma_f32_16x16x32_bf16 v[76:79], a[240:243], a[80:83], v[76:79]\n"
  "v_mfma_f32_16x16x32_bf16 v[96:99], a[244:247], a[152:155], v[96:99]\n"
  "ds_read_b64_tr_b16 a[220:221], v222 offset:49664\n"
  "ds_read_b64_tr_b16 a[222:223], v222 offset:49920\n"
  "s_waitcnt lgkmcnt(8)\n"
  "v_mfma_f32_16x16x32_bf16 v[100:103], a[248:251], a[152:155], v[100:103]\n"
  "v_mfma_f32_16x16x32_bf16 v[96:99], a[252:255], a[156:159], v[96:99]\n"
  "ds_read_b128 a[144:147], v251 offset:26624\n"
  "v_mfma_f32_16x16x32_bf16 v[100:103], a[168:171], a[156:159], v[100:103]\n"
  "ds_read_b64_tr_b16 a[224:225], v225 offset:49664\n"
  "ds_read_b64_tr_b16 a[226:227], v225 offset:49920\n"
  "v_mfma_f32_16x16x32_bf16 v[4:7], a[172:175], a[84:87], v[4:7]\n"
  "ds_read_b128 a[148:151], v251 offset:27648\n"
  "v_mfma_f32_16x16x32_bf16 v[36:39], a[172:175], a[88:91], v[36:39]\n"
  "v_mfma_f32_16x16x32_bf16 v[68:71], a[172:175], a[92:95], v[68:71]\n"
  "ds_read_b64_tr_b16 a[228:229], v228 offset:49152\n"
  "ds_read_b64_tr_b16 a[230:231], v228 offset:49408\n"
  "v_mfma_f32_16x16x32_bf16 v[8:11], a[176:179], a[84:87], v[8:11]\n"
  "ds_read_b64_tr_b16 a[232:233], v231 offset:49152\n"
  "ds_read_b64_tr_b16 a[234:235], v231 offset:49408\n"
  "v_mfma_f32_16x16x32_bf16 v[40:43], a[176:179], a[88:91], v[40:43]\n"
  "ds_read_b64_tr_b16 a[236:237], v228 offset:53248\n"
  "ds_read_b64_tr_b16 a[238:239], v228 offset:53504\n"
  "v_mfma_f32_16x16x32_bf16 v[72:75], a[176:179], a[92:95], v[72:75]\n"
  "ds_read_b64_tr_b16 a[240:241], v231 offset:53248\n"
  "ds_read_b64_tr_b16 a[242:243], v231 offset:53504\n"
  "v_mfma_f32_16x16x32_bf16 v[12:15], a[180:183], a[84:87], v[12:15]\n"
  "ds_read_b64_tr_b16 a[244:245], v225 offset:53248\n"
  "ds_read_b64_tr_b16 a[246:247], v225 offset:53504\n"
  "v_mfma_f32_16x16x32_bf16 v[44:47], a[180:183], a[88:91], v[44:47]\n"
  "v_mfma_f32_16x16x32_bf16 v[76:79], a[180:183], a[92:95], v[76:79]\n"
  "v_mfma_f32_16x16x32_bf16 v[96:99], a[184:187], a[160:163], v[96:99]\n"
  "ds_read_b64_tr_b16 a[248:249], v222 offset:53760\n"
  "ds_read_b64_tr_b16 a[250:251], v222 offset:54016\n"
  "v_mfma_f32_16x16x32_bf16 v[100:103], a[188:191], a[160:163], v[100:103]\n"
  "s_waitcnt lgkmcnt(7)\n"
  "v_mfma_f32_16x16x32_bf16 v[96:99], a[208:211], a[164:167], v[96:99]\n"
  "ds_read_b128 a[152:155], v251 offset:30720\n"
  "v_mfma_f32_16x16x32_bf16 v[100:103], a[212:215], a[164:167], v[100:103]\n"
  "ds_read_b64_tr_b16 a[252:253], v225 offset:53760\n"
  "ds_read_b64_tr_b16 a[254:255], v225 offset:54016\n"
  "v_mfma_f32_16x16x32_bf16 v[4:7], a[216:219], a[96:99], v[4:7]\n"
  "ds_read_b128 a[156:159], v251 offset:31744\n"
  "v_mfma_f32_16x16x32_bf16 v[36:39], a[216:219], a[100:103], v[36:39]\n"
  "v_mfma_f32_16x16x32_bf16 v[68:71], a[216:219], a[104:107], v[68:71]\n"
  "ds_read_b64_tr_b16 a[168:169], v228 offset:32768\n"
  "ds_read_b64_tr_b16 a[170:171], v228 offset:33024\n"
  "v_mfma_f32_16x16x32_bf16 v[8:11], a[220:223], a[96:99], v[8:11]\n"
  "ds_read_b64_tr_b16 a[172:173], v231 offset:32768\n"
  "ds_read_b64_tr_b16 a[174:175], v231 offset:33024\n"
  "v_mfma_f32_16x16x32_bf16 v[40:43], a[220:223], a[100:103], v[40:43]\n"
  "ds_read_b64_tr_b16 a[176:177], v228 offset:36864\n"
  "ds_read_b64_tr_b16 a[178:179], v228 offset:37120\n"
  "v_mfma_f32_16x16x32_bf16 v[72:75], a[220:223], a[104:107], v[72:75]\n"
  "ds_read_b64_tr_b16 a[180:181], v231 offset:36864\n"
  "ds_read_b64_tr_b16 a[182:183], v231 offset:37120\n"
  "v_mfma_f32_16x16x32_bf16 v[12:15], a[224:227], a[96:99], v[12:15]\n"
  "ds_read_b64_tr_b16 a[184:185], v225 offset:32768\n"
  "ds_read_b64_tr_b16 a[186:187], v225 offset:33024\n"
  "v_mfma_f32_16x16x32_bf16 v[44:47], a[224:227], a[100:103], v[44:47]\n"
  "v_mfma_f32_16x16x32_bf16 v[76:79], a[224:227], a[104:107], v[76:79]\n"
  "v_mfma_f32_16x16x32_bf16 v[96:99], a[228:231], a[144:147], v[96:99]\n"
  "ds_read_b64_tr_b16 a[188:189], v222 offset:33280\n"
  "ds_read_b64_tr_b16 a[190:191], v222 offset:33536\n"
  "v_mfma_f32_16x16x32_bf16 v[100:103], a[232:235], a[144:147], v[100:103]\n"
  "s_waitcnt lgkmcnt(7)\n"
  "v_mfma_f32_16x16x32_bf16 v[96:99], a[236:239], a[148:151], v[96:99]\n"
  "s_waitcnt vmcnt(0)\n"
  "s_barrier\n"
  "s_cmp_eq_u32 s19, 3\n"
  "s_cbranch_scc0 L_o17\n"
  "v_cmp_ne_u32 s[92:93], v252, v188\n"
  "s_nop 4\n"
  "s_and_b32 s92, s92, s70\n"
  "s_cbranch_scc0 L_fix0\n"
  "v_mov_b32 v252, v188\n"
  "buffer_store_dword v252, v186, s[84:87], s99 offen sc1\n"
  "L_fix0:\n"
  "buffer_store_dword v252, v187, s[84:87], s99 offen sc1\n"
  "L_o17:\n"
  "v_mfma_f32_16x16x32_bf16 v[100:103], a[240:243], a[148:151], v[100:103]\n"
  "ds_read_b64_tr_b16 a[208:209], v225 offset:33280\n"
  "ds_read_b64_tr_b16 a[210:211], v225 offset:33536\n"
  "v_mfma_f32_16x16x32_bf16 v[4:7], a[244:247], a[108:111], v[4:7]\n"
  "v_mfma_f32_16x16x32_bf16 v[36:39], a[244:247], a[112:115], v[36:39]\n"
  "v_mfma_f32_16x16x32_bf16 v[68:71], a[244:247], a[116:119], v[68:71]\n"
  "ds_read_b64_tr_b16 a[212:213], v225 offset:36864\n"
  "ds_read_b64_tr_b16 a[214:215], v225 offset:37120\n"
  "v_mfma_f32_16x16x32_bf16 v[8:11], a[248:251], a[108:111], v[8:11]\n"
  "v_mfma_f32_16x16x32_bf16 v[40:43], a[248:251], a[112:115], v[40:43]\n"
  "v_mfma_f32_16x16x32_bf16 v[72:75], a[248:251], a[116:119], v[72:75]\n"
  "ds_read_b64_tr_b16 a[216:217], v222 offset:37376\n"
  "ds_read_b64_tr_b16 a[218:219], v222 offset:37632\n"
  "v_mfma_f32_16x16x32_bf16 v[12:15], a[252:255], a[108:111], v[12:15]\n"
  "v_mfma_f32_16x16x32_bf16 v[44:47], a[252:255], a[112:115], v[44:47]\n"
  "s_lshl_b32 s90, s57, 7\n"
  "s_add_u32 s90, s90, s99\n"
  "buffer_load_dword v184, v185, s[84:87], s90 offen sc1\n"
  "v_mfma_f32_16x16x32_bf16 v[76:79], a[252:255], a[116:119], v[76:79]\n"
  "ds_read_b64_tr_b16 a[220:221], v225 offset:37376\n"
  "ds_read_b64_tr_b16 a[222:223], v225 offset:37632\n"
  "v_mfma_f32_16x16x32_bf16 v[96:99], a[168:171], a[152:155], v[96:99]\n"
  "v_mfma_f32_16x16x32_bf16 v[100:103], a[172:175], a[152:155], v[100:103]\n"
  "s_waitcnt lgkmcnt(4)\n"
  "v_mfma_f32_16x16x32_bf16 v[96:99], a[176:179], a[156:159], v[96:99]\n"
  "ds_read_b64_tr_b16 a[224:225], v224\n"
  "ds_read_b64_tr_b16 a[226:227], v224 offset:256\n"
  "v_mfma_f32_16x16x32_bf16 v[100:103], a[180:183], a[156:159], v[100:103]\n"
  "v_mfma_f32_16x16x32_bf16 v[4:7], a[184:187], a[120:123], v[4:7]\n"
  "v_mfma_f32_16x16x32_bf16 v[36:39], a[184:187], a[124:127], v[36:39]\n"
  "s_lshl_b32 s61, s19, 1\n"
  "s_or_b32 s61, s61, 0\n"
  "v_mfma_f32_16x16x32_bf16 v[68:71], a[184:187], a[128:131], v[68:71]\n"
  "s_xor_b32 s61, s61, s64\n"
  "s_mul_i32 s61, s61, 0xd000\n"
  "v_mfma_f32_16x16x32_bf16 v[8:11], a[188:191], a[120:123], v[8:11]\n"
  "v_cvt_pk_bf16_f32 v96, v96, v97\n"
  "v_cvt_pk_bf16_f32 v97, v98, v99\n"
  "v_mfma_f32_16x16x32_bf16 v[40:43], a[188:191], a[124:127], v[40:43]\n"
  "buffer_store_dwordx2 v[96:97], v192, s[36:39], s61 offen sc1\n"
  "s_lshl_b32 s62, s19, 1\n"
  "v_mfma_f32_16x16x32_bf16 v[72:75], a[188:191], a[128:131], v[72:75]\n"
  "s_or_b32 s62, s62, 1\n"
  "s_xor_b32 s62, s62, s64\n"
  "v_mfma_f32_16x16x32_bf16 v[12:15], a[208:211], a[120:123], v[12:15]\n"
  "s_mul_i32 s62, s62, 0xd000\n"
  "v_cvt_pk_bf16_f32 v100, v100, v101\n"
  "v_mfma_f32_16x16x32_bf16 v[44:47], a[208:211], a[124:127], v[44:47]\n"
  "v_cvt_pk_bf16_f32 v101, v102, v103\n"
  "buffer_store_dwordx2 v[100:101], v192, s[36:39], s62 offen sc1\n"
  "v_mfma_f32_16x16x32_bf16 v[76:79], a[208:211], a[128:131], v[76:79]\n"
  "ds_read_b64_tr_b16 a[228:229], v224 offset:4096\n"
  "ds_read_b64_tr_b16 a[230:231], v224 offset:4352\n"
  "v_mfma_f32_16x16x32_bf16 v[4:7], a[212:215], a[132:135], v[4:7]\n"
  "ds_read_b64_tr_b16 a[232:233], v222\n"
  "ds_read_b64_tr_b16 a[234:235], v222 offset:256\n"
  "v_mfma_f32_16x16x32_bf16 v[36:39], a[212:215], a[136:139], v[36:39]\n"
  "ds_read_b64_tr_b16 a[236:237], v222 offset:4096\n"
  "ds_read_b64_tr_b16 a[238:239], v222 offset:4352\n"
  "v_mfma_f32_16x16x32_bf16 v[68:71], a[212:215], a[140:143], v[68:71]\n"
  "ds_read_b64_tr_b16 a[240:241], v224 offset:16384\n"
  "ds_read_b64_tr_b16 a[242:243], v224 offset:16640\n"
  "s_waitcnt lgkmcnt(8)\n"
  "v_mfma_f32_16x16x32_bf16 v[8:11], a[216:219], a[132:135], v[8:11]\n"
  "v_mfma_f32_16x16x32_bf16 v[40:43], a[216:219], a[136:139], v[40:43]\n"
  "ds_read_b64_tr_b16 a[244:245], v224 offset:20480\n"
  "ds_read_b64_tr_b16 a[246:247], v224 offset:20736\n"
  "v_mfma_f32_16x16x32_bf16 v[72:75], a[216:219], a[140:143], v[72:75]\n"
  "s_xor_b32 s62, s64, 1\n"
  "s_mul_i32 s62, s62, 0xd000\n"
  "v_cvt_pk_bf16_f32 v4, v4, v5\n"
  "v_mfma_f32_16x16x32_bf16 v[12:15], a[220:223], a[132:135], v[12:15]\n"
  "v_cvt_pk_bf16_f32 v5, v6, v7\n"
  "v_cvt_pk_bf16_f32 v6, v36, v37\n"
  "v_cvt_pk_bf16_f32 v7, v38, v39\n"
  "v_mfma_f32_16x16x32_bf16 v[44:47], a[220:223], a[136:139], v[44:47]\n"
  "buffer_store_dwordx4 v[4:7], v190, s[36:39], s62 offen sc1\n"
  "v_cvt_pk_bf16_f32 v68, v68, v69\n"
  "v_cvt_pk_bf16_f32 v69, v70, v71\n"
  "v_mfma_f32_16x16x32_bf16 v[76:79], a[220:223], a[140:143], v[76:79]\n"
  "buffer_store_dwordx2 v[68:69], v191, s[36:39], s62 offen offset:1024 sc1\n"
  "s_xor_b32 s61, s64, 2\n"
  "s_mul_i32 s61, s61, 0xd000\n"
  "v_mfma_f32_16x16x32_bf16 v[0:3], a[224:227], a[0:3], v[0:3]\n"
  "v_cvt_pk_bf16_f32 v8, v8, v9\n"
  "v_cvt_pk_bf16_f32 v9, v10, v11\n"
  "v_cvt_pk_bf16_f32 v10, v40, v41\n"
  "v_mfma_f32_16x16x32_bf16 v[32:35], a[224:227], a[4:7], v[32:35]\n"
  "v_cvt_pk_bf16_f32 v11, v42, v43\n"
  "buffer_store_dwordx4 v[8:11], v190, s[36:39], s61 offen sc1\n"
  "v_cvt_pk_bf16_f32 v72, v72, v73\n"
  "v_mfma_f32_16x16x32_bf16 v[64:67], a[224:227], a[8:11], v[64:67]\n"
  "v_cvt_pk_bf16_f32 v73, v74, v75\n"
  "buffer_store_dwordx2 v[72:73], v191, s[36:39], s61 offen offset:1024 sc1\n"
  "s_xor_b32 s62, s64, 3\n"
  "s_waitcnt lgkmcnt(0)\n"
  "v_mfma_f32_16x16x32_bf16 v[0:3], a[228:231], a[12:15], v[0:3]\n"
  "s_mul_i32 s62, s62, 0xd000\n"
  "v_cvt_pk_bf16_f32 v12, v12, v13\n"
  "v_mfma_f32_16x16x32_bf16 v[32:35], a[228:231], a[16:19], v[32:35]\n"
  "v_cvt_pk_bf16_f32 v13, v14, v15\n"
  "v_cvt_pk_bf16_f32 v14, v44, v45\n"
  "v_mfma_f32_16x16x32_bf16 v[64:67], a[228:231], a[20:23], v[64:67]\n"
  "v_cvt_pk_bf16_f32 v15, v46, v47\n"
  "buffer_store_dwordx4 v[12:15], v190, s[36:39], s62 offen sc1\n"
  "v_mfma_f32_16x16x32_bf16 v[0:3], a[232:235], a[24:27], v[0:3]\n"
  "v_cvt_pk_bf16_f32 v76, v76, v77\n"
  "v_cvt_pk_bf16_f32 v77, v78, v79\n"
  "v_mfma_f32_16x16x32_bf16 v[32:35], a[232:235], a[28:31], v[32:35]\n"
  "buffer_store_dwordx2 v[76:77], v191, s[36:39], s62 offen offset:1024 sc1\n"
  "ds_read_b64_tr_b16 a[248:249], v223 offset:49152\n"
  "v_mfma_f32_16x16x32_bf16 v[64:67], a[232:235], a[32:35], v[64:67]\n"
  "ds_read_b64_tr_b16 a[250:251], v223 offset:49408\n"
  "ds_read_b64_tr_b16 a[252:253], v223 offset:53248\n"
  "v_mfma_f32_16x16x32_bf16 v[0:3], a[236:239], a[36:39], v[0:3]\n"
  "ds_read_b64_tr_b16 a[254:255], v223 offset:53504\n"
  "ds_read_b64_tr_b16 a[168:169], v222 offset:49152\n"
  "v_mfma_f32_16x16x32_bf16 v[32:35], a[236:239], a[40:43], v[32:35]\n"
  "ds_read_b64_tr_b16 a[170:171], v222 offset:49408\n"
  "ds_read_b64_tr_b16 a[172:173], v222 offset:53248\n"
  "v_mfma_f32_16x16x32_bf16 v[64:67], a[236:239], a[44:47], v[64:67]\n"
  "ds_read_b64_tr_b16 a[174:175], v222 offset:53504\n"
  "ds_read_b64_tr_b16 a[176:177], v222 offset:32768\n"
  "v_mfma_f32_16x16x32_bf16 v[0:3], a[240:243], a[48:51], v[0:3]\n"
  "ds_read_b64_tr_b16 a[178:179], v222 offset:33024\n"
  "v_mfma_f32_16x16x32_bf16 v[32:35], a[240:243], a[52:55], v[32:35]\n"
  "ds_read_b64_tr_b16 a[180:181], v222 offset:36864\n"
  "ds_read_b64_tr_b16 a[182:183], v222 offset:37120\n"
  "v_mfma_f32_16x16x32_bf16 v[64:67], a[240:243], a[56:59], v[64:67]\n"
  "v_mfma_f32_16x16x32_bf16 v[0:3], a[244:247], a[60:63], v[0:3]\n"
  "v_mfma_f32_16x16x32_bf16 v[32:35], a[244:247], a[64:67], v[32:35]\n"
  "v_mfma_f32_16x16x32_bf16 v[64:67], a[244:247], a[68:71], v[64:67]\n"
  "s_waitcnt lgkmcnt(6)\n"
  "v_mfma_f32_16x16x32_bf16 v[0:3], a[248:251], a[72:75], v[0:3]\n"
  "v_mfma_f32_16x16x32_bf16 v[32:35], a[248:251], a[76:79], v[32:35]\n"
  "v_mfma_f32_16x16x32_bf16 v[64:67], a[248:251], a[80:83], v[64:67]\n"
  "v_mfma_f32_16x16x32_bf16 v[0:3], a[252:255], a[84:87], v[0:3]\n"
  "v_mfma_f32_16x16x32_bf16 v[32:35], a[252:255], a[88:91], v[32:35]\n"
  "v_mfma_f32_16x16x32_bf16 v[64:67], a[252:255], a[92:95], v[64:67]\n"
  "v_mfma_f32_16x16x32_bf16 v[0:3], a[168:171], a[96:99], v[0:3]\n"
  "v_mfma_f32_16x16x32_bf16 v[32:35], a[168:171], a[100:103], v[32:35]\n"
  "v_mfma_f32_16x16x32_bf16 v[64:67], a[168:171], a[104:107], v[64:67]\n"
  "s_waitcnt lgkmcnt(0)\n"
  "v_mfma_f32_16x16x32_bf16 v[0:3], a[172:175], a[108:111], v[0:3]\n"
  "v_mfma_f32_16x16x32_bf16 v[32:35], a[172:175], a[112:115], v[32:35]\n"
  "v_mfma_f32_16x16x32_bf16 v[64:67], a[172:175], a[116:119], v[64:67]\n"
  "v_mfma_f32_16x16x32_bf16 v[0:3], a[176:179], a[120:123], v[0:3]\n"
  "v_mfma_f32_16x16x32_bf16 v[32:35], a[176:179], a[124:127], v[32:35]\n"
  "v_mfma_f32_16x16x32_bf16 v[64:67], a[176:179], a[128:131], v[64:67]\n"
  "s_waitcnt vmcnt(8)\n"
  "v_subrev_u32 v184, s89, v184\n"
  "buffer_load_dword v188, v185, s[84:87], s98 offen sc1\n"
  "v_cmp_lt_u32 s[92:93], 1, v184\n"
  "s_nop 3\n"
  "s_and_b32 s25, s92, s71\n"
  "v_mfma_f32_16x16x32_bf16 v[0:3], a[180:183], a[132:135], v[0:3]\n"
  "v_mfma_f32_16x16x32_bf16 v[32:35], a[180:183], a[136:139], v[32:35]\n"
  "v_mfma_f32_16x16x32_bf16 v[64:67], a[180:183], a[140:143], v[64:67]\n"
  "s_mov_b32 s60, 0xffff0000\n"
  "s_cmp_eq_u32 s19, 3\n"
  "s_cbranch_scc0 L_o20\n"
  "s_cmp_eq_u32 s25, 0\n"
  "s_cbranch_scc1 L_pre\n"
  "s_lshl_b32 s90, s57, 7\n"
  "s_add_u32 s90, s90, s99\n"
  "s_mov_b32 s91, 0\n"
  "L_poll10:\n"
  "buffer_load_dword v184, v185, s[84:87], s90 offen sc1\n"
  "s_waitcnt vmcnt(0)\n"
  "v_subrev_u32 v184, s89, v184\n"
  "v_cmp_lt_u32 s[92:93], 1, v184\n"
  "s_add_u32 s91, s91, 1\n"
  "s_nop 3\n"
  "s_and_b32 s92, s92, s71\n"
  "s_cbranch_scc0 L_polled10\n"
  "s_and_b32 s92, s91, 7\n"
  "s_cbranch_scc1 L_poll10\n"
  "s_waitcnt vmcnt(0)\n"
  "buffer_load_dword v188, v185, s[84:87], s98 offen sc1\n"
  "s_waitcnt vmcnt(0)\n"
  "v_cmp_ne_u32 s[92:93], v252, v188\n"
  "s_nop 4\n"
  "s_and_b32 s92, s92, s71\n"
  "s_cbranch_scc0 L_same10\n"
  "v_mov_b32 v252, v188\n"
  "buffer_store_dword v252, v187, s[84:87], s99 offen sc1\n"
  "L_same10:\n"
  "s_sleep 2\n"
  "s_cmp_lt_u32 s91, 0x9c40\n"
  "s_cbranch_scc1 L_poll10\n"
  "L_polled10:\n"
  "buffer_load_dword v188, v185, s[84:87], s98 offen sc1\n"
  "L_pre:\n"
  "L_o20:\n"
  "s_waitcnt vmcnt(0)\n"
  "v_add_u32 v189, 1, v188\n"
  "s_barrier\n"
  "s_cmp_eq_u32 s19, 3\n"
  "s_cbranch_scc0 L_o21\n"
  "buffer_store_dword v189, v187, s[84:87], s99 offen sc1\n"
  "L_o21:\n"
  "s_xor_b32 s58, s64, 4\n"
  "s_mul_i32 s58, s58, 0x1a00\n"
  "buffer_load_dwordx4 v[152:155], v190, s[40:43], s58 offen sc1\n"
  "buffer_load_dwordx2 v[156:157], v191, s[40:43], s58 offen offset:1024 sc1\n"
  "s_xor_b32 s59, s64, 5\n"
  "s_mul_i32 s59, s59, 0x1a00\n"
  "L_st8:\n"
  "s_cbranch_execz L_stx\n"
  "buffer_load_dwordx4 v[158:161], v190, s[40:43], s59 offen sc1\n"
  "buffer_load_dwordx2 v[162:163], v191, s[40:43], s59 offen offset:1024 sc1\n"
  "s_xor_b32 s58, s64, 6\n"
  "s_mul_i32 s58, s58, 0x1a00\n"
  "buffer_load_dwordx4 v[164:167], v190, s[40:43], s58 offen sc1\n"
  "buffer_load_dwordx2 v[168:169], v191, s[40:43], s58 offen offset:1024 sc1\n"
  "s_xor_b32 s59, s64, 7\n"
  "s_mul_i32 s59, s59, 0x1a00\n"
  "buffer_load_dwordx4 v[170:173], v190, s[40:43], s59 offen sc1\n"
  "buffer_load_dwordx2 v[174:175], v191, s[40:43], s59 offen offset:1024 sc1\n"
  "s_cmp_eq_u32 s19, 3\n"
  "s_cbranch_scc0 L_o22\n"
  "s_lshl_b32 s90, s57, 7\n"
  "s_add_u32 s90, s90, s99\n"
  "s_mov_b32 s91, 0\n"
  "L_poll11:\n"
  "buffer_load_dword v184, v185, s[84:87], s90 offen sc1\n"
  "s_waitcnt vmcnt(0)\n"
  "v_subrev_u32 v184, s89, v184\n"
  "v_cmp_ne_u32 s[92:93], 1, v184\n"
  "s_add_u32 s91, s91, 1\n"
  "s_nop 3\n"
  "s_and_b32 s92, s92, s71\n"
  "s_cbranch_scc0 L_polled11\n"
  "s_and_b32 s92, s91, 7\n"
  "s_cbranch_scc1 L_poll11\n"
  "s_sleep 2\n"
  "s_cmp_lt_u32 s91, 0x9c40\n"
  "s_cbranch_scc1 L_poll11\n"
  "L_polled11:\n"
  "L_o22:\n"
  "s_waitcnt vmcnt(0)\n"
  "s_cmp_lt_u32 s19, 3\n"
  "s_cbranch_scc0 L_o23\n"
  "v_lshlrev_b32 v104, 16, v152\n"
  "v_and_b32 v105, s60, v152\n"
  "v_lshlrev_b32 v106, 16, v153\n"
  "v_and_b32 v107, s60, v153\n"
  "v_pk_add_f32 v[0:1], v[0:1], v[104:105]\n"
  "v_pk_add_f32 v[2:3], v[2:3], v[106:107]\n"
  "v_lshlrev_b32 v108, 16, v154\n"
  "v_and_b32 v109, s60, v154\n"
  "v_lshlrev_b32 v110, 16, v155\n"
  "v_and_b32 v111, s60, v155\n"
  "v_pk_add_f32 v[32:33], v[32:33], v[108:109]\n"
  "v_pk_add_f32 v[34:35], v[34:35], v[110:111]\n"
  "v_lshlrev_b32 v112, 16, v156\n"
  "v_and_b32 v113, s60, v156\n"
  "v_lshlrev_b32 v114, 16, v157\n"
  "v_and_b32 v115, s60, v157\n"
  "v_pk_add_f32 v[64:65], v[64:65], v[112:113]\n"
  "v_pk_add_f32 v[66:67], v[66:67], v[114:115]\n"
  "v_lshlrev_b32 v104, 16, v158\n"
  "v_and_b32 v105, s60, v158\n"
  "v_lshlrev_b32 v106, 16, v159\n"
  "v_and_b32 v107, s60, v159\n"
  "v_pk_add_f32 v[0:1], v[0:1], v[104:105]\n"
  "v_pk_add_f32 v[2:3], v[2:3], v[106:107]\n"
  "v_lshlrev_b32 v108, 16, v160\n"
  "v_and_b32 v109, s60, v160\n"
  "v_lshlrev_b32 v110, 16, v161\n"
  "v_and_b32 v111, s60, v161\n"
  "v_pk_add_f32 v[32:33], v[32:33], v[108:109]\n"
  "v_pk_add_f32 v[34:35], v[34:35], v[110:111]\n"
  "v_lshlrev_b32 v112, 16, v162\n"
  "v_and_b32 v113, s60, v162\n"
  "v_lshlrev_b32 v114, 16, v163\n"
  "v_and_b32 v115, s60, v163\n"
  "v_pk_add_f32 v[64:65], v[64:65], v[112:113]\n"
  "v_pk_add_f32 v[66:67], v[66:67], v[114:115]\n"
  "v_lshlrev_b32 v104, 16, v164\n"
  "v_and_b32 v105, s60, v164\n"
  "v_lshlrev_b32 v106, 16, v165\n"
  "v_and_b32 v107, s60, v165\n"
  "v_pk_add_f32 v[0:1], v[0:1], v[104:105]\n"
  "v_pk_add_f32 v[2:3], v[2:3], v[106:107]\n"
  "v_lshlrev_b32 v108, 16, v166\n"
  "v_and_b32 v109, s60, v166\n"
  "v_lshlrev_b32 v110, 16, v167\n"
  "v_and_b32 v111, s60, v167\n"
  "v_pk_add_f32 v[32:33], v[32:33], v[108:109]\n"
  "v_pk_add_f32 v[34:35], v[34:35], v[110:111]\n"
  "v_lshlrev_b32 v112, 16, v168\n"
  "v_and_b32 v113, s60, v168\n"
  "v_lshlrev_b32 v114, 16, v169\n"
  "v_and_b32 v115, s60, v169\n"
  "v_pk_add_f32 v[64:65], v[64:65], v[112:113]\n"
  "v_pk_add_f32 v[66:67], v[66:67], v[114:115]\n"
  "v_lshlrev_b32 v104, 16, v170\n"
  "v_and_b32 v105, s60, v170\n"
  "v_lshlrev_b32 v106, 16, v171\n"
  "v_and_b32 v107, s60, v171\n"
  "v_pk_add_f32 v[0:1], v[0:1], v[104:105]\n"
  "v_pk_add_f32 v[2:3], v[2:3], v[106:107]\n"
  "v_lshlrev_b32 v108, 16, v172\n"
  "v_and_b32 v109, s60, v172\n"
  "v_lshlrev_b32 v110, 16, v173\n"
  "v_and_b32 v111, s60, v173\n"
  "v_pk_add_f32 v[32:33], v[32:33], v[108:109]\n"
  "v_pk_add_f32 v[34:35], v[34:35], v[110:111]\n"
  "v_lshlrev_b32 v112, 16, v174\n"
  "v_and_b32 v113, s60, v174\n"
  "v_lshlrev_b32 v114, 16, v175\n"
  "v_and_b32 v115, s60, v175\n"
  "v_pk_add_f32 v[64:65], v[64:65], v[112:113]\n"
  "v_pk_add_f32 v[66:67], v[66:67], v[114:115]\n"
  "L_o23:\n"
  "s_barrier\n"
  "s_cmp_eq_u32 s19, 0\n"
  "s_cbranch_scc0 L_o18\n"
  "s_mov_b32 s58, 0\n"
  "buffer_load_dwordx2 v[200:201], v192, s[40:43], s58 offen sc1\n"
  "s_mov_b32 s59, 0x1a00\n"
  "buffer_load_dwordx2 v[202:203], v192, s[40:43], s59 offen sc1\n"
  "s_mov_b32 s58, 0x3400\n"
  "buffer_load_dwordx2 v[204:205], v192, s[40:43], s58 offen sc1\n"
  "s_mov_b32 s59, 0x4e00\n"
  "buffer_load_dwordx2 v[206:207], v192, s[40:43], s59 offen sc1\n"
  "s_mov_b32 s58, 0x6800\n"
  "buffer_load_dwordx2 v[208:209], v192, s[40:43], s58 offen sc1\n"
  "s_mov_b32 s59, 0x8200\n"
  "buffer_load_dwordx2 v[210:211], v192, s[40:43], s59 offen sc1\n"
  "s_mov_b32 s58, 0x9c00\n"
  "buffer_load_dwordx2 v[212:213], v192, s[40:43], s58 offen sc1\n"
  "s_mov_b32 s59, 0xb600\n"
  "buffer_load_dwordx2 v[214:215], v192, s[40:43], s59 offen sc1\n"
  "L_o18:\n"
  "s_xor_b32 s59, s64, 1\n"
  "s_mul_i32 s59, s59, 0x1a00\n"
  "buffer_load_dwordx4 v[134:137], v190, s[40:43], s59 offen sc1\n"
  "buffer_load_dwordx2 v[138:139], v191, s[40:43], s59 offen offset:1024 sc1\n"
  "s_xor_b32 s58, s64, 2\n"
  "s_mul_i32 s58, s58, 0x1a00\n"
  "buffer_load_dwordx4 v[140:143], v190, s[40:43], s58 offen sc1\n"
  "buffer_load_dwordx2 v[144:145], v191, s[40:43], s58 offen offset:1024 sc1\n"
  "s_xor_b32 s59, s64, 3\n"
  "s_mul_i32 s59, s59, 0x1a00\n"
  "buffer_load_dwordx4 v[146:149], v190, s[40:43], s59 offen sc1\n"
  "buffer_load_dwordx2 v[150:151], v191, s[40:43], s59 offen offset:1024 sc1\n"
  "s_cmp_eq_u32 s19, 3\n"
  "s_cbranch_scc0 L_o24\n"
  "v_lshlrev_b32 v104, 16, v152\n"
  "v_and_b32 v105, s60, v152\n"
  "v_lshlrev_b32 v106, 16, v153\n"
  "v_and_b32 v107, s60, v153\n"
  "v_pk_add_f32 v[0:1], v[0:1], v[104:105]\n"
  "v_pk_add_f32 v[2:3], v[2:3], v[106:107]\n"
  "v_lshlrev_b32 v108, 16, v154\n"
  "v_and_b32 v109, s60, v154\n"
  "v_lshlrev_b32 v110, 16, v155\n"
  "v_and_b32 v111, s60, v155\n"
  "v_pk_add_f32 v[32:33], v[32:33], v[108:109]\n"
  "v_pk_add_f32 v[34:35], v[34:35], v[110:111]\n"
  "v_lshlrev_b32 v112, 16, v156\n"
  "v_and_b32 v113, s60, v156\n"
  "v_lshlrev_b32 v114, 16, v157\n"
  "v_and_b32 v115, s60, v157\n"
  "v_pk_add_f32 v[64:65], v[64:65], v[112:113]\n"
  "v_pk_add_f32 v[66:67], v[66:67], v[114:115]\n"
  "v_lshlrev_b32 v104, 16, v158\n"
  "v_and_b32 v105, s60, v158\n"
  "v_lshlrev_b32 v106, 16, v159\n"
  "v_and_b32 v107, s60, v159\n"
  "v_pk_add_f32 v[0:1], v[0:1], v[104:105]\n"
  "v_pk_add_f32 v[2:3], v[2:3], v[106:107]\n"
  "v_lshlrev_b32 v108, 16, v160\n"
  "v_and_b32 v109, s60, v160\n"
  "v_lshlrev_b32 v110, 16, v161\n"
  "v_and_b32 v111, s60, v161\n"
  "v_pk_add_f32 v[32:33], v[32:33], v[108:109]\n"
  "v_pk_add_f32 v[34:35], v[34:35], v[110:111]\n"
  "v_lshlrev_b32 v112, 16, v162\n"
  "v_and_b32 v113, s60, v162\n"
  "v_lshlrev_b32 v114, 16, v163\n"
  "v_and_b32 v115, s60, v163\n"
  "v_pk_add_f32 v[64:65], v[64:65], v[112:113]\n"
  "v_pk_add_f32 v[66:67], v[66:67], v[114:115]\n"
  "v_lshlrev_b32 v104, 16, v164\n"
  "v_and_b32 v105, s60, v164\n"
  "v_lshlrev_b32 v106, 16, v165\n"
  "v_and_b32 v107, s60, v165\n"
  "v_pk_add_f32 v[0:1], v[0:1], v[104:105]\n"
  "v_pk_add_f32 v[2:3], v[2:3], v[106:107]\n"
  "v_lshlrev_b32 v108, 16, v166\n"
  "v_and_b32 v109, s60, v166\n"
  "v_lshlrev_b32 v110, 16, v167\n"
  "v_and_b32 v111, s60, v167\n"
  "v_pk_add_f32 v[32:33], v[32:33], v[108:109]\n"
  "v_pk_add_f32 v[34:35], v[34:35], v[110:111]\n"
  "v_lshlrev_b32 v112, 16, v168\n"
  "v_and_b32 v113, s60, v168\n"
  "v_lshlrev_b32 v114, 16, v169\n"
  "v_and_b32 v115, s60, v169\n"
  "v_pk_add_f32 v[64:65], v[64:65], v[112:113]\n"
  "v_pk_add_f32 v[66:67], v[66:67], v[114:115]\n"
  "v_lshlrev_b32 v104, 16, v170\n"
  "v_and_b32 v105, s60, v170\n"
  "v_lshlrev_b32 v106, 16, v171\n"
  "v_and_b32 v107, s60, v171\n"
  "v_pk_add_f32 v[0:1], v[0:1], v[104:105]\n"
  "v_pk_add_f32 v[2:3], v[2:3], v[106:107]\n"
  "v_lshlrev_b32 v108, 16, v172\n"
  "v_and_b32 v109, s60, v172\n"
  "v_lshlrev_b32 v110, 16, v173\n"
  "v_and_b32 v111, s60, v173\n"
  "v_pk_add_f32 v[32:33], v[32:33], v[108:109]\n"
  "v_pk_add_f32 v[34:35], v[34:35], v[110:111]\n"
  "v_lshlrev_b32 v112, 16, v174\n"
  "v_and_b32 v113, s60, v174\n"
  "v_lshlrev_b32 v114, 16, v175\n"
  "v_and_b32 v115, s60, v175\n"
  "v_pk_add_f32 v[64:65], v[64:65], v[112:113]\n"
  "v_pk_add_f32 v[66:67], v[66:67], v[114:115]\n"
  "L_o24:\n"
  "s_waitcnt vmcnt(5)\n"
  "v_lshlrev_b32 v104, 16, v134\n"
  "v_and_b32 v105, s60, v134\n"
  "v_lshlrev_b32 v106, 16, v135\n"
  "v_and_b32 v107, s60, v135\n"
  "v_pk_add_f32 v[0:1], v[0:1], v[104:105]\n"
  "v_pk_add_f32 v[2:3], v[2:3], v[106:107]\n"
  "v_lshlrev_b32 v108, 16, v136\n"
  "v_and_b32 v109, s60, v136\n"
  "v_lshlrev_b32 v110, 16, v137\n"
  "v_and_b32 v111, s60, v137\n"
  "v_pk_add_f32 v[32:33], v[32:33], v[108:109]\n"
  "v_pk_add_f32 v[34:35], v[34:35], v[110:111]\n"
  "s_waitcnt vmcnt(4)\n"
  "v_lshlrev_b32 v112, 16, v138\n"
  "v_and_b32 v113, s60, v138\n"
  "v_lshlrev_b32 v114, 16, v139\n"
  "v_and_b32 v115, s60, v139\n"
  "v_pk_add_f32 v[64:65], v[64:65], v[112:113]\n"
  "v_pk_add_f32 v[66:67], v[66:67], v[114:115]\n"
  "s_waitcnt vmcnt(3)\n"
  "v_lshlrev_b32 v104, 16, v140\n"
  "v_and_b32 v105, s60, v140\n"
  "v_lshlrev_b32 v106, 16, v141\n"
  "v_and_b32 v107, s60, v141\n"
  "v_pk_add_f32 v[0:1], v[0:1], v[104:105]\n"
  "v_pk_add_f32 v[2:3], v[2:3], v[106:107]\n"
  "v_lshlrev_b32 v108, 16, v142\n"
  "v_and_b32 v109, s60, v142\n"
  "v_lshlrev_b32 v110, 16, v143\n"
  "v_and_b32 v111, s60, v143\n"
  "v_pk_add_f32 v[32:33], v[32:33], v[108:109]\n"
  "v_pk_add_f32 v[34:35], v[34:35], v[110:111]\n"
  "s_waitcnt vmcnt(2)\n"
  "v_lshlrev_b32 v112, 16, v144\n"
  "v_and_b32 v113, s60, v144\n"
  "v_lshlrev_b32 v114, 16, v145\n"
  "v_and_b32 v115, s60, v145\n"
  "v_pk_add_f32 v[64:65], v[64:65], v[112:113]\n"
  "v_pk_add_f32 v[66:67], v[66:67], v[114:115]\n"
  "s_waitcnt vmcnt(1)\n"
  "v_lshlrev_b32 v104, 16, v146\n"
  "v_and_b32 v105, s60, v146\n"
  "v_lshlrev_b32 v106, 16, v147\n"
  "v_and_b32 v107, s60, v147\n"
  "v_pk_add_f32 v[0:1], v[0:1], v[104:105]\n"
  "v_pk_add_f32 v[2:3], v[2:3], v[106:107]\n"
  "v_lshlrev_b32 v108, 16, v148\n"
  "v_and_b32 v109, s60, v148\n"
  "v_lshlrev_b32 v110, 16, v149\n"
  "v_and_b32 v111, s60, v149\n"
  "v_pk_add_f32 v[32:33], v[32:33], v[108:109]\n"
  "v_pk_add_f32 v[34:35], v[34:35], v[110:111]\n"
  "s_waitcnt vmcnt(0)\n"
  "v_lshlrev_b32 v112, 16, v150\n"
  "v_and_b32 v113, s60, v150\n"
  "v_lshlrev_b32 v114, 16, v151\n"
  "v_and_b32 v115, s60, v151\n"
  "v_pk_add_f32 v[64:65], v[64:65], v[112:113]\n"
  "v_pk_add_f32 v[66:67], v[66:67], v[114:115]\n"
  "v_cvt_pk_bf16_f32 v0, v0, v1\n"
  "v_cvt_pk_bf16_f32 v1, v2, v3\n"
  "s_mov_b32 s61, 0\n"
  "buffer_store_dwordx2 v[0:1], v193, s[80:83], s61 offen\n"
  "v_cvt_pk_bf16_f32 v32, v32, v33\n"
  "v_cvt_pk_bf16_f32 v33, v34, v35\n"
  "s_mov_b32 s61, 0x4000\n"
  "buffer_store_dwordx2 v[32:33], v193, s[80:83], s61 offen\n"
  "v_cvt_pk_bf16_f32 v64, v64, v65\n"
  "v_cvt_pk_bf16_f32 v65, v66, v67\n"
  "s_mov_b32 s61, 0x8000\n"
  "buffer_store_dwordx2 v[64:65], v193, s[80:83], s61 offen\n"
  "s_cmp_eq_u32 s19, 0\n"
  "s_cbranch_scc0 L_o19\n"
  "v_lshlrev_b32 v96, 16, v200\n"
  "v_and_b32 v97, s60, v200\n"
  "v_lshlrev_b32 v98, 16, v201\n"
  "v_and_b32 v99, s60, v201\n"
  "v_lshlrev_b32 v104, 16, v202\n"
  "v_and_b32 v105, s60, v202\n"
  "v_lshlrev_b32 v106, 16, v203\n"
  "v_and_b32 v107, s60, v203\n"
  "v_pk_add_f32 v[96:97], v[96:97], v[104:105]\n"
  "v_pk_add_f32 v[98:99], v[98:99], v[106:107]\n"
  "v_lshlrev_b32 v104, 16, v204\n"
  "v_and_b32 v105, s60, v204\n"
  "v_lshlrev_b32 v106, 16, v205\n"
  "v_and_b32 v107, s60, v205\n"
  "v_pk_add_f32 v[96:97], v[96:97], v[104:105]\n"
  "v_pk_add_f32 v[98:99], v[98:99], v[106:107]\n"
  "v_lshlrev_b32 v104, 16, v206\n"
  "v_and_b32 v105, s60, v206\n"
  "v_lshlrev_b32 v106, 16, v207\n"
  "v_and_b32 v107, s60, v207\n"
  "v_pk_add_f32 v[96:97], v[96:97], v[104:105]\n"
  "v_pk_add_f32 v[98:99], v[98:99], v[106:107]\n"
  "v_lshlrev_b32 v104, 16, v208\n"
  "v_and_b32 v105, s60, v208\n"
  "v_lshlrev_b32 v106, 16, v209\n"
  "v_and_b32 v107, s60, v209\n"
  "v_pk_add_f32 v[96:97], v[96:97], v[104:105]\n"
  "v_pk_add_f32 v[98:99], v[98:99], v[106:107]\n"
  "v_lshlrev_b32 v104, 16, v210\n"
  "v_and_b32 v105, s60, v210\n"
  "v_lshlrev_b32 v106, 16, v211\n"
  "v_and_b32 v107, s60, v211\n"
  "v_pk_add_f32 v[96:97], v[96:97], v[104:105]\n"
  "v_pk_add_f32 v[98:99], v[98:99], v[106:107]\n"
  "v_lshlrev_b32 v104, 16, v212\n"
  "v_and_b32 v105, s60, v212\n"
  "v_lshlrev_b32 v106, 16, v213\n"
  "v_and_b32 v107, s60, v213\n"
  "v_pk_add_f32 v[96:97], v[96:97], v[104:105]\n"
  "v_pk_add_f32 v[98:99], v[98:99], v[106:107]\n"
  "v_lshlrev_b32 v104, 16, v214\n"
  "v_and_b32 v105, s60, v214\n"
  "v_lshlrev_b32 v106, 16, v215\n"
  "v_and_b32 v107, s60, v215\n"
  "v_pk_add_f32 v[96:97], v[96:97], v[104:105]\n"
  "v_pk_add_f32 v[98:99], v[98:99], v[106:107]\n"
  "v_cvt_pk_bf16_f32 v96, v96, v97\n"
  "v_cvt_pk_bf16_f32 v97, v98, v99\n"
  "s_mov_b32 s61, 0x30000\n"
  "buffer_store_dwordx2 v[96:97], v193, s[80:83], s61 offen\n"
  "L_o19:\n"
  "s_endpgm\n"
  "L_end:\n"
  "L_slow:\n"
  "s_waitcnt vmcnt(0) lgkmcnt(0)\n"
  "s_nop 15\n"
  "v_and_b32 v160, 3, v254\n"
  "v_lshrrev_b32 v161, 5, v254\n"
  "v_lshlrev_b32 v161, 6, v161\n"
  "v_lshl_or_b32 v160, v160, 4, v161\n"
  "v_bfe_u32 v162, v254, 4, 1\n"
  "v_mul_u32_u24 v162, 48, v162\n"
  "v_xor_b32 v162, v162, v160\n"
  "v_bfe_u32 v161, v254, 2, 3\n"
  "v_bfe_u32 v163, v254, 2, 2\n"
  "v_and_b32 v164, 16, v254\n"
  "v_or_b32 v163, v163, v164\n"
  "v_lshl_or_b32 v166, v163, 8, v160\n"
  "v_xor_b32 v167, 32, v166\n"
  "v_bfe_u32 v163, v254, 4, 1\n"
  "v_lshrrev_b32 v164, 5, v254\n"
  "v_lshl_or_b32 v164, v163, 1, v164\n"
  "v_lshlrev_b32 v164, 4, v164\n"
  "v_and_b32 v163, 15, v254\n"
  "v_lshl_or_b32 v165, v163, 8, v164\n"
  "v_add_u32 v168, s65, v222\n"
  "v_add_u32 v169, s65, v225\n"
  "v_mov_b32 v170, 0xff61b1e6\n"
  "v_mov_b32 v171, 0xff61b1e6\n"
  "v_mov_b32 v172, 0xff61b1e6\n"
  "v_mov_b32 v173, 0xff61b1e6\n"
  "s_mov_b32 s90, 0\n"
  "L_s1:\n"
  "s_waitcnt vmcnt(0) lgkmcnt(0)\n"
  "s_nop 15\n"
  "s_add_u32 s91, s90, 0x1000\n"
  "buffer_load_dwordx4 a[64:67], v165, s[44:47], s90 offen\n"
  "buffer_load_dwordx4 a[68:71], v165, s[44:47], s90 offen offset:64\n"
  "buffer_load_dwordx4 a[72:75], v165, s[44:47], s90 offen offset:128\n"
  "buffer_load_dwordx4 a[76:79], v165, s[44:47], s90 offen offset:192\n"
  "buffer_load_dwordx4 a[80:83], v165, s[44:47], s91 offen\n"
  "buffer_load_dwordx4 a[84:87], v165, s[44:47], s91 offen offset:64\n"
  "buffer_load_dwordx4 a[88:91], v165, s[44:47], s91 offen offset:128\n"
  "buffer_load_dwordx4 a[92:95], v165, s[44:47], s91 offen offset:192\n"
  "s_waitcnt vmcnt(0)\n"
  "v_mfma_f32_16x16x32_bf16 v[128:131], a[64:67], v[200:203], 0\n"
  "v_mfma_f32_16x16x32_bf16 v[132:135], a[80:83], v[200:203], 0\n"
  "v_mfma_f32_16x16x32_bf16 v[128:131], a[68:71], v[204:207], v[128:131]\n"
  "v_mfma_f32_16x16x32_bf16 v[132:135], a[84:87], v[204:207], v[132:135]\n"
  "v_mfma_f32_16x16x32_bf16 v[128:131], a[72:75], v[208:211], v[128:131]\n"
  "v_mfma_f32_16x16x32_bf16 v[132:135], a[88:91], v[208:211], v[132:135]\n"
  "v_mfma_f32_16x16x32_bf16 v[128:131], a[76:79], v[212:215], v[128:131]\n"
  "v_mfma_f32_16x16x32_bf16 v[132:135], a[92:95], v[212:215], v[132:135]\n"
  "v_mfma_f32_16x16x32_bf16 v[136:139], a[64:67], v[112:115], 0\n"
  "v_mfma_f32_16x16x32_bf16 v[140:143], a[80:83], v[112:115], 0\n"
  "v_mfma_f32_16x16x32_bf16 v[136:139], a[68:71], v[116:119], v[136:139]\n"
  "v_mfma_f32_16x16x32_bf16 v[140:143], a[84:87], v[116:119], v[140:143]\n"
  "v_mfma_f32_16x16x32_bf16 v[136:139], a[72:75], v[120:123], v[136:139]\n"
  "v_mfma_f32_16x16x32_bf16 v[140:143], a[88:91], v[120:123], v[140:143]\n"
  "v_mfma_f32_16x16x32_bf16 v[136:139], a[76:79], v[124:127], v[136:139]\n"
  "v_mfma_f32_16x16x32_bf16 v[140:143], a[92:95], v[124:127], v[140:143]\n"
  "v_mfma_f32_16x16x32_bf16 v[144:147], a[64:67], v[96:99], 0\n"
  "v_mfma_f32_16x16x32_bf16 v[148:151], a[80:83], v[96:99], 0\n"
  "v_mfma_f32_16x16x32_bf16 v[144:147], a[68:71], v[100:103], v[144:147]\n"
  "v_mfma_f32_16x16x32_bf16 v[148:151], a[84:87], v[100:103], v[148:151]\n"
  "v_mfma_f32_16x16x32_bf16 v[144:147], a[72:75], v[104:107], v[144:147]\n"
  "v_mfma_f32_16x16x32_bf16 v[148:151], a[88:91], v[104:107], v[148:151]\n"
  "v_mfma_f32_16x16x32_bf16 v[144:147], a[76:79], v[108:111], v[144:147]\n"
  "v_mfma_f32_16x16x32_bf16 v[148:151], a[92:95], v[108:111], v[148:151]\n"
  "v_mfma_f32_16x16x32_bf16 v[152:155], a[64:67], a[48:51], 0\n"
  "v_mfma_f32_16x16x32_bf16 v[156:159], a[80:83], a[48:51], 0\n"
  "v_mfma_f32_16x16x32_bf16 v[152:155], a[68:71], a[52:55], v[152:155]\n"
  "v_mfma_f32_16x16x32_bf16 v[156:159], a[84:87], a[52:55], v[156:159]\n"
  "v_mfma_f32_16x16x32_bf16 v[152:155], a[72:75], a[56:59], v[152:155]\n"
  "v_mfma_f32_16x16x32_bf16 v[156:159], a[88:91], a[56:59], v[156:159]\n"
  "v_mfma_f32_16x16x32_bf16 v[152:155], a[76:79], a[60:63], v[152:155]\n"
  "v_mfma_f32_16x16x32_bf16 v[156:159], a[92:95], a[60:63], v[156:159]\n"
  "v_max3_f32 v170, v170, v128, v129\n"
  "v_max3_f32 v170, v170, v130, v131\n"
  "v_max3_f32 v170, v170, v132, v133\n"
  "v_max3_f32 v170, v170, v134, v135\n"
  "v_max3_f32 v171, v171, v136, v137\n"
  "v_max3_f32 v171, v171, v138, v139\n"
  "v_max3_f32 v171, v171, v140, v141\n"
  "v_max3_f32 v171, v171, v142, v143\n"
  "v_max3_f32 v172, v172, v144, v145\n"
  "v_max3_f32 v172, v172, v146, v147\n"
  "v_max3_f32 v172, v172, v148, v149\n"
  "v_max3_f32 v172, v172, v150, v151\n"
  "v_max3_f32 v173, v173, v152, v153\n"
  "v_max3_f32 v173, v173, v154, v155\n"
  "v_max3_f32 v173, v173, v156, v157\n"
  "v_max3_f32 v173, v173, v158, v159\n"
  "s_waitcnt vmcnt(0) lgkmcnt(0)\n"
  "s_nop 15\n"
  "s_add_u32 s90, s90, 0x2000\n"
  "s_cmp_lt_u32 s90, 0x20000\n"
  "s_cbranch_scc1 L_s1\n"
  "v_mov_b32 v174, v170\n"
  "s_nop 1\n"
  "v_permlane16_swap_b32 v170, v174\n"
  "v_max_f32 v170, v170, v174\n"
  "v_mov_b32 v174, v170\n"
  "s_nop 1\n"
  "v_permlane32_swap_b32 v170, v174\n"
  "v_max_f32 v170, v170, v174\n"
  "v_sub_f32 v184, 0, v170\n"
  "v_sub_f32 v185, 0, v170\n"
  "v_sub_f32 v186, 0, v170\n"
  "v_sub_f32 v187, 0, v170\n"
  "v_mov_b32 v174, v171\n"
  "s_nop 1\n"
  "v_permlane16_swap_b32 v171, v174\n"
  "v_max_f32 v171, v171, v174\n"
  "v_mov_b32 v174, v171\n"
  "s_nop 1\n"
  "v_permlane32_swap_b32 v171, v174\n"
  "v_max_f32 v171, v171, v174\n"
  "v_sub_f32 v188, 0, v171\n"
  "v_sub_f32 v189, 0, v171\n"
  "v_sub_f32 v190, 0, v171\n"
  "v_sub_f32 v191, 0, v171\n"
  "v_mov_b32 v174, v172\n"
  "s_nop 1\n"
  "v_permlane16_swap_b32 v172, v174\n"
  "v_max_f32 v172, v172, v174\n"
  "v_mov_b32 v174, v172\n"
  "s_nop 1\n"
  "v_permlane32_swap_b32 v172, v174\n"
  "v_max_f32 v172, v172, v174\n"
  "v_sub_f32 v192, 0, v172\n"
  "v_sub_f32 v193, 0, v172\n"
  "v_sub_f32 v194, 0, v172\n"
  "v_sub_f32 v195, 0, v172\n"
  "v_mov_b32 v174, v173\n"
  "s_nop 1\n"
  "v_permlane16_swap_b32 v173, v174\n"
  "v_max_f32 v173, v173, v174\n"
  "v_mov_b32 v174, v173\n"
  "s_nop 1\n"
  "v_permlane32_swap_b32 v173, v174\n"
  "v_max_f32 v173, v173, v174\n"
  "v_sub_f32 v196, 0, v173\n"
  "v_sub_f32 v197, 0, v173\n"
  "v_sub_f32 v198, 0, v173\n"
  "v_sub_f32 v199, 0, v173\n"
  "v_mov_b32 v0, 0\n"
  "v_mov_b32 v1, 0\n"
  "v_mov_b32 v2, 0\n"
  "v_mov_b32 v3, 0\n"
  "v_mov_b32 v4, 0\n"
  "v_mov_b32 v5, 0\n"
  "v_mov_b32 v6, 0\n"
  "v_mov_b32 v7, 0\n"
  "v_mov_b32 v8, 0\n"
  "v_mov_b32 v9, 0\n"
  "v_mov_b32 v10, 0\n"
  "v_mov_b32 v11, 0\n"
  "v_mov_b32 v12, 0\n"
  "v_mov_b32 v13, 0\n"
  "v_mov_b32 v14, 0\n"
  "v_mov_b32 v15, 0\n"
  "v_mov_b32 v16, 0\n"
  "v_mov_b32 v17, 0\n"
  "v_mov_b32 v18, 0\n"
  "v_mov_b32 v19, 0\n"
  "v_mov_b32 v20, 0\n"
  "v_mov_b32 v21, 0\n"
  "v_mov_b32 v22, 0\n"
  "v_mov_b32 v23, 0\n"
  "v_mov_b32 v24, 0\n"
  "v_mov_b32 v25, 0\n"
  "v_mov_b32 v26, 0\n"
  "v_mov_b32 v27, 0\n"
  "v_mov_b32 v28, 0\n"
  "v_mov_b32 v29, 0\n"
  "v_mov_b32 v30, 0\n"
  "v_mov_b32 v31, 0\n"
  "v_mov_b32 v32, 0\n"
  "v_mov_b32 v33, 0\n"
  "v_mov_b32 v34, 0\n"
  "v_mov_b32 v35, 0\n"
  "v_mov_b32 v36, 0\n"
  "v_mov_b32 v37, 0\n"
  "v_mov_b32 v38, 0\n"
  "v_mov_b32 v39, 0\n"
  "v_mov_b32 v40, 0\n"
  "v_mov_b32 v41, 0\n"
  "v_mov_b32 v42, 0\n"
  "v_mov_b32 v43, 0\n"
  "v_mov_b32 v44, 0\n"
  "v_mov_b32 v45, 0\n"
  "v_mov_b32 v46, 0\n"
  "v_mov_b32 v47, 0\n"
  "v_mov_b32 v48, 0\n"
  "v_mov_b32 v49, 0\n"
  "v_mov_b32 v50, 0\n"
  "v_mov_b32 v51, 0\n"
  "v_mov_b32 v52, 0\n"
  "v_mov_b32 v53, 0\n"
  "v_mov_b32 v54, 0\n"
  "v_mov_b32 v55, 0\n"
  "v_mov_b32 v56, 0\n"
  "v_mov_b32 v57, 0\n"
  "v_mov_b32 v58, 0\n"
  "v_mov_b32 v59, 0\n"
  "v_mov_b32 v60, 0\n"
  "v_mov_b32 v61, 0\n"
  "v_mov_b32 v62, 0\n"
  "v_mov_b32 v63, 0\n"
  "v_mov_b32 v64, 0\n"
  "v_mov_b32 v65, 0\n"
  "v_mov_b32 v66, 0\n"
  "v_mov_b32 v67, 0\n"
  "v_mov_b32 v68, 0\n"
  "v_mov_b32 v69, 0\n"
  "v_mov_b32 v70, 0\n"
  "v_mov_b32 v71, 0\n"
  "v_mov_b32 v72, 0\n"
  "v_mov_b32 v73, 0\n"
  "v_mov_b32 v74, 0\n"
  "v_mov_b32 v75, 0\n"
  "v_mov_b32 v76, 0\n"
  "v_mov_b32 v77, 0\n"
  "v_mov_b32 v78, 0\n"
  "v_mov_b32 v79, 0\n"
  "v_mov_b32 v80, 0\n"
  "v_mov_b32 v81, 0\n"
  "v_mov_b32 v82, 0\n"
  "v_mov_b32 v83, 0\n"
  "v_mov_b32 v84, 0\n"
  "v_mov_b32 v85, 0\n"
  "v_mov_b32 v86, 0\n"
  "v_mov_b32 v87, 0\n"
  "v_mov_b32 v88, 0\n"
  "v_mov_b32 v89, 0\n"
  "v_mov_b32 v90, 0\n"
  "v_mov_b32 v91, 0\n"
  "v_mov_b32 v92, 0\n"
  "v_mov_b32 v93, 0\n"
  "v_mov_b32 v94, 0\n"
  "v_mov_b32 v95, 0\n"
  "v_accvgpr_write_b32 a0, 0\n"
  "v_accvgpr_write_b32 a1, 0\n"
  "v_accvgpr_write_b32 a2, 0\n"
  "v_accvgpr_write_b32 a3, 0\n"
  "v_accvgpr_write_b32 a4, 0\n"
  "v_accvgpr_write_b32 a5, 0\n"
  "v_accvgpr_write_b32 a6, 0\n"
  "v_accvgpr_write_b32 a7, 0\n"
  "v_accvgpr_write_b32 a8, 0\n"
  "v_accvgpr_write_b32 a9, 0\n"
  "v_accvgpr_write_b32 a10, 0\n"
  "v_accvgpr_write_b32 a11, 0\n"
  "v_accvgpr_write_b32 a12, 0\n"
  "v_accvgpr_write_b32 a13, 0\n"
  "v_accvgpr_write_b32 a14, 0\n"
  "v_accvgpr_write_b32 a15, 0\n"
  "v_accvgpr_write_b32 a16, 0\n"
  "v_accvgpr_write_b32 a17, 0\n"
  "v_accvgpr_write_b32 a18, 0\n"
  "v_accvgpr_write_b32 a19, 0\n"
  "v_accvgpr_write_b32 a20, 0\n"
  "v_accvgpr_write_b32 a21, 0\n"
  "v_accvgpr_write_b32 a22, 0\n"
  "v_accvgpr_write_b32 a23, 0\n"
  "v_accvgpr_write_b32 a24, 0\n"
  "v_accvgpr_write_b32 a25, 0\n"
  "v_accvgpr_write_b32 a26, 0\n"
  "v_accvgpr_write_b32 a27, 0\n"
  "v_accvgpr_write_b32 a28, 0\n"
  "v_accvgpr_write_b32 a29, 0\n"
  "v_accvgpr_write_b32 a30, 0\n"
  "v_accvgpr_write_b32 a31, 0\n"
  "v_accvgpr_write_b32 a32, 0\n"
  "v_accvgpr_write_b32 a33, 0\n"
  "v_accvgpr_write_b32 a34, 0\n"
  "v_accvgpr_write_b32 a35, 0\n"
  "v_accvgpr_write_b32 a36, 0\n"
  "v_accvgpr_write_b32 a37, 0\n"
  "v_accvgpr_write_b32 a38, 0\n"
  "v_accvgpr_write_b32 a39, 0\n"
  "v_accvgpr_write_b32 a40, 0\n"
  "v_accvgpr_write_b32 a41, 0\n"
  "v_accvgpr_write_b32 a42, 0\n"
  "v_accvgpr_write_b32 a43, 0\n"
  "v_accvgpr_write_b32 a44, 0\n"
  "v_accvgpr_write_b32 a45, 0\n"
  "v_accvgpr_write_b32 a46, 0\n"
  "v_accvgpr_write_b32 a47, 0\n"
  "s_mov_b32 s90, 0\n"
  "L_s2:\n"
  "s_waitcnt vmcnt(0) lgkmcnt(0)\n"
  "s_nop 15\n"
  "s_add_u32 s91, s90, 0x1000\n"
  "buffer_load_dwordx4 a[64:67], v165, s[44:47], s90 offen\n"
  "buffer_load_dwordx4 a[68:71], v165, s[44:47], s90 offen offset:64\n"
  "buffer_load_dwordx4 a[72:75], v165, s[44:47], s90 offen offset:128\n"
  "buffer_load_dwordx4 a[76:79], v165, s[44:47], s90 offen offset:192\n"
  "buffer_load_dwordx4 a[80:83], v165, s[44:47], s91 offen\n"
  "buffer_load_dwordx4 a[84:87], v165, s[44:47], s91 offen offset:64\n"
  "buffer_load_dwordx4 a[88:91], v165, s[44:47], s91 offen offset:128\n"
  "buffer_load_dwordx4 a[92:95], v165, s[44:47], s91 offen offset:192\n"
  "s_waitcnt vmcnt(0)\n"
  "v_mfma_f32_16x16x32_bf16 v[128:131], a[64:67], v[200:203], v[184:187]\n"
  "v_mfma_f32_16x16x32_bf16 v[132:135], a[80:83], v[200:203], v[184:187]\n"
  "v_mfma_f32_16x16x32_bf16 v[128:131], a[68:71], v[204:207], v[128:131]\n"
  "v_mfma_f32_16x16x32_bf16 v[132:135], a[84:87], v[204:207], v[132:135]\n"
  "v_mfma_f32_16x16x32_bf16 v[128:131], a[72:75], v[208:211], v[128:131]\n"
  "v_mfma_f32_16x16x32_bf16 v[132:135], a[88:91], v[208:211], v[132:135]\n"
  "v_mfma_f32_16x16x32_bf16 v[128:131], a[76:79], v[212:215], v[128:131]\n"
  "v_mfma_f32_16x16x32_bf16 v[132:135], a[92:95], v[212:215], v[132:135]\n"
  "v_mfma_f32_16x16x32_bf16 v[136:139], a[64:67], v[112:115], v[188:191]\n"
  "v_mfma_f32_16x16x32_bf16 v[140:143], a[80:83], v[112:115], v[188:191]\n"
  "v_mfma_f32_16x16x32_bf16 v[136:139], a[68:71], v[116:119], v[136:139]\n"
  "v_mfma_f32_16x16x32_bf16 v[140:143], a[84:87], v[116:119], v[140:143]\n"
  "v_mfma_f32_16x16x32_bf16 v[136:139], a[72:75], v[120:123], v[136:139]\n"
  "v_mfma_f32_16x16x32_bf16 v[140:143], a[88:91], v[120:123], v[140:143]\n"
  "v_mfma_f32_16x16x32_bf16 v[136:139], a[76:79], v[124:127], v[136:139]\n"
  "v_mfma_f32_16x16x32_bf16 v[140:143], a[92:95], v[124:127], v[140:143]\n"
  "v_mfma_f32_16x16x32_bf16 v[144:147], a[64:67], v[96:99], v[192:195]\n"
  "v_mfma_f32_16x16x32_bf16 v[148:151], a[80:83], v[96:99], v[192:195]\n"
  "v_mfma_f32_16x16x32_bf16 v[144:147], a[68:71], v[100:103], v[144:147]\n"
  "v_mfma_f32_16x16x32_bf16 v[148:151], a[84:87], v[100:103], v[148:151]\n"
  "v_mfma_f32_16x16x32_bf16 v[144:147], a[72:75], v[104:107], v[144:147]\n"
  "v_mfma_f32_16x16x32_bf16 v[148:151], a[88:91], v[104:107], v[148:151]\n"
  "v_mfma_f32_16x16x32_bf16 v[144:147], a[76:79], v[108:111], v[144:147]\n"
  "v_mfma_f32_16x16x32_bf16 v[148:151], a[92:95], v[108:111], v[148:151]\n"
  "v_mfma_f32_16x16x32_bf16 v[152:155], a[64:67], a[48:51], v[196:199]\n"
  "v_mfma_f32_16x16x32_bf16 v[156:159], a[80:83], a[48:51], v[196:199]\n"
  "v_mfma_f32_16x16x32_bf16 v[152:155], a[68:71], a[52:55], v[152:155]\n"
  "v_mfma_f32_16x16x32_bf16 v[156:159], a[84:87], a[52:55], v[156:159]\n"
  "v_mfma_f32_16x16x32_bf16 v[152:155], a[72:75], a[56:59], v[152:155]\n"
  "v_mfma_f32_16x16x32_bf16 v[156:159], a[88:91], a[56:59], v[156:159]\n"
  "v_mfma_f32_16x16x32_bf16 v[152:155], a[76:79], a[60:63], v[152:155]\n"
  "v_mfma_f32_16x16x32_bf16 v[156:159], a[92:95], a[60:63], v[156:159]\n"
  "v_exp_f32 v128, v128\n"
  "v_exp_f32 v129, v129\n"
  "v_exp_f32 v130, v130\n"
  "v_exp_f32 v131, v131\n"
  "v_exp_f32 v132, v132\n"
  "v_exp_f32 v133, v133\n"
  "v_exp_f32 v134, v134\n"
  "v_exp_f32 v135, v135\n"
  "v_cvt_pk_bf16_f32 v128, v128, v129\n"
  "v_cvt_pk_bf16_f32 v129, v130, v131\n"
  "v_cvt_pk_bf16_f32 v130, v132, v133\n"
  "v_cvt_pk_bf16_f32 v131, v134, v135\n"
  "v_exp_f32 v136, v136\n"
  "v_exp_f32 v137, v137\n"
  "v_exp_f32 v138, v138\n"
  "v_exp_f32 v139, v139\n"
  "v_exp_f32 v140, v140\n"
  "v_exp_f32 v141, v141\n"
  "v_exp_f32 v142, v142\n"
  "v_exp_f32 v143, v143\n"
  "v_cvt_pk_bf16_f32 v136, v136, v137\n"
  "v_cvt_pk_bf16_f32 v137, v138, v139\n"
  "v_cvt_pk_bf16_f32 v138, v140, v141\n"
  "v_cvt_pk_bf16_f32 v139, v142, v143\n"
  "v_exp_f32 v144, v144\n"
  "v_exp_f32 v145, v145\n"
  "v_exp_f32 v146, v146\n"
  "v_exp_f32 v147, v147\n"
  "v_exp_f32 v148, v148\n"
  "v_exp_f32 v149, v149\n"
  "v_exp_f32 v150, v150\n"
  "v_exp_f32 v151, v151\n"
  "v_cvt_pk_bf16_f32 v144, v144, v145\n"
  "v_cvt_pk_bf16_f32 v145, v146, v147\n"
  "v_cvt_pk_bf16_f32 v146, v148, v149\n"
  "v_cvt_pk_bf16_f32 v147, v150, v151\n"
  "v_exp_f32 v152, v152\n"
  "v_exp_f32 v153, v153\n"
  "v_exp_f32 v154, v154\n"
  "v_exp_f32 v155, v155\n"
  "v_exp_f32 v156, v156\n"
  "v_exp_f32 v157, v157\n"
  "v_exp_f32 v158, v158\n"
  "v_exp_f32 v159, v159\n"
  "v_cvt_pk_bf16_f32 v152, v152, v153\n"
  "v_cvt_pk_bf16_f32 v153, v154, v155\n"
  "v_cvt_pk_bf16_f32 v154, v156, v157\n"
  "v_cvt_pk_bf16_f32 v155, v158, v159\n"
  "s_add_u32 m0, s65, 0\n"
  "s_add_u32 s66, s90, 0\n"
  "buffer_load_dwordx4 v166, s[48:51], s66 offen lds\n"
  "buffer_load_dwordx4 v167, s[48:51], s66 offen offset:1024 lds\n"
  "buffer_load_dwordx4 v166, s[48:51], s66 offen offset:2048 lds\n"
  "buffer_load_dwordx4 v167, s[48:51], s66 offen offset:3072 lds\n"
  "s_waitcnt vmcnt(0)\n"
  "ds_read_b64_tr_b16 a[96:97], v168\n"
  "ds_read_b64_tr_b16 a[98:99], v168 offset:256\n"
  "ds_read_b64_tr_b16 a[100:101], v169\n"
  "ds_read_b64_tr_b16 a[102:103], v169 offset:256\n"
  "ds_read_b64_tr_b16 a[104:105], v168 offset:512\n"
  "ds_read_b64_tr_b16 a[106:107], v168 offset:768\n"
  "ds_read_b64_tr_b16 a[108:109], v169 offset:512\n"
  "ds_read_b64_tr_b16 a[110:111], v169 offset:768\n"
  "s_waitcnt lgkmcnt(0)\n"
  "s_add_u32 m0, s65, 0\n"
  "s_add_u32 s67, s90, 0x80\n"
  "buffer_load_dwordx4 v166, s[48:51], s67 offen lds\n"
  "buffer_load_dwordx4 v167, s[48:51], s67 offen offset:1024 lds\n"
  "buffer_load_dwordx4 v166, s[48:51], s67 offen offset:2048 lds\n"
  "buffer_load_dwordx4 v167, s[48:51], s67 offen offset:3072 lds\n"
  "s_waitcnt vmcnt(0)\n"
  "ds_read_b64_tr_b16 a[112:113], v168\n"
  "ds_read_b64_tr_b16 a[114:115], v168 offset:256\n"
  "ds_read_b64_tr_b16 a[116:117], v169\n"
  "ds_read_b64_tr_b16 a[118:119], v169 offset:256\n"
  "ds_read_b64_tr_b16 a[120:121], v168 offset:512\n"
  "ds_read_b64_tr_b16 a[122:123], v168 offset:768\n"
  "ds_read_b64_tr_b16 a[124:125], v169 offset:512\n"
  "ds_read_b64_tr_b16 a[126:127], v169 offset:768\n"
  "s_waitcnt lgkmcnt(0)\n"
  "v_mfma_f32_16x16x32_bf16 v[0:3], a[96:99], v[128:131], v[0:3]\n"
  "v_mfma_f32_16x16x32_bf16 v[4:7], a[100:103], v[128:131], v[4:7]\n"
  "v_mfma_f32_16x16x32_bf16 v[8:11], a[104:107], v[128:131], v[8:11]\n"
  "v_mfma_f32_16x16x32_bf16 v[12:15], a[108:111], v[128:131], v[12:15]\n"
  "v_mfma_f32_16x16x32_bf16 v[16:19], a[112:115], v[128:131], v[16:19]\n"
  "v_mfma_f32_16x16x32_bf16 v[20:23], a[116:119], v[128:131], v[20:23]\n"
  "v_mfma_f32_16x16x32_bf16 v[24:27], a[120:123], v[128:131], v[24:27]\n"
  "v_mfma_f32_16x16x32_bf16 v[28:31], a[124:127], v[128:131], v[28:31]\n"
  "v_mfma_f32_16x16x32_bf16 a[32:35], a[128:131], v[128:131], a[32:35]\n"
  "v_mfma_f32_16x16x32_bf16 v[32:35], a[96:99], v[136:139], v[32:35]\n"
  "v_mfma_f32_16x16x32_bf16 v[36:39], a[100:103], v[136:139], v[36:39]\n"
  "v_mfma_f32_16x16x32_bf16 v[40:43], a[104:107], v[136:139], v[40:43]\n"
  "v_mfma_f32_16x16x32_bf16 v[44:47], a[108:111], v[136:139], v[44:47]\n"
  "v_mfma_f32_16x16x32_bf16 v[48:51], a[112:115], v[136:139], v[48:51]\n"
  "v_mfma_f32_16x16x32_bf16 v[52:55], a[116:119], v[136:139], v[52:55]\n"
  "v_mfma_f32_16x16x32_bf16 v[56:59], a[120:123], v[136:139], v[56:59]\n"
  "v_mfma_f32_16x16x32_bf16 v[60:63], a[124:127], v[136:139], v[60:63]\n"
  "v_mfma_f32_16x16x32_bf16 a[36:39], a[128:131], v[136:139], a[36:39]\n"
  "v_mfma_f32_16x16x32_bf16 v[64:67], a[96:99], v[144:147], v[64:67]\n"
  "v_mfma_f32_16x16x32_bf16 v[68:71], a[100:103], v[144:147], v[68:71]\n"
  "v_mfma_f32_16x16x32_bf16 v[72:75], a[104:107], v[144:147], v[72:75]\n"
  "v_mfma_f32_16x16x32_bf16 v[76:79], a[108:111], v[144:147], v[76:79]\n"
  "v_mfma_f32_16x16x32_bf16 v[80:83], a[112:115], v[144:147], v[80:83]\n"
  "v_mfma_f32_16x16x32_bf16 v[84:87], a[116:119], v[144:147], v[84:87]\n"
  "v_mfma_f32_16x16x32_bf16 v[88:91], a[120:123], v[144:147], v[88:91]\n"
  "v_mfma_f32_16x16x32_bf16 v[92:95], a[124:127], v[144:147], v[92:95]\n"
  "v_mfma_f32_16x16x32_bf16 a[40:43], a[128:131], v[144:147], a[40:43]\n"
  "v_mfma_f32_16x16x32_bf16 a[0:3], a[96:99], v[152:155], a[0:3]\n"
  "v_mfma_f32_16x16x32_bf16 a[4:7], a[100:103], v[152:155], a[4:7]\n"
  "v_mfma_f32_16x16x32_bf16 a[8:11], a[104:107], v[152:155], a[8:11]\n"
  "v_mfma_f32_16x16x32_bf16 a[12:15], a[108:111], v[152:155], a[12:15]\n"
  "v_mfma_f32_16x16x32_bf16 a[16:19], a[112:115], v[152:155], a[16:19]\n"
  "v_mfma_f32_16x16x32_bf16 a[20:23], a[116:119], v[152:155], a[20:23]\n"
  "v_mfma_f32_16x16x32_bf16 a[24:27], a[120:123], v[152:155], a[24:27]\n"
  "v_mfma_f32_16x16x32_bf16 a[28:31], a[124:127], v[152:155], a[28:31]\n"
  "v_mfma_f32_16x16x32_bf16 a[44:47], a[128:131], v[152:155], a[44:47]\n"
  "s_waitcnt vmcnt(0) lgkmcnt(0)\n"
  "s_nop 15\n"
  "s_add_u32 s90, s90, 0x2000\n"
  "s_cmp_lt_u32 s90, 0x20000\n"
  "s_cbranch_scc1 L_s2\n"
  "s_add_u32 m0, s65, 0\n"
  "s_add_u32 s66, s88, 2\n"
  "s_and_b32 s66, s66, 3\n"
  "s_lshl_b32 s66, s66, 17\n"
  "buffer_load_dwordx4 v240, s[72:75], s66 offen lds\n"
  "buffer_load_dwordx4 v241, s[72:75], s66 offen offset:1024 lds\n"
  "buffer_load_dwordx4 v242, s[72:75], s66 offen offset:2048 lds\n"
  "buffer_load_dwordx4 v243, s[72:75], s66 offen offset:3072 lds\n"
  "s_cmp_eq_u64 s[96:97], 0\n"
  "s_cbranch_scc1 L_gok\n"
  "v_accvgpr_read_b32 v160, a44\n"
  "v_rcp_f32 v160, v160\n"
  "s_cmp_eq_u32 s19, 0\n"
  "s_cbranch_scc0 L_o25\n"
  "v_accvgpr_read_b32 v96, a0\n"
  "v_accvgpr_read_b32 v97, a1\n"
  "v_accvgpr_read_b32 v98, a2\n"
  "v_accvgpr_read_b32 v99, a3\n"
  "v_accvgpr_read_b32 v100, a4\n"
  "v_accvgpr_read_b32 v101, a5\n"
  "v_accvgpr_read_b32 v102, a6\n"
  "v_accvgpr_read_b32 v103, a7\n"
  "L_o25:\n"
  "s_cmp_eq_u32 s19, 1\n"
  "s_cbranch_scc0 L_o26\n"
  "v_accvgpr_read_b32 v96, a8\n"
  "v_accvgpr_read_b32 v97, a9\n"
  "v_accvgpr_read_b32 v98, a10\n"
  "v_accvgpr_read_b32 v99, a11\n"
  "v_accvgpr_read_b32 v100, a12\n"
  "v_accvgpr_read_b32 v101, a13\n"
  "v_accvgpr_read_b32 v102, a14\n"
  "v_accvgpr_read_b32 v103, a15\n"
  "L_o26:\n"
  "s_cmp_eq_u32 s19, 2\n"
  "s_cbranch_scc0 L_o27\n"
  "v_accvgpr_read_b32 v96, a16\n"
  "v_accvgpr_read_b32 v97, a17\n"
  "v_accvgpr_read_b32 v98, a18\n"
  "v_accvgpr_read_b32 v99, a19\n"
  "v_accvgpr_read_b32 v100, a20\n"
  "v_accvgpr_read_b32 v101, a21\n"
  "v_accvgpr_read_b32 v102, a22\n"
  "v_accvgpr_read_b32 v103, a23\n"
  "L_o27:\n"
  "s_cmp_eq_u32 s19, 3\n"
  "s_cbranch_scc0 L_o28\n"
  "v_accvgpr_read_b32 v96, a24\n"
  "v_accvgpr_read_b32 v97, a25\n"
  "v_accvgpr_read_b32 v98, a26\n"
  "v_accvgpr_read_b32 v99, a27\n"
  "v_accvgpr_read_b32 v100, a28\n"
  "v_accvgpr_read_b32 v101, a29\n"
  "v_accvgpr_read_b32 v102, a30\n"
  "v_accvgpr_read_b32 v103, a31\n"
  "L_o28:\n"
  "v_mul_f32 v96, v160, v96\n"
  "v_mul_f32 v97, v160, v97\n"
  "v_mul_f32 v98, v160, v98\n"
  "v_mul_f32 v99, v160, v99\n"
  "v_mul_f32 v100, v160, v100\n"
  "v_mul_f32 v101, v160, v101\n"
  "v_mul_f32 v102, v160, v102\n"
  "v_mul_f32 v103, v160, v103\n"
  "v_cvt_pk_bf16_f32 v96, v96, v97\n"
  "v_cvt_pk_bf16_f32 v97, v98, v99\n"
  "v_cvt_pk_bf16_f32 v98, v100, v101\n"
  "v_cvt_pk_bf16_f32 v99, v102, v103\n"
  "buffer_store_dwordx4 v[96:99], v251, s[52:55], s24 offen sc1\n"
  "s_mov_b32 s24, 0x40000000\n"
  "L_gok:\n"
  "v_accvgpr_read_b32 v199, a32\n"
  "v_rcp_f32 v198, v199\n"
  "s_nop 0\n"
  "v_mov_b32 v199, v198\n"
  "v_pk_mul_f32 v[0:1], v[0:1], v[198:199]\n"
  "v_pk_mul_f32 v[2:3], v[2:3], v[198:199]\n"
  "v_pk_mul_f32 v[4:5], v[4:5], v[198:199]\n"
  "v_pk_mul_f32 v[6:7], v[6:7], v[198:199]\n"
  "v_cvt_pk_bf16_f32 v128, v0, v1\n"
  "v_cvt_pk_bf16_f32 v129, v2, v3\n"
  "v_cvt_pk_bf16_f32 v130, v4, v5\n"
  "v_cvt_pk_bf16_f32 v131, v6, v7\n"
  "buffer_store_dwordx4 v[128:131], v250, s[52:55], 0 offen sc1\n"
  "v_pk_mul_f32 v[8:9], v[8:9], v[198:199]\n"
  "v_pk_mul_f32 v[10:11], v[10:11], v[198:199]\n"
  "v_pk_mul_f32 v[12:13], v[12:13], v[198:199]\n"
  "v_pk_mul_f32 v[14:15], v[14:15], v[198:199]\n"
  "v_cvt_pk_bf16_f32 v132, v8, v9\n"
  "v_cvt_pk_bf16_f32 v133, v10, v11\n"
  "v_cvt_pk_bf16_f32 v134, v12, v13\n"
  "v_cvt_pk_bf16_f32 v135, v14, v15\n"
  "buffer_store_dwordx4 v[132:135], v250, s[52:55], 0 offen offset:1024 sc1\n"
  "v_pk_mul_f32 v[16:17], v[16:17], v[198:199]\n"
  "v_pk_mul_f32 v[18:19], v[18:19], v[198:199]\n"
  "v_pk_mul_f32 v[20:21], v[20:21], v[198:199]\n"
  "v_pk_mul_f32 v[22:23], v[22:23], v[198:199]\n"
  "v_cvt_pk_bf16_f32 v152, v16, v17\n"
  "v_cvt_pk_bf16_f32 v153, v18, v19\n"
  "v_cvt_pk_bf16_f32 v154, v20, v21\n"
  "v_cvt_pk_bf16_f32 v155, v22, v23\n"
  "buffer_store_dwordx4 v[152:155], v250, s[52:55], 0 offen offset:2048 sc1\n"
  "v_pk_mul_f32 v[24:25], v[24:25], v[198:199]\n"
  "v_pk_mul_f32 v[26:27], v[26:27], v[198:199]\n"
  "v_pk_mul_f32 v[28:29], v[28:29], v[198:199]\n"
  "v_pk_mul_f32 v[30:31], v[30:31], v[198:199]\n"
  "v_cvt_pk_bf16_f32 v156, v24, v25\n"
  "v_cvt_pk_bf16_f32 v157, v26, v27\n"
  "v_cvt_pk_bf16_f32 v158, v28, v29\n"
  "v_cvt_pk_bf16_f32 v159, v30, v31\n"
  "buffer_store_dwordx4 v[156:159], v250, s[52:55], 0 offen offset:3072 sc1\n"
  "v_accvgpr_read_b32 v199, a36\n"
  "v_rcp_f32 v198, v199\n"
  "s_nop 0\n"
  "v_mov_b32 v199, v198\n"
  "v_pk_mul_f32 v[32:33], v[32:33], v[198:199]\n"
  "v_pk_mul_f32 v[34:35], v[34:35], v[198:199]\n"
  "v_pk_mul_f32 v[36:37], v[36:37], v[198:199]\n"
  "v_pk_mul_f32 v[38:39], v[38:39], v[198:199]\n"
  "v_cvt_pk_bf16_f32 v136, v32, v33\n"
  "v_cvt_pk_bf16_f32 v137, v34, v35\n"
  "v_cvt_pk_bf16_f32 v138, v36, v37\n"
  "v_cvt_pk_bf16_f32 v139, v38, v39\n"
  "buffer_store_dwordx4 v[136:139], v250, s[52:55], s68 offen sc1\n"
  "v_pk_mul_f32 v[40:41], v[40:41], v[198:199]\n"
  "v_pk_mul_f32 v[42:43], v[42:43], v[198:199]\n"
  "v_pk_mul_f32 v[44:45], v[44:45], v[198:199]\n"
  "v_pk_mul_f32 v[46:47], v[46:47], v[198:199]\n"
  "v_cvt_pk_bf16_f32 v140, v40, v41\n"
  "v_cvt_pk_bf16_f32 v141, v42, v43\n"
  "v_cvt_pk_bf16_f32 v142, v44, v45\n"
  "v_cvt_pk_bf16_f32 v143, v46, v47\n"
  "buffer_store_dwordx4 v[140:143], v250, s[52:55], s68 offen offset:1024 sc1\n"
  "v_pk_mul_f32 v[48:49], v[48:49], v[198:199]\n"
  "v_pk_mul_f32 v[50:51], v[50:51], v[198:199]\n"
  "v_pk_mul_f32 v[52:53], v[52:53], v[198:199]\n"
  "v_pk_mul_f32 v[54:55], v[54:55], v[198:199]\n"
  "v_cvt_pk_bf16_f32 v160, v48, v49\n"
  "v_cvt_pk_bf16_f32 v161, v50, v51\n"
  "v_cvt_pk_bf16_f32 v162, v52, v53\n"
  "v_cvt_pk_bf16_f32 v163, v54, v55\n"
  "buffer_store_dwordx4 v[160:163], v250, s[52:55], s68 offen offset:2048 sc1\n"
  "v_pk_mul_f32 v[56:57], v[56:57], v[198:199]\n"
  "v_pk_mul_f32 v[58:59], v[58:59], v[198:199]\n"
  "v_pk_mul_f32 v[60:61], v[60:61], v[198:199]\n"
  "v_pk_mul_f32 v[62:63], v[62:63], v[198:199]\n"
  "v_cvt_pk_bf16_f32 v164, v56, v57\n"
  "v_cvt_pk_bf16_f32 v165, v58, v59\n"
  "v_cvt_pk_bf16_f32 v166, v60, v61\n"
  "v_cvt_pk_bf16_f32 v167, v62, v63\n"
  "buffer_store_dwordx4 v[164:167], v250, s[52:55], s68 offen offset:3072 sc1\n"
  "v_accvgpr_read_b32 v199, a40\n"
  "v_rcp_f32 v198, v199\n"
  "s_nop 0\n"
  "v_mov_b32 v199, v198\n"
  "v_pk_mul_f32 v[64:65], v[64:65], v[198:199]\n"
  "v_pk_mul_f32 v[66:67], v[66:67], v[198:199]\n"
  "v_pk_mul_f32 v[68:69], v[68:69], v[198:199]\n"
  "v_pk_mul_f32 v[70:71], v[70:71], v[198:199]\n"
  "v_cvt_pk_bf16_f32 v144, v64, v65\n"
  "v_cvt_pk_bf16_f32 v145, v66, v67\n"
  "v_cvt_pk_bf16_f32 v146, v68, v69\n"
  "v_cvt_pk_bf16_f32 v147, v70, v71\n"
  "buffer_store_dwordx4 v[144:147], v250, s[52:55], s69 offen sc1\n"
  "v_pk_mul_f32 v[72:73], v[72:73], v[198:199]\n"
  "v_pk_mul_f32 v[74:75], v[74:75], v[198:199]\n"
  "v_pk_mul_f32 v[76:77], v[76:77], v[198:199]\n"
  "v_pk_mul_f32 v[78:79], v[78:79], v[198:199]\n"
  "v_cvt_pk_bf16_f32 v148, v72, v73\n"
  "v_cvt_pk_bf16_f32 v149, v74, v75\n"
  "v_cvt_pk_bf16_f32 v150, v76, v77\n"
  "v_cvt_pk_bf16_f32 v151, v78, v79\n"
  "buffer_store_dwordx4 v[148:151], v250, s[52:55], s69 offen offset:1024 sc1\n"
  "v_pk_mul_f32 v[80:81], v[80:81], v[198:199]\n"
  "v_pk_mul_f32 v[82:83], v[82:83], v[198:199]\n"
  "v_pk_mul_f32 v[84:85], v[84:85], v[198:199]\n"
  "v_pk_mul_f32 v[86:87], v[86:87], v[198:199]\n"
  "v_cvt_pk_bf16_f32 v168, v80, v81\n"
  "v_cvt_pk_bf16_f32 v169, v82, v83\n"
  "v_cvt_pk_bf16_f32 v170, v84, v85\n"
  "v_cvt_pk_bf16_f32 v171, v86, v87\n"
  "buffer_store_dwordx4 v[168:171], v250, s[52:55], s69 offen offset:2048 sc1\n"
  "v_pk_mul_f32 v[88:89], v[88:89], v[198:199]\n"
  "v_pk_mul_f32 v[90:91], v[90:91], v[198:199]\n"
  "v_pk_mul_f32 v[92:93], v[92:93], v[198:199]\n"
  "v_pk_mul_f32 v[94:95], v[94:95], v[198:199]\n"
  "v_cvt_pk_bf16_f32 v172, v88, v89\n"
  "v_cvt_pk_bf16_f32 v173, v90, v91\n"
  "v_cvt_pk_bf16_f32 v174, v92, v93\n"
  "v_cvt_pk_bf16_f32 v175, v94, v95\n"
  "buffer_store_dwordx4 v[172:175], v250, s[52:55], s69 offen offset:3072 sc1\n"
  "s_waitcnt vmcnt(0) lgkmcnt(0)\n"
  "s_nop 15\n"
  "s_branch L_c\n"
  :: "{s[16:17]}"(__builtin_amdgcn_kernarg_segment_ptr()), "{s18}"(blockIdx.x), "{s19}"(wave), "s"((__attribute__((address_space(3))) char*)lds)
  : CLOBBERS);
}


extern "C" void kernel_launch(void* const* d_in, const int* in_sizes, int n_in, void* d_out, int out_size, void* d_ws, size_t ws_size, hipStream_t stream) {

  mha_fused<<<256, 256, 0, stream>>>(d_in[0], d_in[1], d_in[2], d_in[3], d_in[4], (char*)d_ws + 0, d_out, d_ws);
}
```
